```python
import torch

def _softmax(x, axis=-1):
    return torch.softmax(x, dim=axis)
def _gelu(x):
    return torch.nn.functional.gelu(x, approximate="tanh")
def _silu(x):
    return torch.nn.functional.silu(x)
def _sigmoid(x):
    return torch.sigmoid(x)
def _layernorm(x, axis=-1, eps=1e-5):
    m = x.mean(dim=axis, keepdim=True)
    v = x.var(dim=axis, keepdim=True, unbiased=False)
    return (x - m) / torch.sqrt(v + eps)
def _scatter_add(tgt, idx, src, axis=0):
    return tgt.clone().index_add_(axis, idx, src)
def _segment_sum(src, seg, n):
    out = torch.zeros((n,) + src.shape[1:], dtype=src.dtype)
    return out.index_add_(0, seg, src)
def _pad(x, pw):
    flat = tuple(v for pair in reversed(pw) for v in pair)
    return torch.nn.functional.pad(x, flat)

kernel_name = 'standalone__softmax_full_causal__prompt'

def setup_inputs():

    _w = (lambda a: (lambda t: t.to(torch.bfloat16) if t.is_floating_point() else t)(torch.as_tensor(a)))
    B, H, S, D = 1, 16, 2048, 256
    return {
        "q": _w((lambda *s: torch.randn(*s, dtype=torch.bfloat16) * 0.8)(B, H, S, D)),
        "k": _w((lambda *s: torch.randn(*s, dtype=torch.bfloat16) * 0.8)(B, H, S, D)),
        "v": _w((lambda *s: torch.randn(*s, dtype=torch.bfloat16) * 0.05)(B, H, S, D)),
    }

def reference(q, k, v):
    B, H, S, D = q.shape[0], q.shape[1], q.shape[2], q.shape[3]
    scale = D ** -0.5
    scores = torch.einsum('bhqd,bhkd->bhqk', q, k) * scale
    qi = torch.reshape(torch.arange(S), (1, 1, S, 1))
    kj = torch.reshape(torch.arange(S), (1, 1, 1, S))
    causal_bias = (lambda x, dt: x.to(dt))(kj > qi, scores.dtype) * (-1e9)
    attn = _softmax(scores + causal_bias, axis=-1)
    return torch.einsum('bhqk,bhkd->bhqd', attn, v)
```

```cpp
#include <hip/hip_runtime.h>
#include <hip/hip_bf16.h>
__global__ __launch_bounds__(256) void naive_attn(const __hip_bfloat16* q, const __hip_bfloat16* k, const __hip_bfloat16* v, __hip_bfloat16* o) {
    const int S = 2048, D = 256;
    int wave = (blockIdx.x * blockDim.x + threadIdx.x) >> 6;
    int lane = threadIdx.x & 63;
    int h = wave / S, row = wave % S;
    const __hip_bfloat16* qp = q + ((size_t)h * S + row) * D + lane * 4;
    float qv[4];
    for (int i = 0; i < 4; ++i) qv[i] = __bfloat162float(qp[i]);
    float m = -1e30f, l = 0.f, acc[4] = {0, 0, 0, 0};
    for (int j = 0; j <= row; ++j) {
        const __hip_bfloat16* kp = k + ((size_t)h * S + j) * D + lane * 4;
        float s = 0;
        for (int i = 0; i < 4; ++i) s += qv[i] * __bfloat162float(kp[i]);
        for (int off = 32; off; off >>= 1) s += __shfl_xor(s, off);
        s *= 0.0625f;
        float mn = fmaxf(m, s);
        float a = __expf(m - mn), p = __expf(s - mn);
        l = l * a + p;
        const __hip_bfloat16* vp = v + ((size_t)h * S + j) * D + lane * 4;
        for (int i = 0; i < 4; ++i) acc[i] = acc[i] * a + p * __bfloat162float(vp[i]);
        m = mn;
    }
    __hip_bfloat16* op = o + ((size_t)h * S + row) * D + lane * 4;
    for (int i = 0; i < 4; ++i) op[i] = __float2bfloat16(acc[i] / l);
}
extern "C" void kernel_launch(void* const* d_in, const int* in_sizes, int n_in, void* d_out, int out_size, void* d_ws, size_t ws_size, hipStream_t stream) {
    naive_attn<<<16 * 2048 / 4, 256, 0, stream>>>((const __hip_bfloat16*)d_in[0], (const __hip_bfloat16*)d_in[1], (const __hip_bfloat16*)d_in[2], (__hip_bfloat16*)d_out);
}
```

```cpp
#include <hip/hip_runtime.h>
#include <hip/hip_bf16.h>
#include <stdint.h>

__global__ __launch_bounds__(256, 1) void attn_causal_d256(const char* __restrict__ q, const char* __restrict__ k,
                                                            const char* __restrict__ v, char* __restrict__ o, char* __restrict__ ws) {
    __shared__ __attribute__((aligned(1024))) char lds[163840];
    asm volatile(
    "s_load_dwordx8 s[80:87], %0, 0x0\n"
    "s_load_dwordx2 s[76:77], %0, 0x20\n"
    "s_mov_b32 s72, %1\n"
    "v_and_b32 v210, 63, %2\n"
    "s_getpc_b64 s[78:79]\n"
    ".Lcw0:\n"
    "v_lshrrev_b32 v205, 3, %2\n"
    "s_bfe_u32 s74, %1, 0x40003\n"
    "s_lshl_b32 s74, s74, 12\n"
    "v_lshl_add_u32 v205, v205, 7, s74\n"
    "v_min_u32 v205, .Lcw1-.Lcw0-4, v205\n"
    "global_load_dword v204, v205, s[78:79]\n"
    "v_readfirstlane_b32 s13, %2\n"
    "s_bfe_u32 s20, s72, 0x40003\n"
    "s_lshr_b32 s15, s72, 7\n"
    "s_and_b32 s16, s72, 7\n"
    "s_lshr_b32 s21, s20, 1\n"
    "v_and_b32 v144, 31, v210\n"
    "v_lshrrev_b32 v145, 5, v210\n"
    "s_lshl3_add_u32 s19, s15, s16\n"
    "s_sub_u32 s15, 15, s21\n"
    "s_bitcmp1_b32 0x4000, s20\n"
    "s_cselect_b32 s22, s21, s15\n"
    "v_xor_b32 v148, v144, v145\n"
    "v_lshlrev_b32 v147, 9, v145\n"
    "s_lshl4_add_u32 s15, s19, s22\n"
    "s_lshr_b32 s68, s13, 6\n"
    "s_lshl2_add_u32 s15, s15, s68\n"
    "s_lshl_b32 s8, s15, 14\n"
    "s_xor_b32 s16, s13, 0x80\n"
    "v_lshl_add_u32 v240, v148, 4, v147\n"
    "s_lshl_b32 s16, s16, 8\n"
    "s_add_u32 s14, s16, 0x18000\n"
    "s_mov_b32 m0, s14\n"
    "s_mov_b32 s94, 16384\n"
    "s_mov_b32 s95, 0x20000\n"
    "v_xor_b32 v241, 32, v240\n"
    "v_xor_b32 v242, 64, v240\n"
    "v_xor_b32 v243, 96, v240\n"
    "v_xor_b32 v244, 128, v240\n"
    "v_xor_b32 v245, 160, v240\n"
    "v_xor_b32 v246, 192, v240\n"
    "v_xor_b32 v247, 224, v240\n"
    "s_lshl_b32 s5, s21, 2\n"
    "s_lshl_b32 s22, s22, 2\n"
    "s_mov_b32 s12, 0x23222323\n"
    "s_mov_b32 s13, 0x63222322\n"
    "s_lshl_b32 s11, s21, 3\n"
    "s_lshr_b64 s[12:13], s[12:13], s11\n"
    "s_and_b32 s4, s12, 0xff\n"
    "s_and_b32 s6, s20, 1\n"
    "s_cmp_lt_u32 s4, s22\n"
    "s_cselect_b32 s67, s6, 2\n"
    "s_sub_u32 s11, s22, s4\n"
    "s_add_u32 s0, s11, 4\n"
    "s_add_u32 s13, s0, s5\n"
    "s_add_u32 s13, s13, 4\n"
    "s_add_u32 s65, s11, s68\n"
    "s_cmp_eq_u32 s21, 0\n"
    "s_cselect_b32 s71, 2, 5\n"
    "s_sub_u32 s12, s4, s71\n"
    "s_cmp_eq_u32 s67, 1\n"
    "s_cselect_b32 s0, 0, s0\n"
    "s_cselect_b32 s13, s4, s13\n"
    "s_cselect_b32 s65, s12, s65\n"
    "s_cselect_b32 s63, s71, 1\n"
    "s_sub_u32 s71, s71, 1\n"
    "s_add_u32 s11, s22, 4\n"
    "s_add_u32 s12, s22, s68\n"
    "s_cmp_eq_u32 s67, 2\n"
    "s_cselect_b32 s2, s22, s13\n"
    "s_cselect_b32 s0, s22, s0\n"
    "s_cselect_b32 s13, s11, s13\n"
    "s_cselect_b32 s1, 0, s4\n"
    "s_cselect_b32 s3, -1, 0\n"
    "s_cselect_b32 s65, s12, s65\n"
    "s_sub_u32 s2, s2, 1\n"
    "v_subrev_u32 v146, s0, v210\n"
    "v_add_u32 v147, s1, v210\n"
    "v_xor_b32 v146, s3, v146\n"
    "v_sub_u32 v148, s2, v210\n"
    "v_cmp_gt_i32 vcc, 0, v146\n"
    "v_mov_b32 v146, 0x40000000\n"
    "s_nop 0\n"
    "v_cndmask_b32 v147, v148, v147, vcc\n"
    "v_cmp_gt_u32 vcc, s13, v210\n"
    "s_waitcnt lgkmcnt(0)\n"
    "s_add_u32 s92, s80, s8\n"
    "s_addc_u32 s93, s81, 0\n"
    "s_and_b32 s93, s93, 0xffff\n"
    "buffer_load_dwordx4 v240, s[92:95], 0 offen offset:0 lds\n"
    "v_lshlrev_b32 v147, 14, v147\n"
    "s_nop 0\n"
    "v_cndmask_b32 v211, v146, v147, vcc\n"
    "s_mov_b32 s59, -1\n"
    "buffer_load_dwordx4 v241, s[92:95], 0 offen offset:1024 lds\n"
    "s_nop 0\n"
    "s_add_u32 s59, s59, 1\n"
    "v_readlane_b32 s54, v211, s59\n"
    "s_add_u32 s59, s59, 1\n"
    "buffer_load_dwordx4 v242, s[92:95], 0 offen offset:2048 lds\n"
    "v_readlane_b32 s55, v211, s59\n"
    "s_mov_b32 s38, 0x100000\n"
    "s_mov_b32 s39, 0x20000\n"
    "s_mov_b32 s42, 0x100000\n"
    "buffer_load_dwordx4 v243, s[92:95], 0 offen offset:3072 lds\n"
    "s_mov_b32 s43, 0x20000\n"
    "s_and_b32 s11, s68, 1\n"
    "s_lshl_b32 s11, s11, 7\n"
    "s_lshl_b32 s12, s68, 12\n"
    "s_add_u32 m0, s14, 4096\n"
    "s_mov_b32 s24, 4096\n"
    "buffer_load_dwordx4 v244, s[92:95], s24 offen offset:0 lds\n"
    "s_or_b32 s11, s11, s12\n"
    "v_xor_b32 v188, s11, v240\n"
    "v_xor_b32 v189, 32, v188\n"
    "v_xor_b32 v190, 64, v188\n"
    "buffer_load_dwordx4 v245, s[92:95], s24 offen offset:1024 lds\n"
    "v_xor_b32 v191, 96, v188\n"
    "v_lshlrev_b32 v147, 9, v145\n"
    "v_add_u32 v147, s12, v147\n"
    "v_lshlrev_b32 v148, 1, v145\n"
    "buffer_load_dwordx4 v246, s[92:95], s24 offen offset:2048 lds\n"
    "v_xor_b32 v148, v148, v144\n"
    "v_lshl_add_u32 v192, v148, 4, v147\n"
    "v_xor_b32 v193, 64, v192\n"
    "v_xor_b32 v194, 128, v192\n"
    "buffer_load_dwordx4 v247, s[92:95], s24 offen offset:3072 lds\n"
    "v_xor_b32 v195, 192, v192\n"
    "s_mov_b32 s58, s12\n"
    "s_sub_u32 s70, s5, s68\n"
    "s_add_u32 s70, s70, 3\n"
    "s_add_u32 m0, s14, 8192\n"
    "s_mov_b32 s24, 8192\n"
    "buffer_load_dwordx4 v240, s[92:95], s24 offen offset:0 lds\n"
    "s_lshl_b32 s9, s70, 14\n"
    "s_add_u32 s70, s70, 1\n"
    "s_lshl3_add_u32 s7, s19, s21\n"
    "s_lshl_b32 s19, s19, 20\n"
    "buffer_load_dwordx4 v241, s[92:95], s24 offen offset:1024 lds\n"
    "s_add_u32 s9, s9, s19\n"
    "s_cmp_eq_u32 s67, 0\n"
    "s_cselect_b32 s10, s9, s8\n"
    "v_mov_b32 v213, 0\n"
    "buffer_load_dwordx4 v242, s[92:95], s24 offen offset:2048 lds\n"
    "s_lshl2_add_u32 s15, s7, s68\n"
    "s_lshl_b32 s16, s15, 14\n"
    "s_add_u32 s16, s16, 0x100000\n"
    "s_lshl_b32 s17, s15, 8\n"
    "buffer_load_dwordx4 v243, s[92:95], s24 offen offset:3072 lds\n"
    "s_lshl_b32 s15, s15, 10\n"
    "s_add_u32 s15, s15, 0x40000\n"
    "s_add_u32 s36, s82, s19\n"
    "s_addc_u32 s37, s83, 0\n"
    "s_add_u32 m0, s14, 12288\n"
    "s_mov_b32 s24, 12288\n"
    "buffer_load_dwordx4 v244, s[92:95], s24 offen offset:0 lds\n"
    "s_and_b32 s37, s37, 0xffff\n"
    "s_add_u32 s52, s76, s17\n"
    "s_addc_u32 s53, s77, 0\n"
    "s_add_u32 s40, s84, s19\n"
    "s_addc_u32 s41, s85, 0\n"
    "s_and_b32 s41, s41, 0xffff\n"
    "buffer_load_dwordx4 v245, s[92:95], s24 offen offset:1024 lds\n"
    "s_add_u32 s48, s86, s10\n"
    "s_addc_u32 s49, s87, 0\n"
    "s_add_u32 s50, s76, s16\n"
    "s_addc_u32 s51, s77, 0\n"
    "buffer_load_dwordx4 v246, s[92:95], s24 offen offset:2048 lds\n"
    "s_add_u32 s44, s76, s15\n"
    "s_addc_u32 s45, s77, 0\n"
    "s_mov_b32 s60, 0x3db8aa3b\n"
    "s_mov_b32 s64, 0xffff0000\n"
    "buffer_load_dwordx4 v247, s[92:95], s24 offen offset:3072 lds\n"
    "s_cmp_lg_u32 s67, 1\n"
    "s_cbranch_scc1 .Ltok_1\n"
    "global_load_dword v212, v213, s[52:53] offset:64 sc1\n"
    ".Ltok_1:\n"
    "s_add_u32 m0, s58, 0\n"
    "s_nop 0\n"
    "buffer_load_dwordx4 v188, s[36:39], s54 offen offset:0 lds\n"
    "s_mov_b32 s61, 0x41000000\n"
    "s_mov_b32 s62, 0xf149f2ca\n"
    "s_mov_b32 s29, 0x44000000\n"
    "buffer_load_dwordx4 v189, s[36:39], s54 offen offset:1024 lds\n"
    "s_lshl_b32 s91, s68, 13\n"
    "s_add_u32 s91, s91, 0x20000\n"
    "v_and_b32 v144, 15, v210\n"
    "buffer_load_dwordx4 v190, s[36:39], s54 offen offset:2048 lds\n"
    "v_lshrrev_b32 v146, 4, v210\n"
    "v_and_b32 v147, 1, v146\n"
    "v_lshl_add_u32 v147, v147, 1, v145\n"
    "buffer_load_dwordx4 v191, s[36:39], s54 offen offset:3072 lds\n"
    "v_lshlrev_b32 v148, 1, v146\n"
    "v_xor_b32 v148, v148, v144\n"
    "v_lshlrev_b32 v148, 4, v148\n"
    "v_lshl_add_u32 v148, v146, 8, v148\n"
    "v_add_u32 v218, s91, v148\n"
    "v_and_b32 v148, 7, v210\n"
    "v_lshrrev_b32 v149, 3, v144\n"
    "v_lshl_add_u32 v148, v148, 1, v149\n"
    "v_xor_b32 v148, v148, v145\n"
    "v_lshlrev_b32 v148, 4, v148\n"
    "v_lshl_add_u32 v148, v144, 8, v148\n"
    "v_and_b32 v149, 1, v146\n"
    "v_lshl_add_u32 v148, v149, 3, v148\n"
    "v_add_u32 v220, s91, v148\n"
    "v_xor_b32 v147, v147, v144\n"
    "v_lshlrev_b32 v147, 4, v147\n"
    "v_lshl_add_u32 v176, v144, 9, v147\n"
    "v_lshl_add_u32 v224, v144, 8, v147\n"
    "v_add_u32 v224, s91, v224\n"
    "v_add_u32 v160, s14, v176\n"
    "v_xor_b32 v161, 64, v160\n"
    "v_xor_b32 v177, 64, v176\n"
    "v_xor_b32 v225, 64, v224\n"
    "v_xor_b32 v162, 128, v160\n"
    "v_xor_b32 v178, 128, v176\n"
    "v_xor_b32 v226, 128, v224\n"
    "v_xor_b32 v163, 192, v160\n"
    "v_xor_b32 v179, 192, v176\n"
    "v_xor_b32 v227, 192, v224\n"
    "v_lshrrev_b32 v147, 2, v210\n"
    "v_and_b32 v148, 7, v147\n"
    "v_lshlrev_b32 v148, 5, v148\n"
    "v_lshl_add_u32 v147, v147, 9, v148\n"
    "v_and_b32 v148, 3, v210\n"
    "v_lshl_add_u32 v147, v148, 3, v147\n"
    "v_add_u32 v180, 0x10000, v147\n"
    "v_xor_b32 v181, 32, v180\n"
    "v_xor_b32 v182, 64, v180\n"
    "v_xor_b32 v183, 96, v180\n"
    "v_xor_b32 v184, 128, v180\n"
    "v_xor_b32 v185, 160, v180\n"
    "v_xor_b32 v186, 192, v180\n"
    "v_xor_b32 v187, 224, v180\n"
    "v_lshlrev_b32 v147, 2, v146\n"
    "v_sub_u32 v219, v144, v147\n"
    "v_lshlrev_b32 v214, 4, v210\n"
    "v_lshlrev_b32 v148, 9, v146\n"
    "v_lshl_add_u32 v232, v144, 4, v148\n"
    "v_add_u32 v233, 0x1000, v232\n"
    "v_add_u32 v234, 0x2000, v232\n"
    "v_add_u32 v235, 0x3000, v232\n"
    "v_xor_b32 v147, v144, v146\n"
    "v_lshlrev_b32 v148, 9, v146\n"
    "v_lshl_add_u32 v228, v147, 4, v148\n"
    "v_xor_b32 v147, 64, v228\n"
    "v_add_u32 v229, 0x400, v147\n"
    "v_xor_b32 v147, 128, v228\n"
    "v_add_u32 v230, 0x800, v147\n"
    "v_xor_b32 v147, 192, v228\n"
    "v_add_u32 v231, 0xc00, v147\n"
    "v_mov_b32 v128, 0\n"
    "v_mov_b32 v129, 0\n"
    "v_mov_b32 v130, 0\n"
    "v_mov_b32 v131, 0\n"
    "s_nop 1\n"
    "v_mfma_f32_32x32x16_bf16 v[0:15], v[128:131], v[128:131], 0\n"
    "v_mfma_f32_32x32x16_bf16 v[16:31], v[128:131], v[128:131], 0\n"
    "v_mfma_f32_32x32x16_bf16 v[32:47], v[128:131], v[128:131], 0\n"
    "v_mfma_f32_32x32x16_bf16 v[48:63], v[128:131], v[128:131], 0\n"
    "v_mfma_f32_32x32x16_bf16 v[64:79], v[128:131], v[128:131], 0\n"
    "v_mfma_f32_32x32x16_bf16 v[80:95], v[128:131], v[128:131], 0\n"
    "v_mfma_f32_32x32x16_bf16 v[96:111], v[128:131], v[128:131], 0\n"
    "v_mfma_f32_32x32x16_bf16 v[112:127], v[128:131], v[128:131], 0\n"
    "v_mov_b32 v236, 0xc1500000\n"
    "v_mov_b32 v237, 0xc1500000\n"
    "v_mov_b32 v238, 0xc1500000\n"
    "v_mov_b32 v239, 0xc1500000\n"
    "v_mov_b32 v248, 0xc1500000\n"
    "v_mov_b32 v249, 0xc1500000\n"
    "v_mov_b32 v250, 0xc1500000\n"
    "v_mov_b32 v251, 0xc1500000\n"
    "s_sub_u32 s65, 0, s65\n"
    "s_sub_u32 s32, 3, s68\n"
    "s_cmp_eq_u32 s67, 2\n"
    "s_cselect_b32 s32, s32, 0\n"
    "s_mov_b32 s35, s67\n"
    "s_mov_b32 s28, 0\n"
    "s_mov_b32 s66, 0\n"
    "s_add_u32 s92, s80, s9\n"
    "s_addc_u32 s93, s81, 0\n"
    "s_and_b32 s93, s93, 0xffff\n"
    "s_waitcnt vmcnt(12)\n"
    "ds_read_b128 v[128:131], v160 offset:0\n"
    "ds_read_b128 v[132:135], v161 offset:0\n"
    "ds_read_b128 v[136:139], v162 offset:0\n"
    "ds_read_b128 v[140:143], v163 offset:0\n"
    "ds_read_b128 v[144:147], v160 offset:256\n"
    "ds_read_b128 v[148:151], v161 offset:256\n"
    "ds_read_b128 v[152:155], v162 offset:256\n"
    "ds_read_b128 v[156:159], v163 offset:256\n"
    "s_add_u32 m0, s58, 16384\n"
    "s_nop 0\n"
    "buffer_load_dwordx4 v188, s[36:39], s55 offen offset:0 lds\n"
    "s_waitcnt lgkmcnt(7)\n"
    "v_lshlrev_b32 v240, 16, v128\n"
    "v_and_b32 v241, s64, v128\n"
    "v_lshlrev_b32 v242, 16, v129\n"
    "v_and_b32 v243, s64, v129\n"
    "v_pk_mul_f32 v[240:241], v[240:241], s[60:61] op_sel_hi:[1,0]\n"
    "v_lshlrev_b32 v244, 16, v130\n"
    "v_and_b32 v245, s64, v130\n"
    "v_pk_mul_f32 v[242:243], v[242:243], s[60:61] op_sel_hi:[1,0]\n"
    "v_cvt_pk_bf16_f32 v128, v240, v241\n"
    "v_lshlrev_b32 v246, 16, v131\n"
    "v_and_b32 v247, s64, v131\n"
    "v_pk_mul_f32 v[244:245], v[244:245], s[60:61] op_sel_hi:[1,0]\n"
    "v_cvt_pk_bf16_f32 v129, v242, v243\n"
    "v_accvgpr_write_b32 a0, v128\n"
    "s_waitcnt lgkmcnt(6)\n"
    "v_lshlrev_b32 v240, 16, v132\n"
    "v_and_b32 v241, s64, v132\n"
    "v_pk_mul_f32 v[246:247], v[246:247], s[60:61] op_sel_hi:[1,0]\n"
    "v_cvt_pk_bf16_f32 v130, v244, v245\n"
    "v_accvgpr_write_b32 a1, v129\n"
    "v_lshlrev_b32 v242, 16, v133\n"
    "v_and_b32 v243, s64, v133\n"
    "v_pk_mul_f32 v[240:241], v[240:241], s[60:61] op_sel_hi:[1,0]\n"
    "v_cvt_pk_bf16_f32 v131, v246, v247\n"
    "v_accvgpr_write_b32 a2, v130\n"
    "v_lshlrev_b32 v244, 16, v134\n"
    "v_and_b32 v245, s64, v134\n"
    "v_pk_mul_f32 v[242:243], v[242:243], s[60:61] op_sel_hi:[1,0]\n"
    "v_cvt_pk_bf16_f32 v132, v240, v241\n"
    "v_accvgpr_write_b32 a3, v131\n"
    "v_lshlrev_b32 v246, 16, v135\n"
    "v_and_b32 v247, s64, v135\n"
    "v_pk_mul_f32 v[244:245], v[244:245], s[60:61] op_sel_hi:[1,0]\n"
    "buffer_load_dwordx4 v189, s[36:39], s55 offen offset:1024 lds\n"
    "v_cvt_pk_bf16_f32 v133, v242, v243\n"
    "v_accvgpr_write_b32 a8, v132\n"
    "s_waitcnt lgkmcnt(5)\n"
    "v_lshlrev_b32 v240, 16, v136\n"
    "v_and_b32 v241, s64, v136\n"
    "v_pk_mul_f32 v[246:247], v[246:247], s[60:61] op_sel_hi:[1,0]\n"
    "v_cvt_pk_bf16_f32 v134, v244, v245\n"
    "v_accvgpr_write_b32 a9, v133\n"
    "v_lshlrev_b32 v242, 16, v137\n"
    "v_and_b32 v243, s64, v137\n"
    "v_pk_mul_f32 v[240:241], v[240:241], s[60:61] op_sel_hi:[1,0]\n"
    "v_cvt_pk_bf16_f32 v135, v246, v247\n"
    "v_accvgpr_write_b32 a10, v134\n"
    "v_lshlrev_b32 v244, 16, v138\n"
    "v_and_b32 v245, s64, v138\n"
    "v_pk_mul_f32 v[242:243], v[242:243], s[60:61] op_sel_hi:[1,0]\n"
    "v_cvt_pk_bf16_f32 v136, v240, v241\n"
    "v_accvgpr_write_b32 a11, v135\n"
    "v_lshlrev_b32 v246, 16, v139\n"
    "v_and_b32 v247, s64, v139\n"
    "v_pk_mul_f32 v[244:245], v[244:245], s[60:61] op_sel_hi:[1,0]\n"
    "v_cvt_pk_bf16_f32 v137, v242, v243\n"
    "v_accvgpr_write_b32 a16, v136\n"
    "s_waitcnt lgkmcnt(4)\n"
    "v_lshlrev_b32 v240, 16, v140\n"
    "v_and_b32 v241, s64, v140\n"
    "v_pk_mul_f32 v[246:247], v[246:247], s[60:61] op_sel_hi:[1,0]\n"
    "v_cvt_pk_bf16_f32 v138, v244, v245\n"
    "v_accvgpr_write_b32 a17, v137\n"
    "v_lshlrev_b32 v242, 16, v141\n"
    "v_and_b32 v243, s64, v141\n"
    "v_pk_mul_f32 v[240:241], v[240:241], s[60:61] op_sel_hi:[1,0]\n"
    "v_cvt_pk_bf16_f32 v139, v246, v247\n"
    "v_accvgpr_write_b32 a18, v138\n"
    "buffer_load_dwordx4 v190, s[36:39], s55 offen offset:2048 lds\n"
    "v_lshlrev_b32 v244, 16, v142\n"
    "v_and_b32 v245, s64, v142\n"
    "v_pk_mul_f32 v[242:243], v[242:243], s[60:61] op_sel_hi:[1,0]\n"
    "v_cvt_pk_bf16_f32 v140, v240, v241\n"
    "v_accvgpr_write_b32 a19, v139\n"
    "v_lshlrev_b32 v246, 16, v143\n"
    "v_and_b32 v247, s64, v143\n"
    "v_pk_mul_f32 v[244:245], v[244:245], s[60:61] op_sel_hi:[1,0]\n"
    "v_cvt_pk_bf16_f32 v141, v242, v243\n"
    "v_accvgpr_write_b32 a24, v140\n"
    "s_waitcnt lgkmcnt(3)\n"
    "v_lshlrev_b32 v240, 16, v144\n"
    "v_and_b32 v241, s64, v144\n"
    "v_pk_mul_f32 v[246:247], v[246:247], s[60:61] op_sel_hi:[1,0]\n"
    "v_cvt_pk_bf16_f32 v142, v244, v245\n"
    "v_accvgpr_write_b32 a25, v141\n"
    "v_lshlrev_b32 v242, 16, v145\n"
    "v_and_b32 v243, s64, v145\n"
    "v_pk_mul_f32 v[240:241], v[240:241], s[60:61] op_sel_hi:[1,0]\n"
    "v_cvt_pk_bf16_f32 v143, v246, v247\n"
    "v_accvgpr_write_b32 a26, v142\n"
    "v_lshlrev_b32 v244, 16, v146\n"
    "v_and_b32 v245, s64, v146\n"
    "v_pk_mul_f32 v[242:243], v[242:243], s[60:61] op_sel_hi:[1,0]\n"
    "v_cvt_pk_bf16_f32 v144, v240, v241\n"
    "v_accvgpr_write_b32 a27, v143\n"
    "v_lshlrev_b32 v246, 16, v147\n"
    "v_and_b32 v247, s64, v147\n"
    "v_pk_mul_f32 v[244:245], v[244:245], s[60:61] op_sel_hi:[1,0]\n"
    "v_cvt_pk_bf16_f32 v145, v242, v243\n"
    "v_accvgpr_write_b32 a32, v144\n"
    "s_waitcnt lgkmcnt(2)\n"
    "v_lshlrev_b32 v240, 16, v148\n"
    "v_and_b32 v241, s64, v148\n"
    "buffer_load_dwordx4 v191, s[36:39], s55 offen offset:3072 lds\n"
    "v_pk_mul_f32 v[246:247], v[246:247], s[60:61] op_sel_hi:[1,0]\n"
    "v_cvt_pk_bf16_f32 v146, v244, v245\n"
    "v_accvgpr_write_b32 a33, v145\n"
    "v_lshlrev_b32 v242, 16, v149\n"
    "v_and_b32 v243, s64, v149\n"
    "v_pk_mul_f32 v[240:241], v[240:241], s[60:61] op_sel_hi:[1,0]\n"
    "v_cvt_pk_bf16_f32 v147, v246, v247\n"
    "v_accvgpr_write_b32 a34, v146\n"
    "v_lshlrev_b32 v244, 16, v150\n"
    "v_and_b32 v245, s64, v150\n"
    "v_pk_mul_f32 v[242:243], v[242:243], s[60:61] op_sel_hi:[1,0]\n"
    "v_cvt_pk_bf16_f32 v148, v240, v241\n"
    "v_accvgpr_write_b32 a35, v147\n"
    "v_lshlrev_b32 v246, 16, v151\n"
    "v_and_b32 v247, s64, v151\n"
    "v_pk_mul_f32 v[244:245], v[244:245], s[60:61] op_sel_hi:[1,0]\n"
    "v_cvt_pk_bf16_f32 v149, v242, v243\n"
    "v_accvgpr_write_b32 a40, v148\n"
    "s_waitcnt lgkmcnt(1)\n"
    "v_lshlrev_b32 v240, 16, v152\n"
    "v_and_b32 v241, s64, v152\n"
    "v_pk_mul_f32 v[246:247], v[246:247], s[60:61] op_sel_hi:[1,0]\n"
    "v_cvt_pk_bf16_f32 v150, v244, v245\n"
    "v_accvgpr_write_b32 a41, v149\n"
    "v_lshlrev_b32 v242, 16, v153\n"
    "v_and_b32 v243, s64, v153\n"
    "v_pk_mul_f32 v[240:241], v[240:241], s[60:61] op_sel_hi:[1,0]\n"
    "v_cvt_pk_bf16_f32 v151, v246, v247\n"
    "v_accvgpr_write_b32 a42, v150\n"
    "v_lshlrev_b32 v244, 16, v154\n"
    "v_and_b32 v245, s64, v154\n"
    "v_pk_mul_f32 v[242:243], v[242:243], s[60:61] op_sel_hi:[1,0]\n"
    "v_cvt_pk_bf16_f32 v152, v240, v241\n"
    "v_accvgpr_write_b32 a43, v151\n"
    "v_lshlrev_b32 v246, 16, v155\n"
    "v_and_b32 v247, s64, v155\n"
    "v_pk_mul_f32 v[244:245], v[244:245], s[60:61] op_sel_hi:[1,0]\n"
    "v_cvt_pk_bf16_f32 v153, v242, v243\n"
    "v_accvgpr_write_b32 a48, v152\n"
    "s_waitcnt lgkmcnt(0)\n"
    "v_lshlrev_b32 v240, 16, v156\n"
    "v_and_b32 v241, s64, v156\n"
    "v_pk_mul_f32 v[246:247], v[246:247], s[60:61] op_sel_hi:[1,0]\n"
    "v_cvt_pk_bf16_f32 v154, v244, v245\n"
    "v_accvgpr_write_b32 a49, v153\n"
    "v_lshlrev_b32 v242, 16, v157\n"
    "v_and_b32 v243, s64, v157\n"
    "v_pk_mul_f32 v[240:241], v[240:241], s[60:61] op_sel_hi:[1,0]\n"
    "v_cvt_pk_bf16_f32 v155, v246, v247\n"
    "v_accvgpr_write_b32 a50, v154\n"
    "v_lshlrev_b32 v244, 16, v158\n"
    "v_and_b32 v245, s64, v158\n"
    "v_pk_mul_f32 v[242:243], v[242:243], s[60:61] op_sel_hi:[1,0]\n"
    "v_cvt_pk_bf16_f32 v156, v240, v241\n"
    "v_accvgpr_write_b32 a51, v155\n"
    "v_lshlrev_b32 v246, 16, v159\n"
    "v_and_b32 v247, s64, v159\n"
    "v_pk_mul_f32 v[244:245], v[244:245], s[60:61] op_sel_hi:[1,0]\n"
    "v_cvt_pk_bf16_f32 v157, v242, v243\n"
    "v_accvgpr_write_b32 a56, v156\n"
    "v_pk_mul_f32 v[246:247], v[246:247], s[60:61] op_sel_hi:[1,0]\n"
    "v_cvt_pk_bf16_f32 v158, v244, v245\n"
    "v_accvgpr_write_b32 a57, v157\n"
    "v_cvt_pk_bf16_f32 v159, v246, v247\n"
    "v_accvgpr_write_b32 a58, v158\n"
    "v_accvgpr_write_b32 a59, v159\n"
    "s_waitcnt vmcnt(8)\n"
    "ds_read_b128 v[128:131], v160 offset:8192\n"
    "ds_read_b128 v[132:135], v161 offset:8192\n"
    "ds_read_b128 v[136:139], v162 offset:8192\n"
    "ds_read_b128 v[140:143], v163 offset:8192\n"
    "ds_read_b128 v[144:147], v160 offset:8448\n"
    "ds_read_b128 v[148:151], v161 offset:8448\n"
    "ds_read_b128 v[152:155], v162 offset:8448\n"
    "ds_read_b128 v[156:159], v163 offset:8448\n"
    "s_add_u32 m0, s58, 65536\n"
    "s_nop 0\n"
    "buffer_load_dwordx4 v192, s[40:43], s54 offen offset:0 lds\n"
    "s_waitcnt lgkmcnt(7)\n"
    "v_lshlrev_b32 v240, 16, v128\n"
    "v_and_b32 v241, s64, v128\n"
    "v_lshlrev_b32 v242, 16, v129\n"
    "v_and_b32 v243, s64, v129\n"
    "v_pk_mul_f32 v[240:241], v[240:241], s[60:61] op_sel_hi:[1,0]\n"
    "v_lshlrev_b32 v244, 16, v130\n"
    "v_and_b32 v245, s64, v130\n"
    "v_pk_mul_f32 v[242:243], v[242:243], s[60:61] op_sel_hi:[1,0]\n"
    "v_cvt_pk_bf16_f32 v128, v240, v241\n"
    "v_lshlrev_b32 v246, 16, v131\n"
    "v_and_b32 v247, s64, v131\n"
    "v_pk_mul_f32 v[244:245], v[244:245], s[60:61] op_sel_hi:[1,0]\n"
    "v_cvt_pk_bf16_f32 v129, v242, v243\n"
    "v_accvgpr_write_b32 a4, v128\n"
    "s_waitcnt lgkmcnt(6)\n"
    "v_lshlrev_b32 v240, 16, v132\n"
    "v_and_b32 v241, s64, v132\n"
    "v_pk_mul_f32 v[246:247], v[246:247], s[60:61] op_sel_hi:[1,0]\n"
    "v_cvt_pk_bf16_f32 v130, v244, v245\n"
    "v_accvgpr_write_b32 a5, v129\n"
    "v_lshlrev_b32 v242, 16, v133\n"
    "v_and_b32 v243, s64, v133\n"
    "v_pk_mul_f32 v[240:241], v[240:241], s[60:61] op_sel_hi:[1,0]\n"
    "v_cvt_pk_bf16_f32 v131, v246, v247\n"
    "v_accvgpr_write_b32 a6, v130\n"
    "v_lshlrev_b32 v244, 16, v134\n"
    "v_and_b32 v245, s64, v134\n"
    "v_pk_mul_f32 v[242:243], v[242:243], s[60:61] op_sel_hi:[1,0]\n"
    "v_cvt_pk_bf16_f32 v132, v240, v241\n"
    "v_accvgpr_write_b32 a7, v131\n"
    "v_lshlrev_b32 v246, 16, v135\n"
    "v_and_b32 v247, s64, v135\n"
    "v_pk_mul_f32 v[244:245], v[244:245], s[60:61] op_sel_hi:[1,0]\n"
    "buffer_load_dwordx4 v193, s[40:43], s54 offen offset:1024 lds\n"
    "v_cvt_pk_bf16_f32 v133, v242, v243\n"
    "v_accvgpr_write_b32 a12, v132\n"
    "s_waitcnt lgkmcnt(5)\n"
    "v_lshlrev_b32 v240, 16, v136\n"
    "v_and_b32 v241, s64, v136\n"
    "v_pk_mul_f32 v[246:247], v[246:247], s[60:61] op_sel_hi:[1,0]\n"
    "v_cvt_pk_bf16_f32 v134, v244, v245\n"
    "v_accvgpr_write_b32 a13, v133\n"
    "v_lshlrev_b32 v242, 16, v137\n"
    "v_and_b32 v243, s64, v137\n"
    "v_pk_mul_f32 v[240:241], v[240:241], s[60:61] op_sel_hi:[1,0]\n"
    "v_cvt_pk_bf16_f32 v135, v246, v247\n"
    "v_accvgpr_write_b32 a14, v134\n"
    "v_lshlrev_b32 v244, 16, v138\n"
    "v_and_b32 v245, s64, v138\n"
    "v_pk_mul_f32 v[242:243], v[242:243], s[60:61] op_sel_hi:[1,0]\n"
    "v_cvt_pk_bf16_f32 v136, v240, v241\n"
    "v_accvgpr_write_b32 a15, v135\n"
    "v_lshlrev_b32 v246, 16, v139\n"
    "v_and_b32 v247, s64, v139\n"
    "v_pk_mul_f32 v[244:245], v[244:245], s[60:61] op_sel_hi:[1,0]\n"
    "v_cvt_pk_bf16_f32 v137, v242, v243\n"
    "v_accvgpr_write_b32 a20, v136\n"
    "s_waitcnt lgkmcnt(4)\n"
    "v_lshlrev_b32 v240, 16, v140\n"
    "v_and_b32 v241, s64, v140\n"
    "v_pk_mul_f32 v[246:247], v[246:247], s[60:61] op_sel_hi:[1,0]\n"
    "v_cvt_pk_bf16_f32 v138, v244, v245\n"
    "v_accvgpr_write_b32 a21, v137\n"
    "v_lshlrev_b32 v242, 16, v141\n"
    "v_and_b32 v243, s64, v141\n"
    "v_pk_mul_f32 v[240:241], v[240:241], s[60:61] op_sel_hi:[1,0]\n"
    "v_cvt_pk_bf16_f32 v139, v246, v247\n"
    "v_accvgpr_write_b32 a22, v138\n"
    "buffer_load_dwordx4 v194, s[40:43], s54 offen offset:2048 lds\n"
    "v_lshlrev_b32 v244, 16, v142\n"
    "v_and_b32 v245, s64, v142\n"
    "v_pk_mul_f32 v[242:243], v[242:243], s[60:61] op_sel_hi:[1,0]\n"
    "v_cvt_pk_bf16_f32 v140, v240, v241\n"
    "v_accvgpr_write_b32 a23, v139\n"
    "v_lshlrev_b32 v246, 16, v143\n"
    "v_and_b32 v247, s64, v143\n"
    "v_pk_mul_f32 v[244:245], v[244:245], s[60:61] op_sel_hi:[1,0]\n"
    "v_cvt_pk_bf16_f32 v141, v242, v243\n"
    "v_accvgpr_write_b32 a28, v140\n"
    "s_waitcnt lgkmcnt(3)\n"
    "v_lshlrev_b32 v240, 16, v144\n"
    "v_and_b32 v241, s64, v144\n"
    "v_pk_mul_f32 v[246:247], v[246:247], s[60:61] op_sel_hi:[1,0]\n"
    "v_cvt_pk_bf16_f32 v142, v244, v245\n"
    "v_accvgpr_write_b32 a29, v141\n"
    "v_lshlrev_b32 v242, 16, v145\n"
    "v_and_b32 v243, s64, v145\n"
    "v_pk_mul_f32 v[240:241], v[240:241], s[60:61] op_sel_hi:[1,0]\n"
    "v_cvt_pk_bf16_f32 v143, v246, v247\n"
    "v_accvgpr_write_b32 a30, v142\n"
    "v_lshlrev_b32 v244, 16, v146\n"
    "v_and_b32 v245, s64, v146\n"
    "v_pk_mul_f32 v[242:243], v[242:243], s[60:61] op_sel_hi:[1,0]\n"
    "v_cvt_pk_bf16_f32 v144, v240, v241\n"
    "v_accvgpr_write_b32 a31, v143\n"
    "v_lshlrev_b32 v246, 16, v147\n"
    "v_and_b32 v247, s64, v147\n"
    "v_pk_mul_f32 v[244:245], v[244:245], s[60:61] op_sel_hi:[1,0]\n"
    "v_cvt_pk_bf16_f32 v145, v242, v243\n"
    "v_accvgpr_write_b32 a36, v144\n"
    "s_waitcnt lgkmcnt(2)\n"
    "v_lshlrev_b32 v240, 16, v148\n"
    "v_and_b32 v241, s64, v148\n"
    "buffer_load_dwordx4 v195, s[40:43], s54 offen offset:3072 lds\n"
    "s_add_u32 s59, s59, 1\n"
    "v_readlane_b32 s54, v211, s59\n"
    "v_pk_mul_f32 v[246:247], v[246:247], s[60:61] op_sel_hi:[1,0]\n"
    "v_cvt_pk_bf16_f32 v146, v244, v245\n"
    "v_accvgpr_write_b32 a37, v145\n"
    "v_lshlrev_b32 v242, 16, v149\n"
    "v_and_b32 v243, s64, v149\n"
    "v_pk_mul_f32 v[240:241], v[240:241], s[60:61] op_sel_hi:[1,0]\n"
    "v_cvt_pk_bf16_f32 v147, v246, v247\n"
    "v_accvgpr_write_b32 a38, v146\n"
    "v_lshlrev_b32 v244, 16, v150\n"
    "v_and_b32 v245, s64, v150\n"
    "v_pk_mul_f32 v[242:243], v[242:243], s[60:61] op_sel_hi:[1,0]\n"
    "v_cvt_pk_bf16_f32 v148, v240, v241\n"
    "v_accvgpr_write_b32 a39, v147\n"
    "v_lshlrev_b32 v246, 16, v151\n"
    "v_and_b32 v247, s64, v151\n"
    "v_pk_mul_f32 v[244:245], v[244:245], s[60:61] op_sel_hi:[1,0]\n"
    "v_cvt_pk_bf16_f32 v149, v242, v243\n"
    "v_accvgpr_write_b32 a44, v148\n"
    "s_waitcnt lgkmcnt(1)\n"
    "v_lshlrev_b32 v240, 16, v152\n"
    "v_and_b32 v241, s64, v152\n"
    "v_pk_mul_f32 v[246:247], v[246:247], s[60:61] op_sel_hi:[1,0]\n"
    "v_cvt_pk_bf16_f32 v150, v244, v245\n"
    "v_accvgpr_write_b32 a45, v149\n"
    "v_lshlrev_b32 v242, 16, v153\n"
    "v_and_b32 v243, s64, v153\n"
    "v_pk_mul_f32 v[240:241], v[240:241], s[60:61] op_sel_hi:[1,0]\n"
    "v_cvt_pk_bf16_f32 v151, v246, v247\n"
    "v_accvgpr_write_b32 a46, v150\n"
    "v_lshlrev_b32 v244, 16, v154\n"
    "v_and_b32 v245, s64, v154\n"
    "v_pk_mul_f32 v[242:243], v[242:243], s[60:61] op_sel_hi:[1,0]\n"
    "v_cvt_pk_bf16_f32 v152, v240, v241\n"
    "v_accvgpr_write_b32 a47, v151\n"
    "v_lshlrev_b32 v246, 16, v155\n"
    "v_and_b32 v247, s64, v155\n"
    "v_pk_mul_f32 v[244:245], v[244:245], s[60:61] op_sel_hi:[1,0]\n"
    "v_cvt_pk_bf16_f32 v153, v242, v243\n"
    "v_accvgpr_write_b32 a52, v152\n"
    "s_waitcnt lgkmcnt(0)\n"
    "v_lshlrev_b32 v240, 16, v156\n"
    "v_and_b32 v241, s64, v156\n"
    "v_pk_mul_f32 v[246:247], v[246:247], s[60:61] op_sel_hi:[1,0]\n"
    "v_cvt_pk_bf16_f32 v154, v244, v245\n"
    "v_accvgpr_write_b32 a53, v153\n"
    "v_lshlrev_b32 v242, 16, v157\n"
    "v_and_b32 v243, s64, v157\n"
    "v_pk_mul_f32 v[240:241], v[240:241], s[60:61] op_sel_hi:[1,0]\n"
    "v_cvt_pk_bf16_f32 v155, v246, v247\n"
    "v_accvgpr_write_b32 a54, v154\n"
    "v_lshlrev_b32 v244, 16, v158\n"
    "v_and_b32 v245, s64, v158\n"
    "v_pk_mul_f32 v[242:243], v[242:243], s[60:61] op_sel_hi:[1,0]\n"
    "v_cvt_pk_bf16_f32 v156, v240, v241\n"
    "v_accvgpr_write_b32 a55, v155\n"
    "v_lshlrev_b32 v246, 16, v159\n"
    "v_and_b32 v247, s64, v159\n"
    "v_pk_mul_f32 v[244:245], v[244:245], s[60:61] op_sel_hi:[1,0]\n"
    "v_cvt_pk_bf16_f32 v157, v242, v243\n"
    "v_accvgpr_write_b32 a60, v156\n"
    "v_pk_mul_f32 v[246:247], v[246:247], s[60:61] op_sel_hi:[1,0]\n"
    "v_cvt_pk_bf16_f32 v158, v244, v245\n"
    "v_accvgpr_write_b32 a61, v157\n"
    "v_cvt_pk_bf16_f32 v159, v246, v247\n"
    "v_accvgpr_write_b32 a62, v158\n"
    "v_accvgpr_write_b32 a63, v159\n"
    "v_mov_b32 v244, 0xc1500000\n"
    "v_mov_b32 v245, 0xc1500000\n"
    "v_mov_b32 v246, 0xc1500000\n"
    "v_mov_b32 v247, 0xc1500000\n"
    "s_waitcnt vmcnt(8) lgkmcnt(0)\n"
    "s_barrier\n"
    "ds_read_b128 a[128:131], v176 offset:0\n"
    "ds_read_b128 a[132:135], v177 offset:0\n"
    "ds_read_b128 a[136:139], v178 offset:0\n"
    "ds_read_b128 a[140:143], v179 offset:0\n"
    "ds_read_b128 a[144:147], v176 offset:256\n"
    "ds_read_b128 a[148:151], v177 offset:256\n"
    "ds_read_b128 a[152:155], v178 offset:256\n"
    "ds_read_b128 a[156:159], v179 offset:256\n"
    "s_cmp_lg_u32 s67, 1\n"
    "s_cbranch_scc1 .Lfl_2\n"
    "v_add_u32 v212, 1, v212\n"
    "s_nop 0\n"
    "v_readfirstlane_b32 s88, v212\n"
    "global_store_dword v213, v212, s[52:53] offset:0 sc1\n"
    "s_branch .Lfl_3\n"
    ".Lfl_2:\n"
    "global_store_dword v213, v213, s[52:53] offset:128 sc1\n"
    ".Lfl_3:\n"
    "s_waitcnt lgkmcnt(6)\n"
    "v_mfma_f32_16x16x32_bf16 v[128:131], a[128:131], a[0:3], v[236:239]\n"
    "s_add_u32 m0, s58, 32768\n"
    "s_nop 0\n"
    "buffer_load_dwordx4 v188, s[36:39], s54 offen offset:0 lds\n"
    "v_mfma_f32_16x16x32_bf16 v[132:135], a[128:131], a[4:7], v[244:247]\n"
    "ds_read_b128 a[128:131], v176 offset:8192\n"
    "v_mov_b32 v252, 0xc1500000\n"
    "v_mfma_f32_16x16x32_bf16 v[128:131], a[132:135], a[8:11], v[128:131]\n"
    "buffer_load_dwordx4 v189, s[36:39], s54 offen offset:1024 lds\n"
    "v_mfma_f32_16x16x32_bf16 v[132:135], a[132:135], a[12:15], v[132:135]\n"
    "s_waitcnt lgkmcnt(6)\n"
    "ds_read_b128 a[132:135], v177 offset:8192\n"
    "v_mov_b32 v253, 0xc1500000\n"
    "v_mfma_f32_16x16x32_bf16 v[128:131], a[136:139], a[16:19], v[128:131]\n"
    "buffer_load_dwordx4 v190, s[36:39], s54 offen offset:2048 lds\n"
    "v_mfma_f32_16x16x32_bf16 v[132:135], a[136:139], a[20:23], v[132:135]\n"
    "s_waitcnt lgkmcnt(6)\n"
    "ds_read_b128 a[136:139], v178 offset:8192\n"
    "v_mov_b32 v254, 0xc1500000\n"
    "v_mfma_f32_16x16x32_bf16 v[128:131], a[140:143], a[24:27], v[128:131]\n"
    "buffer_load_dwordx4 v191, s[36:39], s54 offen offset:3072 lds\n"
    "v_mfma_f32_16x16x32_bf16 v[132:135], a[140:143], a[28:31], v[132:135]\n"
    "s_waitcnt lgkmcnt(6)\n"
    "ds_read_b128 a[140:143], v179 offset:8192\n"
    "v_mov_b32 v255, 0xc1500000\n"
    "v_mfma_f32_16x16x32_bf16 v[128:131], a[144:147], a[32:35], v[128:131]\n"
    "s_add_u32 m0, s58, 81920\n"
    "s_nop 0\n"
    "buffer_load_dwordx4 v192, s[40:43], s55 offen offset:0 lds\n"
    "v_mfma_f32_16x16x32_bf16 v[132:135], a[144:147], a[36:39], v[132:135]\n"
    "s_waitcnt lgkmcnt(6)\n"
    "ds_read_b128 a[144:147], v176 offset:8448\n"
    "v_mov_b32 v196, 0x41500000\n"
    "v_mfma_f32_16x16x32_bf16 v[128:131], a[148:151], a[40:43], v[128:131]\n"
    "buffer_load_dwordx4 v193, s[40:43], s55 offen offset:1024 lds\n"
    "v_mfma_f32_16x16x32_bf16 v[132:135], a[148:151], a[44:47], v[132:135]\n"
    "s_waitcnt lgkmcnt(6)\n"
    "ds_read_b128 a[148:151], v177 offset:8448\n"
    "v_mov_b32 v197, 0x41500000\n"
    "v_mfma_f32_16x16x32_bf16 v[128:131], a[152:155], a[48:51], v[128:131]\n"
    "buffer_load_dwordx4 v194, s[40:43], s55 offen offset:2048 lds\n"
    "v_mfma_f32_16x16x32_bf16 v[132:135], a[152:155], a[52:55], v[132:135]\n"
    "s_waitcnt lgkmcnt(6)\n"
    "ds_read_b128 a[152:155], v178 offset:8448\n"
    "v_mov_b32 v198, 0\n"
    "v_mfma_f32_16x16x32_bf16 v[128:131], a[156:159], a[56:59], v[128:131]\n"
    "buffer_load_dwordx4 v195, s[40:43], s55 offen offset:3072 lds\n"
    "s_add_u32 s59, s59, 1\n"
    "v_readlane_b32 s55, v211, s59\n"
    "v_mfma_f32_16x16x32_bf16 v[132:135], a[156:159], a[60:63], v[132:135]\n"
    "s_waitcnt lgkmcnt(6)\n"
    "ds_read_b128 a[156:159], v179 offset:8448\n"
    "v_mov_b32 v199, 0\n"
    "v_mfma_f32_16x16x32_bf16 v[136:139], a[128:131], a[0:3], v[248:251]\n"
    "s_nop 3\n"
    "v_mfma_f32_16x16x32_bf16 v[140:143], a[128:131], a[4:7], v[236:239]\n"
    "s_waitcnt lgkmcnt(6)\n"
    "s_add_u32 m0, s58, 49152\n"
    "s_nop 0\n"
    "buffer_load_dwordx4 v188, s[36:39], s55 offen offset:0 lds\n"
    "v_mfma_f32_16x16x32_bf16 v[136:139], a[132:135], a[8:11], v[136:139]\n"
    "buffer_load_dwordx4 v189, s[36:39], s55 offen offset:1024 lds\n"
    "v_mfma_f32_16x16x32_bf16 v[140:143], a[132:135], a[12:15], v[140:143]\n"
    "s_waitcnt lgkmcnt(5)\n"
    "buffer_load_dwordx4 v190, s[36:39], s55 offen offset:2048 lds\n"
    "v_mfma_f32_16x16x32_bf16 v[136:139], a[136:139], a[16:19], v[136:139]\n"
    "buffer_load_dwordx4 v191, s[36:39], s55 offen offset:3072 lds\n"
    "v_mfma_f32_16x16x32_bf16 v[140:143], a[136:139], a[20:23], v[140:143]\n"
    "s_waitcnt lgkmcnt(4)\n"
    "v_min_f32 v204, v128, v132\n"
    "v_cmp_gt_f32 vcc, 0xc2540000, v204\n"
    "v_mfma_f32_16x16x32_bf16 v[136:139], a[140:143], a[24:27], v[136:139]\n"
    "v_exp_f32 v240, v128\n"
    "v_mfma_f32_16x16x32_bf16 v[140:143], a[140:143], a[28:31], v[140:143]\n"
    "s_waitcnt lgkmcnt(3)\n"
    "v_exp_f32 v241, v129\n"
    "v_mfma_f32_16x16x32_bf16 v[136:139], a[144:147], a[32:35], v[136:139]\n"
    "v_exp_f32 v242, v130\n"
    "v_mfma_f32_16x16x32_bf16 v[140:143], a[144:147], a[36:39], v[140:143]\n"
    "s_waitcnt lgkmcnt(2)\n"
    "v_add_f32 v216, v240, v241\n"
    "v_cvt_pk_bf16_f32 v160, v240, v241\n"
    "v_mfma_f32_16x16x32_bf16 v[136:139], a[148:151], a[40:43], v[136:139]\n"
    "v_exp_f32 v243, v131\n"
    "v_mfma_f32_16x16x32_bf16 v[140:143], a[148:151], a[44:47], v[140:143]\n"
    "s_waitcnt lgkmcnt(1)\n"
    "v_add_f32 v216, v216, v242\n"
    "v_mfma_f32_16x16x32_bf16 v[136:139], a[152:155], a[48:51], v[136:139]\n"
    "v_exp_f32 v240, v132\n"
    "v_mfma_f32_16x16x32_bf16 v[140:143], a[152:155], a[52:55], v[140:143]\n"
    "s_waitcnt lgkmcnt(0)\n"
    "v_add_f32 v216, v216, v243\n"
    "v_cvt_pk_bf16_f32 v161, v242, v243\n"
    "v_mfma_f32_16x16x32_bf16 v[136:139], a[156:159], a[56:59], v[136:139]\n"
    "v_exp_f32 v241, v133\n"
    "v_mfma_f32_16x16x32_bf16 v[140:143], a[156:159], a[60:63], v[140:143]\n"
    "v_exp_f32 v242, v134\n"
    "s_waitcnt vmcnt(12)\n"
    "s_barrier\n"
    "s_add_u32 m0, s58, 98304\n"
    "s_nop 0\n"
    "buffer_load_dwordx4 v192, s[40:43], s54 offen offset:0 lds\n"
    "ds_read_b128 a[128:131], v176 offset:16384\n"
    "ds_read_b128 a[132:135], v177 offset:16384\n"
    "s_and_b32 s72, vcc_lo, 0xffff\n"
    "s_cbranch_scc1 .Llow_4\n"
    ".Llowb_5:\n"
    "v_add_f32 v217, v240, v241\n"
    "ds_read_b128 a[136:139], v178 offset:16384\n"
    "v_cvt_pk_bf16_f32 v164, v240, v241\n"
    "buffer_load_dwordx4 v193, s[40:43], s54 offen offset:1024 lds\n"
    "v_exp_f32 v243, v135\n"
    "ds_read_b128 a[140:143], v179 offset:16384\n"
    "v_add_f32 v217, v217, v242\n"
    "buffer_load_dwordx4 v194, s[40:43], s54 offen offset:2048 lds\n"
    "v_exp_f32 v240, v136\n"
    "ds_read_b128 a[144:147], v176 offset:16640\n"
    "v_add_f32 v217, v217, v243\n"
    "buffer_load_dwordx4 v195, s[40:43], s54 offen offset:3072 lds\n"
    "s_add_u32 s59, s59, 1\n"
    "v_readlane_b32 s54, v211, s59\n"
    "v_cvt_pk_bf16_f32 v165, v242, v243\n"
    "ds_read_b128 a[148:151], v177 offset:16640\n"
    "v_exp_f32 v241, v137\n"
    "ds_read_b128 a[152:155], v178 offset:16640\n"
    "v_add_f32 v216, v216, v240\n"
    "ds_read_b128 a[156:159], v179 offset:16640\n"
    "v_exp_f32 v242, v138\n"
    "v_add_f32 v216, v216, v241\n"
    "v_cvt_pk_bf16_f32 v162, v240, v241\n"
    "s_waitcnt lgkmcnt(6)\n"
    "s_add_u32 s65, s65, 1\n"
    "s_cbranch_scc1 .LX0\n"
    ".LG1:\n"
    "v_mfma_f32_16x16x32_bf16 v[144:147], a[128:131], a[0:3], v[236:239]\n"
    "v_exp_f32 v243, v139\n"
    "v_mfma_f32_16x16x32_bf16 v[144:147], a[132:135], a[8:11], v[144:147]\n"
    "s_waitcnt lgkmcnt(4)\n"
    "ds_read_b64_tr_b16 a[160:161], v180 offset:0\n"
    "v_add_f32 v216, v216, v242\n"
    "v_mfma_f32_16x16x32_bf16 v[144:147], a[136:139], a[16:19], v[144:147]\n"
    "s_add_u32 s59, s59, 1\n"
    "ds_read_b64_tr_b16 a[162:163], v180 offset:8192\n"
    "v_add_f32 v216, v216, v243\n"
    "v_mfma_f32_16x16x32_bf16 v[144:147], a[140:143], a[24:27], v[144:147]\n"
    "ds_read_b64_tr_b16 a[164:165], v181 offset:0\n"
    "v_cvt_pk_bf16_f32 v163, v242, v243\n"
    "v_mfma_f32_16x16x32_bf16 v[148:151], a[140:143], a[28:31], v[244:247]\n"
    "s_add_u32 m0, s58, 0\n"
    "v_exp_f32 v240, v140\n"
    "v_mfma_f32_16x16x32_bf16 v[148:151], a[128:131], a[4:7], v[148:151]\n"
    "buffer_load_dwordx4 v188, s[36:39], s54 offen offset:0 lds\n"
    "v_mfma_f32_16x16x32_bf16 v[148:151], a[132:135], a[12:15], v[148:151]\n"
    "ds_read_b128 a[140:143], v179 offset:24576\n"
    "v_add_f32 v217, v217, v240\n"
    "v_mfma_f32_16x16x32_bf16 v[148:151], a[136:139], a[20:23], v[148:151]\n"
    "s_waitcnt lgkmcnt(6)\n"
    "ds_read_b128 a[128:131], v176 offset:24576\n"
    "ds_read_b128 a[132:135], v177 offset:24576\n"
    "v_mfma_f32_16x16x32_bf16 v[148:151], a[144:147], a[36:39], v[148:151]\n"
    "v_exp_f32 v241, v141\n"
    "v_mfma_f32_16x16x32_bf16 v[148:151], a[148:151], a[44:47], v[148:151]\n"
    "s_waitcnt lgkmcnt(6)\n"
    "ds_read_b128 a[136:139], v178 offset:24576\n"
    "v_add_f32 v217, v217, v241\n"
    "v_mfma_f32_16x16x32_bf16 v[148:151], a[152:155], a[52:55], v[148:151]\n"
    "ds_read_b64_tr_b16 a[166:167], v181 offset:8192\n"
    "v_cvt_pk_bf16_f32 v166, v240, v241\n"
    "v_mfma_f32_16x16x32_bf16 v[148:151], a[156:159], a[60:63], v[148:151]\n"
    "ds_read_b64_tr_b16 a[168:169], v182 offset:0\n"
    "ds_read_b64_tr_b16 a[170:171], v182 offset:8192\n"
    "v_mfma_f32_16x16x32_bf16 v[144:147], a[156:159], a[56:59], v[144:147]\n"
    "v_exp_f32 v242, v142\n"
    "v_mfma_f32_16x16x32_bf16 v[144:147], a[144:147], a[32:35], v[144:147]\n"
    "buffer_load_dwordx4 v189, s[36:39], s54 offen offset:1024 lds\n"
    "v_mfma_f32_16x16x32_bf16 v[144:147], a[148:151], a[40:43], v[144:147]\n"
    "ds_read_b128 a[156:159], v179 offset:24832\n"
    "v_add_f32 v217, v217, v242\n"
    "v_mfma_f32_16x16x32_bf16 v[144:147], a[152:155], a[48:51], v[144:147]\n"
    "s_waitcnt lgkmcnt(5)\n"
    "ds_read_b128 a[144:147], v176 offset:24832\n"
    "ds_read_b128 a[148:151], v177 offset:24832\n"
    "v_mfma_f32_16x16x32_bf16 v[152:155], a[128:131], a[0:3], v[248:251]\n"
    "v_exp_f32 v243, v143\n"
    "v_mfma_f32_16x16x32_bf16 v[152:155], a[132:135], a[8:11], v[152:155]\n"
    "s_waitcnt lgkmcnt(6)\n"
    "ds_read_b128 a[152:155], v178 offset:24832\n"
    "v_add_f32 v217, v217, v243\n"
    "v_mfma_f32_16x16x32_bf16 v[152:155], a[136:139], a[16:19], v[152:155]\n"
    "ds_read_b64_tr_b16 a[172:173], v183 offset:0\n"
    "v_add_f32 v215, v216, v217\n"
    "v_mfma_f32_16x16x32_bf16 v[152:155], a[140:143], a[24:27], v[152:155]\n"
    "ds_read_b64_tr_b16 a[174:175], v183 offset:8192\n"
    "v_cmp_lt_f32 vcc, s29, v215\n"
    "v_mfma_f32_16x16x32_bf16 v[156:159], a[140:143], a[28:31], v[252:255]\n"
    "ds_read_b64_tr_b16 a[176:177], v184 offset:0\n"
    "v_cvt_pk_bf16_f32 v167, v242, v243\n"
    "v_mfma_f32_16x16x32_bf16 v[156:159], a[128:131], a[4:7], v[156:159]\n"
    "s_cbranch_vccnz .Lstub_6\n"
    ".Lstubb_7:\n"
    "buffer_load_dwordx4 v190, s[36:39], s54 offen offset:2048 lds\n"
    "v_mfma_f32_16x16x32_bf16 v[156:159], a[132:135], a[12:15], v[156:159]\n"
    "ds_read_b64_tr_b16 a[178:179], v184 offset:8192\n"
    "v_add_f32 v198, v198, v216\n"
    "v_mfma_f32_16x16x32_bf16 v[156:159], a[136:139], a[20:23], v[156:159]\n"
    "s_waitcnt lgkmcnt(5)\n"
    "v_exp_f32 v240, v144\n"
    "v_mfma_f32_16x16x32_bf16 v[156:159], a[144:147], a[36:39], v[156:159]\n"
    "ds_read_b64_tr_b16 a[180:181], v185 offset:0\n"
    "v_add_f32 v199, v199, v217\n"
    "v_mfma_f32_16x16x32_bf16 v[156:159], a[148:151], a[44:47], v[156:159]\n"
    "s_waitcnt lgkmcnt(5)\n"
    "ds_read_b64_tr_b16 a[182:183], v185 offset:8192\n"
    "ds_read_b64_tr_b16 a[184:185], v186 offset:0\n"
    "v_mfma_f32_16x16x32_bf16 v[156:159], a[152:155], a[52:55], v[156:159]\n"
    "v_exp_f32 v241, v145\n"
    "v_mfma_f32_16x16x32_bf16 v[156:159], a[156:159], a[60:63], v[156:159]\n"
    "ds_read_b64_tr_b16 a[186:187], v186 offset:8192\n"
    "v_add_f32 v216, v240, v241\n"
    "v_mfma_f32_16x16x32_bf16 v[152:155], a[156:159], a[56:59], v[152:155]\n"
    "ds_read_b64_tr_b16 a[188:189], v187 offset:0\n"
    "v_cvt_pk_bf16_f32 v168, v240, v241\n"
    "v_mfma_f32_16x16x32_bf16 v[152:155], a[144:147], a[32:35], v[152:155]\n"
    "v_exp_f32 v242, v146\n"
    "v_mfma_f32_16x16x32_bf16 v[152:155], a[148:151], a[40:43], v[152:155]\n"
    "buffer_load_dwordx4 v191, s[36:39], s54 offen offset:3072 lds\n"
    "v_mfma_f32_16x16x32_bf16 v[152:155], a[152:155], a[48:51], v[152:155]\n"
    "ds_read_b64_tr_b16 a[190:191], v187 offset:8192\n"
    "v_add_f32 v216, v216, v242\n"
    "v_mfma_f32_16x16x32_bf16 v[0:3], a[160:163], v[160:163], v[0:3]\n"
    "v_exp_f32 v243, v147\n"
    "v_mfma_f32_16x16x32_bf16 v[4:7], a[160:163], v[164:167], v[4:7]\n"
    "ds_read_b64_tr_b16 a[160:161], v180 offset:256\n"
    "v_add_f32 v216, v216, v243\n"
    "v_mfma_f32_16x16x32_bf16 v[12:15], a[164:167], v[164:167], v[12:15]\n"
    "s_waitcnt vmcnt(12)\n"
    "s_barrier\n"
    "ds_read_b64_tr_b16 a[162:163], v180 offset:8448\n"
    "v_cvt_pk_bf16_f32 v169, v242, v243\n"
    "v_mfma_f32_16x16x32_bf16 v[8:11], a[164:167], v[160:163], v[8:11]\n"
    "ds_read_b64_tr_b16 a[164:165], v181 offset:256\n"
    "ds_read_b64_tr_b16 a[166:167], v181 offset:8448\n"
    "v_mfma_f32_16x16x32_bf16 v[16:19], a[168:171], v[160:163], v[16:19]\n"
    "v_exp_f32 v240, v148\n"
    "v_mfma_f32_16x16x32_bf16 v[20:23], a[168:171], v[164:167], v[20:23]\n"
    "s_waitcnt lgkmcnt(12)\n"
    "ds_read_b64_tr_b16 a[168:169], v182 offset:256\n"
    "ds_read_b64_tr_b16 a[170:171], v182 offset:8448\n"
    "v_mfma_f32_16x16x32_bf16 v[28:31], a[172:175], v[164:167], v[28:31]\n"
    "ds_read_b128 a[128:131], v176 offset:32768\n"
    "s_waitcnt lgkmcnt(11)\n"
    "ds_read_b128 a[132:135], v177 offset:32768\n"
    "v_mfma_f32_16x16x32_bf16 v[24:27], a[172:175], v[160:163], v[24:27]\n"
    "ds_read_b64_tr_b16 a[172:173], v183 offset:256\n"
    "ds_read_b64_tr_b16 a[174:175], v183 offset:8448\n"
    "v_mfma_f32_16x16x32_bf16 v[32:35], a[176:179], v[160:163], v[32:35]\n"
    "s_add_u32 m0, s58, 114688\n"
    "v_exp_f32 v241, v149\n"
    "v_mfma_f32_16x16x32_bf16 v[36:39], a[176:179], v[164:167], v[36:39]\n"
    "s_waitcnt lgkmcnt(10)\n"
    "buffer_load_dwordx4 v192, s[40:43], s55 offen offset:0 lds\n"
    "v_mfma_f32_16x16x32_bf16 v[44:47], a[180:183], v[164:167], v[44:47]\n"
    "ds_read_b64_tr_b16 a[176:177], v184 offset:256\n"
    "v_add_f32 v217, v240, v241\n"
    "v_mfma_f32_16x16x32_bf16 v[40:43], a[180:183], v[160:163], v[40:43]\n"
    "ds_read_b64_tr_b16 a[178:179], v184 offset:8448\n"
    "v_cvt_pk_bf16_f32 v172, v240, v241\n"
    "v_mfma_f32_16x16x32_bf16 v[48:51], a[184:187], v[160:163], v[48:51]\n"
    "v_exp_f32 v242, v150\n"
    "v_mfma_f32_16x16x32_bf16 v[52:55], a[184:187], v[164:167], v[52:55]\n"
    "ds_read_b64_tr_b16 a[180:181], v185 offset:256\n"
    "v_add_f32 v217, v217, v242\n"
    "v_mfma_f32_16x16x32_bf16 v[60:63], a[188:191], v[164:167], v[60:63]\n"
    "ds_read_b64_tr_b16 a[182:183], v185 offset:8448\n"
    "ds_read_b64_tr_b16 a[184:185], v186 offset:256\n"
    "v_mfma_f32_16x16x32_bf16 v[56:59], a[188:191], v[160:163], v[56:59]\n"
    "s_waitcnt lgkmcnt(13)\n"
    "ds_read_b64_tr_b16 a[186:187], v186 offset:8448\n"
    "ds_read_b64_tr_b16 a[188:189], v187 offset:256\n"
    "v_mfma_f32_16x16x32_bf16 v[64:67], a[160:163], v[160:163], v[64:67]\n"
    "v_exp_f32 v243, v151\n"
    "v_mfma_f32_16x16x32_bf16 v[68:71], a[160:163], v[164:167], v[68:71]\n"
    "s_waitcnt lgkmcnt(13)\n"
    "buffer_load_dwordx4 v193, s[40:43], s55 offen offset:1024 lds\n"
    "v_mfma_f32_16x16x32_bf16 v[76:79], a[164:167], v[164:167], v[76:79]\n"
    "ds_read_b64_tr_b16 a[190:191], v187 offset:8448\n"
    "v_add_f32 v217, v217, v243\n"
    "v_mfma_f32_16x16x32_bf16 v[72:75], a[164:167], v[160:163], v[72:75]\n"
    "s_waitcnt lgkmcnt(12)\n"
    "ds_read_b128 a[136:139], v178 offset:32768\n"
    "v_cvt_pk_bf16_f32 v173, v242, v243\n"
    "v_mfma_f32_16x16x32_bf16 v[80:83], a[168:171], v[160:163], v[80:83]\n"
    "v_exp_f32 v240, v152\n"
    "v_mfma_f32_16x16x32_bf16 v[84:87], a[168:171], v[164:167], v[84:87]\n"
    "s_waitcnt lgkmcnt(9)\n"
    "ds_read_b128 a[140:143], v179 offset:32768\n"
    "v_add_f32 v216, v216, v240\n"
    "v_mfma_f32_16x16x32_bf16 v[92:95], a[172:175], v[164:167], v[92:95]\n"
    "ds_read_b128 a[144:147], v176 offset:33024\n"
    "ds_read_b128 a[148:151], v177 offset:33024\n"
    "v_mfma_f32_16x16x32_bf16 v[88:91], a[172:175], v[160:163], v[88:91]\n"
    "s_waitcnt lgkmcnt(10)\n"
    "ds_read_b128 a[152:155], v178 offset:33024\n"
    "ds_read_b128 a[156:159], v179 offset:33024\n"
    "v_mfma_f32_16x16x32_bf16 v[96:99], a[176:179], v[160:163], v[96:99]\n"
    "v_exp_f32 v241, v153\n"
    "v_mfma_f32_16x16x32_bf16 v[100:103], a[176:179], v[164:167], v[100:103]\n"
    "s_waitcnt lgkmcnt(10)\n"
    "buffer_load_dwordx4 v194, s[40:43], s55 offen offset:2048 lds\n"
    "v_mfma_f32_16x16x32_bf16 v[108:111], a[180:183], v[164:167], v[108:111]\n"
    "v_add_f32 v216, v216, v241\n"
    "v_cvt_pk_bf16_f32 v170, v240, v241\n"
    "v_mfma_f32_16x16x32_bf16 v[104:107], a[180:183], v[160:163], v[104:107]\n"
    "s_waitcnt lgkmcnt(8)\n"
    "v_mfma_f32_16x16x32_bf16 v[112:115], a[184:187], v[160:163], v[112:115]\n"
    "v_exp_f32 v242, v154\n"
    "v_mfma_f32_16x16x32_bf16 v[116:119], a[184:187], v[164:167], v[116:119]\n"
    "s_waitcnt lgkmcnt(6)\n"
    "buffer_load_dwordx4 v195, s[40:43], s55 offen offset:3072 lds\n"
    "v_mfma_f32_16x16x32_bf16 v[124:127], a[188:191], v[164:167], v[124:127]\n"
    "s_add_u32 s65, s65, 1\n"
    "v_readlane_b32 s55, v211, s59\n"
    "v_mfma_f32_16x16x32_bf16 v[120:123], a[188:191], v[160:163], v[120:123]\n"
    "s_cbranch_scc1 .LX1\n"
    ".LG2:\n"
    "v_mfma_f32_16x16x32_bf16 v[128:131], a[128:131], a[0:3], v[236:239]\n"
    "v_exp_f32 v243, v155\n"
    "v_mfma_f32_16x16x32_bf16 v[128:131], a[132:135], a[8:11], v[128:131]\n"
    "s_waitcnt lgkmcnt(4)\n"
    "ds_read_b64_tr_b16 a[160:161], v180 offset:16384\n"
    "v_add_f32 v216, v216, v242\n"
    "v_mfma_f32_16x16x32_bf16 v[128:131], a[136:139], a[16:19], v[128:131]\n"
    "s_add_u32 s59, s59, 1\n"
    "ds_read_b64_tr_b16 a[162:163], v180 offset:24576\n"
    "v_add_f32 v216, v216, v243\n"
    "v_mfma_f32_16x16x32_bf16 v[128:131], a[140:143], a[24:27], v[128:131]\n"
    "ds_read_b64_tr_b16 a[164:165], v181 offset:16384\n"
    "v_cvt_pk_bf16_f32 v171, v242, v243\n"
    "v_mfma_f32_16x16x32_bf16 v[132:135], a[140:143], a[28:31], v[244:247]\n"
    "s_add_u32 m0, s58, 16384\n"
    "v_exp_f32 v240, v156\n"
    "v_mfma_f32_16x16x32_bf16 v[132:135], a[128:131], a[4:7], v[132:135]\n"
    "buffer_load_dwordx4 v188, s[36:39], s55 offen offset:0 lds\n"
    "v_mfma_f32_16x16x32_bf16 v[132:135], a[132:135], a[12:15], v[132:135]\n"
    "ds_read_b128 a[140:143], v179 offset:40960\n"
    "v_add_f32 v217, v217, v240\n"
    "v_mfma_f32_16x16x32_bf16 v[132:135], a[136:139], a[20:23], v[132:135]\n"
    "s_waitcnt lgkmcnt(6)\n"
    "ds_read_b128 a[128:131], v176 offset:40960\n"
    "ds_read_b128 a[132:135], v177 offset:40960\n"
    "v_mfma_f32_16x16x32_bf16 v[132:135], a[144:147], a[36:39], v[132:135]\n"
    "v_exp_f32 v241, v157\n"
    "v_mfma_f32_16x16x32_bf16 v[132:135], a[148:151], a[44:47], v[132:135]\n"
    "s_waitcnt lgkmcnt(6)\n"
    "ds_read_b128 a[136:139], v178 offset:40960\n"
    "v_add_f32 v217, v217, v241\n"
    "v_mfma_f32_16x16x32_bf16 v[132:135], a[152:155], a[52:55], v[132:135]\n"
    "ds_read_b64_tr_b16 a[166:167], v181 offset:24576\n"
    "v_cvt_pk_bf16_f32 v174, v240, v241\n"
    "v_mfma_f32_16x16x32_bf16 v[132:135], a[156:159], a[60:63], v[132:135]\n"
    "ds_read_b64_tr_b16 a[168:169], v182 offset:16384\n"
    "ds_read_b64_tr_b16 a[170:171], v182 offset:24576\n"
    "v_mfma_f32_16x16x32_bf16 v[128:131], a[156:159], a[56:59], v[128:131]\n"
    "v_exp_f32 v242, v158\n"
    "v_mfma_f32_16x16x32_bf16 v[128:131], a[144:147], a[32:35], v[128:131]\n"
    "buffer_load_dwordx4 v189, s[36:39], s55 offen offset:1024 lds\n"
    "v_mfma_f32_16x16x32_bf16 v[128:131], a[148:151], a[40:43], v[128:131]\n"
    "ds_read_b128 a[156:159], v179 offset:41216\n"
    "v_add_f32 v217, v217, v242\n"
    "v_mfma_f32_16x16x32_bf16 v[128:131], a[152:155], a[48:51], v[128:131]\n"
    "s_waitcnt lgkmcnt(5)\n"
    "ds_read_b128 a[144:147], v176 offset:41216\n"
    "ds_read_b128 a[148:151], v177 offset:41216\n"
    "v_mfma_f32_16x16x32_bf16 v[136:139], a[128:131], a[0:3], v[248:251]\n"
    "v_exp_f32 v243, v159\n"
    "v_mfma_f32_16x16x32_bf16 v[136:139], a[132:135], a[8:11], v[136:139]\n"
    "s_waitcnt lgkmcnt(6)\n"
    "ds_read_b128 a[152:155], v178 offset:41216\n"
    "v_add_f32 v217, v217, v243\n"
    "v_mfma_f32_16x16x32_bf16 v[136:139], a[136:139], a[16:19], v[136:139]\n"
    "ds_read_b64_tr_b16 a[172:173], v183 offset:16384\n"
    "v_add_f32 v215, v216, v217\n"
    "v_mfma_f32_16x16x32_bf16 v[136:139], a[140:143], a[24:27], v[136:139]\n"
    "ds_read_b64_tr_b16 a[174:175], v183 offset:24576\n"
    "v_cmp_lt_f32 vcc, s29, v215\n"
    "v_mfma_f32_16x16x32_bf16 v[140:143], a[140:143], a[28:31], v[252:255]\n"
    "ds_read_b64_tr_b16 a[176:177], v184 offset:16384\n"
    "v_cvt_pk_bf16_f32 v175, v242, v243\n"
    "v_mfma_f32_16x16x32_bf16 v[140:143], a[128:131], a[4:7], v[140:143]\n"
    "s_cbranch_vccnz .Lstub_8\n"
    ".Lstubb_9:\n"
    "buffer_load_dwordx4 v190, s[36:39], s55 offen offset:2048 lds\n"
    "v_mfma_f32_16x16x32_bf16 v[140:143], a[132:135], a[12:15], v[140:143]\n"
    "ds_read_b64_tr_b16 a[178:179], v184 offset:24576\n"
    "v_add_f32 v198, v198, v216\n"
    "v_mfma_f32_16x16x32_bf16 v[140:143], a[136:139], a[20:23], v[140:143]\n"
    "s_waitcnt lgkmcnt(5)\n"
    "v_exp_f32 v240, v128\n"
    "v_mfma_f32_16x16x32_bf16 v[140:143], a[144:147], a[36:39], v[140:143]\n"
    "ds_read_b64_tr_b16 a[180:181], v185 offset:16384\n"
    "v_add_f32 v199, v199, v217\n"
    "v_mfma_f32_16x16x32_bf16 v[140:143], a[148:151], a[44:47], v[140:143]\n"
    "s_waitcnt lgkmcnt(5)\n"
    "ds_read_b64_tr_b16 a[182:183], v185 offset:24576\n"
    "ds_read_b64_tr_b16 a[184:185], v186 offset:16384\n"
    "v_mfma_f32_16x16x32_bf16 v[140:143], a[152:155], a[52:55], v[140:143]\n"
    "v_exp_f32 v241, v129\n"
    "v_mfma_f32_16x16x32_bf16 v[140:143], a[156:159], a[60:63], v[140:143]\n"
    "ds_read_b64_tr_b16 a[186:187], v186 offset:24576\n"
    "v_add_f32 v216, v240, v241\n"
    "v_mfma_f32_16x16x32_bf16 v[136:139], a[156:159], a[56:59], v[136:139]\n"
    "ds_read_b64_tr_b16 a[188:189], v187 offset:16384\n"
    "v_cvt_pk_bf16_f32 v160, v240, v241\n"
    "v_mfma_f32_16x16x32_bf16 v[136:139], a[144:147], a[32:35], v[136:139]\n"
    "v_exp_f32 v242, v130\n"
    "v_mfma_f32_16x16x32_bf16 v[136:139], a[148:151], a[40:43], v[136:139]\n"
    "buffer_load_dwordx4 v191, s[36:39], s55 offen offset:3072 lds\n"
    "v_mfma_f32_16x16x32_bf16 v[136:139], a[152:155], a[48:51], v[136:139]\n"
    "ds_read_b64_tr_b16 a[190:191], v187 offset:24576\n"
    "v_add_f32 v216, v216, v242\n"
    "v_mfma_f32_16x16x32_bf16 v[0:3], a[160:163], v[168:171], v[0:3]\n"
    "v_exp_f32 v243, v131\n"
    "v_mfma_f32_16x16x32_bf16 v[4:7], a[160:163], v[172:175], v[4:7]\n"
    "ds_read_b64_tr_b16 a[160:161], v180 offset:16640\n"
    "v_add_f32 v216, v216, v243\n"
    "v_mfma_f32_16x16x32_bf16 v[12:15], a[164:167], v[172:175], v[12:15]\n"
    "s_waitcnt vmcnt(12)\n"
    "s_barrier\n"
    "ds_read_b64_tr_b16 a[162:163], v180 offset:24832\n"
    "v_cvt_pk_bf16_f32 v161, v242, v243\n"
    "v_mfma_f32_16x16x32_bf16 v[8:11], a[164:167], v[168:171], v[8:11]\n"
    "ds_read_b64_tr_b16 a[164:165], v181 offset:16640\n"
    "ds_read_b64_tr_b16 a[166:167], v181 offset:24832\n"
    "v_mfma_f32_16x16x32_bf16 v[16:19], a[168:171], v[168:171], v[16:19]\n"
    "v_exp_f32 v240, v132\n"
    "v_mfma_f32_16x16x32_bf16 v[20:23], a[168:171], v[172:175], v[20:23]\n"
    "s_waitcnt lgkmcnt(12)\n"
    "ds_read_b64_tr_b16 a[168:169], v182 offset:16640\n"
    "ds_read_b64_tr_b16 a[170:171], v182 offset:24832\n"
    "v_mfma_f32_16x16x32_bf16 v[28:31], a[172:175], v[172:175], v[28:31]\n"
    "ds_read_b128 a[128:131], v176 offset:49152\n"
    "s_waitcnt lgkmcnt(11)\n"
    "ds_read_b128 a[132:135], v177 offset:49152\n"
    "v_mfma_f32_16x16x32_bf16 v[24:27], a[172:175], v[168:171], v[24:27]\n"
    "ds_read_b64_tr_b16 a[172:173], v183 offset:16640\n"
    "ds_read_b64_tr_b16 a[174:175], v183 offset:24832\n"
    "v_mfma_f32_16x16x32_bf16 v[32:35], a[176:179], v[168:171], v[32:35]\n"
    "s_add_u32 m0, s58, 65536\n"
    "v_exp_f32 v241, v133\n"
    "v_mfma_f32_16x16x32_bf16 v[36:39], a[176:179], v[172:175], v[36:39]\n"
    "s_waitcnt lgkmcnt(10)\n"
    "buffer_load_dwordx4 v192, s[40:43], s54 offen offset:0 lds\n"
    "v_mfma_f32_16x16x32_bf16 v[44:47], a[180:183], v[172:175], v[44:47]\n"
    "ds_read_b64_tr_b16 a[176:177], v184 offset:16640\n"
    "v_add_f32 v217, v240, v241\n"
    "v_mfma_f32_16x16x32_bf16 v[40:43], a[180:183], v[168:171], v[40:43]\n"
    "ds_read_b64_tr_b16 a[178:179], v184 offset:24832\n"
    "v_cvt_pk_bf16_f32 v164, v240, v241\n"
    "v_mfma_f32_16x16x32_bf16 v[48:51], a[184:187], v[168:171], v[48:51]\n"
    "v_exp_f32 v242, v134\n"
    "v_mfma_f32_16x16x32_bf16 v[52:55], a[184:187], v[172:175], v[52:55]\n"
    "ds_read_b64_tr_b16 a[180:181], v185 offset:16640\n"
    "v_add_f32 v217, v217, v242\n"
    "v_mfma_f32_16x16x32_bf16 v[60:63], a[188:191], v[172:175], v[60:63]\n"
    "ds_read_b64_tr_b16 a[182:183], v185 offset:24832\n"
    "ds_read_b64_tr_b16 a[184:185], v186 offset:16640\n"
    "v_mfma_f32_16x16x32_bf16 v[56:59], a[188:191], v[168:171], v[56:59]\n"
    "s_waitcnt lgkmcnt(13)\n"
    "ds_read_b64_tr_b16 a[186:187], v186 offset:24832\n"
    "ds_read_b64_tr_b16 a[188:189], v187 offset:16640\n"
    "v_mfma_f32_16x16x32_bf16 v[64:67], a[160:163], v[168:171], v[64:67]\n"
    "v_exp_f32 v243, v135\n"
    "v_mfma_f32_16x16x32_bf16 v[68:71], a[160:163], v[172:175], v[68:71]\n"
    "s_waitcnt lgkmcnt(13)\n"
    "buffer_load_dwordx4 v193, s[40:43], s54 offen offset:1024 lds\n"
    "v_mfma_f32_16x16x32_bf16 v[76:79], a[164:167], v[172:175], v[76:79]\n"
    "ds_read_b64_tr_b16 a[190:191], v187 offset:24832\n"
    "v_add_f32 v217, v217, v243\n"
    "v_mfma_f32_16x16x32_bf16 v[72:75], a[164:167], v[168:171], v[72:75]\n"
    "s_waitcnt lgkmcnt(12)\n"
    "ds_read_b128 a[136:139], v178 offset:49152\n"
    "v_cvt_pk_bf16_f32 v165, v242, v243\n"
    "v_mfma_f32_16x16x32_bf16 v[80:83], a[168:171], v[168:171], v[80:83]\n"
    "v_exp_f32 v240, v136\n"
    "v_mfma_f32_16x16x32_bf16 v[84:87], a[168:171], v[172:175], v[84:87]\n"
    "s_waitcnt lgkmcnt(9)\n"
    "ds_read_b128 a[140:143], v179 offset:49152\n"
    "v_add_f32 v216, v216, v240\n"
    "v_mfma_f32_16x16x32_bf16 v[92:95], a[172:175], v[172:175], v[92:95]\n"
    "ds_read_b128 a[144:147], v176 offset:49408\n"
    "ds_read_b128 a[148:151], v177 offset:49408\n"
    "v_mfma_f32_16x16x32_bf16 v[88:91], a[172:175], v[168:171], v[88:91]\n"
    "s_waitcnt lgkmcnt(10)\n"
    "ds_read_b128 a[152:155], v178 offset:49408\n"
    "ds_read_b128 a[156:159], v179 offset:49408\n"
    "v_mfma_f32_16x16x32_bf16 v[96:99], a[176:179], v[168:171], v[96:99]\n"
    "v_exp_f32 v241, v137\n"
    "v_mfma_f32_16x16x32_bf16 v[100:103], a[176:179], v[172:175], v[100:103]\n"
    "s_waitcnt lgkmcnt(10)\n"
    "buffer_load_dwordx4 v194, s[40:43], s54 offen offset:2048 lds\n"
    "v_mfma_f32_16x16x32_bf16 v[108:111], a[180:183], v[172:175], v[108:111]\n"
    "v_add_f32 v216, v216, v241\n"
    "v_cvt_pk_bf16_f32 v162, v240, v241\n"
    "v_mfma_f32_16x16x32_bf16 v[104:107], a[180:183], v[168:171], v[104:107]\n"
    "s_waitcnt lgkmcnt(8)\n"
    "v_mfma_f32_16x16x32_bf16 v[112:115], a[184:187], v[168:171], v[112:115]\n"
    "v_exp_f32 v242, v138\n"
    "v_mfma_f32_16x16x32_bf16 v[116:119], a[184:187], v[172:175], v[116:119]\n"
    "s_waitcnt lgkmcnt(6)\n"
    "buffer_load_dwordx4 v195, s[40:43], s54 offen offset:3072 lds\n"
    "v_mfma_f32_16x16x32_bf16 v[124:127], a[188:191], v[172:175], v[124:127]\n"
    "s_add_u32 s65, s65, 1\n"
    "v_readlane_b32 s54, v211, s59\n"
    "v_mfma_f32_16x16x32_bf16 v[120:123], a[188:191], v[168:171], v[120:123]\n"
    "s_cbranch_scc1 .LX2\n"
    ".LG3:\n"
    "v_mfma_f32_16x16x32_bf16 v[144:147], a[128:131], a[0:3], v[236:239]\n"
    "v_exp_f32 v243, v139\n"
    "v_mfma_f32_16x16x32_bf16 v[144:147], a[132:135], a[8:11], v[144:147]\n"
    "s_waitcnt lgkmcnt(4)\n"
    "ds_read_b64_tr_b16 a[160:161], v180 offset:32768\n"
    "v_add_f32 v216, v216, v242\n"
    "v_mfma_f32_16x16x32_bf16 v[144:147], a[136:139], a[16:19], v[144:147]\n"
    "s_add_u32 s59, s59, 1\n"
    "ds_read_b64_tr_b16 a[162:163], v180 offset:40960\n"
    "v_add_f32 v216, v216, v243\n"
    "v_mfma_f32_16x16x32_bf16 v[144:147], a[140:143], a[24:27], v[144:147]\n"
    "ds_read_b64_tr_b16 a[164:165], v181 offset:32768\n"
    "v_cvt_pk_bf16_f32 v163, v242, v243\n"
    "v_mfma_f32_16x16x32_bf16 v[148:151], a[140:143], a[28:31], v[244:247]\n"
    "s_add_u32 m0, s58, 32768\n"
    "v_exp_f32 v240, v140\n"
    "v_mfma_f32_16x16x32_bf16 v[148:151], a[128:131], a[4:7], v[148:151]\n"
    "buffer_load_dwordx4 v188, s[36:39], s54 offen offset:0 lds\n"
    "v_mfma_f32_16x16x32_bf16 v[148:151], a[132:135], a[12:15], v[148:151]\n"
    "ds_read_b128 a[140:143], v179 offset:57344\n"
    "v_add_f32 v217, v217, v240\n"
    "v_mfma_f32_16x16x32_bf16 v[148:151], a[136:139], a[20:23], v[148:151]\n"
    "s_waitcnt lgkmcnt(6)\n"
    "ds_read_b128 a[128:131], v176 offset:57344\n"
    "ds_read_b128 a[132:135], v177 offset:57344\n"
    "v_mfma_f32_16x16x32_bf16 v[148:151], a[144:147], a[36:39], v[148:151]\n"
    "v_exp_f32 v241, v141\n"
    "v_mfma_f32_16x16x32_bf16 v[148:151], a[148:151], a[44:47], v[148:151]\n"
    "s_waitcnt lgkmcnt(6)\n"
    "ds_read_b128 a[136:139], v178 offset:57344\n"
    "v_add_f32 v217, v217, v241\n"
    "v_mfma_f32_16x16x32_bf16 v[148:151], a[152:155], a[52:55], v[148:151]\n"
    "ds_read_b64_tr_b16 a[166:167], v181 offset:40960\n"
    "v_cvt_pk_bf16_f32 v166, v240, v241\n"
    "v_mfma_f32_16x16x32_bf16 v[148:151], a[156:159], a[60:63], v[148:151]\n"
    "ds_read_b64_tr_b16 a[168:169], v182 offset:32768\n"
    "ds_read_b64_tr_b16 a[170:171], v182 offset:40960\n"
    "v_mfma_f32_16x16x32_bf16 v[144:147], a[156:159], a[56:59], v[144:147]\n"
    "v_exp_f32 v242, v142\n"
    "v_mfma_f32_16x16x32_bf16 v[144:147], a[144:147], a[32:35], v[144:147]\n"
    "buffer_load_dwordx4 v189, s[36:39], s54 offen offset:1024 lds\n"
    "v_mfma_f32_16x16x32_bf16 v[144:147], a[148:151], a[40:43], v[144:147]\n"
    "ds_read_b128 a[156:159], v179 offset:57600\n"
    "v_add_f32 v217, v217, v242\n"
    "v_mfma_f32_16x16x32_bf16 v[144:147], a[152:155], a[48:51], v[144:147]\n"
    "s_waitcnt lgkmcnt(5)\n"
    "ds_read_b128 a[144:147], v176 offset:57600\n"
    "ds_read_b128 a[148:151], v177 offset:57600\n"
    "v_mfma_f32_16x16x32_bf16 v[152:155], a[128:131], a[0:3], v[248:251]\n"
    "v_exp_f32 v243, v143\n"
    "v_mfma_f32_16x16x32_bf16 v[152:155], a[132:135], a[8:11], v[152:155]\n"
    "s_waitcnt lgkmcnt(6)\n"
    "ds_read_b128 a[152:155], v178 offset:57600\n"
    "v_add_f32 v217, v217, v243\n"
    "v_mfma_f32_16x16x32_bf16 v[152:155], a[136:139], a[16:19], v[152:155]\n"
    "ds_read_b64_tr_b16 a[172:173], v183 offset:32768\n"
    "v_add_f32 v215, v216, v217\n"
    "v_mfma_f32_16x16x32_bf16 v[152:155], a[140:143], a[24:27], v[152:155]\n"
    "ds_read_b64_tr_b16 a[174:175], v183 offset:40960\n"
    "v_cmp_lt_f32 vcc, s29, v215\n"
    "v_mfma_f32_16x16x32_bf16 v[156:159], a[140:143], a[28:31], v[252:255]\n"
    "ds_read_b64_tr_b16 a[176:177], v184 offset:32768\n"
    "v_cvt_pk_bf16_f32 v167, v242, v243\n"
    "v_mfma_f32_16x16x32_bf16 v[156:159], a[128:131], a[4:7], v[156:159]\n"
    "s_cbranch_vccnz .Lstub_10\n"
    ".Lstubb_11:\n"
    "buffer_load_dwordx4 v190, s[36:39], s54 offen offset:2048 lds\n"
    "v_mfma_f32_16x16x32_bf16 v[156:159], a[132:135], a[12:15], v[156:159]\n"
    "ds_read_b64_tr_b16 a[178:179], v184 offset:40960\n"
    "v_add_f32 v198, v198, v216\n"
    "v_mfma_f32_16x16x32_bf16 v[156:159], a[136:139], a[20:23], v[156:159]\n"
    "s_waitcnt lgkmcnt(5)\n"
    "v_exp_f32 v240, v144\n"
    "v_mfma_f32_16x16x32_bf16 v[156:159], a[144:147], a[36:39], v[156:159]\n"
    "ds_read_b64_tr_b16 a[180:181], v185 offset:32768\n"
    "v_add_f32 v199, v199, v217\n"
    "v_mfma_f32_16x16x32_bf16 v[156:159], a[148:151], a[44:47], v[156:159]\n"
    "s_waitcnt lgkmcnt(5)\n"
    "ds_read_b64_tr_b16 a[182:183], v185 offset:40960\n"
    "ds_read_b64_tr_b16 a[184:185], v186 offset:32768\n"
    "v_mfma_f32_16x16x32_bf16 v[156:159], a[152:155], a[52:55], v[156:159]\n"
    "v_exp_f32 v241, v145\n"
    "v_mfma_f32_16x16x32_bf16 v[156:159], a[156:159], a[60:63], v[156:159]\n"
    "ds_read_b64_tr_b16 a[186:187], v186 offset:40960\n"
    "v_add_f32 v216, v240, v241\n"
    "v_mfma_f32_16x16x32_bf16 v[152:155], a[156:159], a[56:59], v[152:155]\n"
    "ds_read_b64_tr_b16 a[188:189], v187 offset:32768\n"
    "v_cvt_pk_bf16_f32 v168, v240, v241\n"
    "v_mfma_f32_16x16x32_bf16 v[152:155], a[144:147], a[32:35], v[152:155]\n"
    "v_exp_f32 v242, v146\n"
    "v_mfma_f32_16x16x32_bf16 v[152:155], a[148:151], a[40:43], v[152:155]\n"
    "buffer_load_dwordx4 v191, s[36:39], s54 offen offset:3072 lds\n"
    "v_mfma_f32_16x16x32_bf16 v[152:155], a[152:155], a[48:51], v[152:155]\n"
    "ds_read_b64_tr_b16 a[190:191], v187 offset:40960\n"
    "v_add_f32 v216, v216, v242\n"
    "v_mfma_f32_16x16x32_bf16 v[0:3], a[160:163], v[160:163], v[0:3]\n"
    "v_exp_f32 v243, v147\n"
    "v_mfma_f32_16x16x32_bf16 v[4:7], a[160:163], v[164:167], v[4:7]\n"
    "ds_read_b64_tr_b16 a[160:161], v180 offset:33024\n"
    "v_add_f32 v216, v216, v243\n"
    "v_mfma_f32_16x16x32_bf16 v[12:15], a[164:167], v[164:167], v[12:15]\n"
    "s_waitcnt vmcnt(12)\n"
    "s_barrier\n"
    "ds_read_b64_tr_b16 a[162:163], v180 offset:41216\n"
    "v_cvt_pk_bf16_f32 v169, v242, v243\n"
    "v_mfma_f32_16x16x32_bf16 v[8:11], a[164:167], v[160:163], v[8:11]\n"
    "ds_read_b64_tr_b16 a[164:165], v181 offset:33024\n"
    "ds_read_b64_tr_b16 a[166:167], v181 offset:41216\n"
    "v_mfma_f32_16x16x32_bf16 v[16:19], a[168:171], v[160:163], v[16:19]\n"
    "v_exp_f32 v240, v148\n"
    "v_mfma_f32_16x16x32_bf16 v[20:23], a[168:171], v[164:167], v[20:23]\n"
    "s_waitcnt lgkmcnt(12)\n"
    "ds_read_b64_tr_b16 a[168:169], v182 offset:33024\n"
    "ds_read_b64_tr_b16 a[170:171], v182 offset:41216\n"
    "v_mfma_f32_16x16x32_bf16 v[28:31], a[172:175], v[164:167], v[28:31]\n"
    "ds_read_b128 a[128:131], v176 offset:0\n"
    "s_waitcnt lgkmcnt(11)\n"
    "ds_read_b128 a[132:135], v177 offset:0\n"
    "v_mfma_f32_16x16x32_bf16 v[24:27], a[172:175], v[160:163], v[24:27]\n"
    "ds_read_b64_tr_b16 a[172:173], v183 offset:33024\n"
    "ds_read_b64_tr_b16 a[174:175], v183 offset:41216\n"
    "v_mfma_f32_16x16x32_bf16 v[32:35], a[176:179], v[160:163], v[32:35]\n"
    "s_add_u32 m0, s58, 81920\n"
    "v_exp_f32 v241, v149\n"
    "v_mfma_f32_16x16x32_bf16 v[36:39], a[176:179], v[164:167], v[36:39]\n"
    "s_waitcnt lgkmcnt(10)\n"
    "buffer_load_dwordx4 v192, s[40:43], s55 offen offset:0 lds\n"
    "v_mfma_f32_16x16x32_bf16 v[44:47], a[180:183], v[164:167], v[44:47]\n"
    "ds_read_b64_tr_b16 a[176:177], v184 offset:33024\n"
    "v_add_f32 v217, v240, v241\n"
    "v_mfma_f32_16x16x32_bf16 v[40:43], a[180:183], v[160:163], v[40:43]\n"
    "ds_read_b64_tr_b16 a[178:179], v184 offset:41216\n"
    "v_cvt_pk_bf16_f32 v172, v240, v241\n"
    "v_mfma_f32_16x16x32_bf16 v[48:51], a[184:187], v[160:163], v[48:51]\n"
    "v_exp_f32 v242, v150\n"
    "v_mfma_f32_16x16x32_bf16 v[52:55], a[184:187], v[164:167], v[52:55]\n"
    "ds_read_b64_tr_b16 a[180:181], v185 offset:33024\n"
    "v_add_f32 v217, v217, v242\n"
    "v_mfma_f32_16x16x32_bf16 v[60:63], a[188:191], v[164:167], v[60:63]\n"
    "ds_read_b64_tr_b16 a[182:183], v185 offset:41216\n"
    "ds_read_b64_tr_b16 a[184:185], v186 offset:33024\n"
    "v_mfma_f32_16x16x32_bf16 v[56:59], a[188:191], v[160:163], v[56:59]\n"
    "s_waitcnt lgkmcnt(13)\n"
    "ds_read_b64_tr_b16 a[186:187], v186 offset:41216\n"
    "ds_read_b64_tr_b16 a[188:189], v187 offset:33024\n"
    "v_mfma_f32_16x16x32_bf16 v[64:67], a[160:163], v[160:163], v[64:67]\n"
    "v_exp_f32 v243, v151\n"
    "v_mfma_f32_16x16x32_bf16 v[68:71], a[160:163], v[164:167], v[68:71]\n"
    "s_waitcnt lgkmcnt(13)\n"
    "buffer_load_dwordx4 v193, s[40:43], s55 offen offset:1024 lds\n"
    "v_mfma_f32_16x16x32_bf16 v[76:79], a[164:167], v[164:167], v[76:79]\n"
    "ds_read_b64_tr_b16 a[190:191], v187 offset:41216\n"
    "v_add_f32 v217, v217, v243\n"
    "v_mfma_f32_16x16x32_bf16 v[72:75], a[164:167], v[160:163], v[72:75]\n"
    "s_waitcnt lgkmcnt(12)\n"
    "ds_read_b128 a[136:139], v178 offset:0\n"
    "v_cvt_pk_bf16_f32 v173, v242, v243\n"
    "v_mfma_f32_16x16x32_bf16 v[80:83], a[168:171], v[160:163], v[80:83]\n"
    "v_exp_f32 v240, v152\n"
    "v_mfma_f32_16x16x32_bf16 v[84:87], a[168:171], v[164:167], v[84:87]\n"
    "s_waitcnt lgkmcnt(9)\n"
    "ds_read_b128 a[140:143], v179 offset:0\n"
    "v_add_f32 v216, v216, v240\n"
    "v_mfma_f32_16x16x32_bf16 v[92:95], a[172:175], v[164:167], v[92:95]\n"
    "ds_read_b128 a[144:147], v176 offset:256\n"
    "ds_read_b128 a[148:151], v177 offset:256\n"
    "v_mfma_f32_16x16x32_bf16 v[88:91], a[172:175], v[160:163], v[88:91]\n"
    "s_waitcnt lgkmcnt(10)\n"
    "ds_read_b128 a[152:155], v178 offset:256\n"
    "ds_read_b128 a[156:159], v179 offset:256\n"
    "v_mfma_f32_16x16x32_bf16 v[96:99], a[176:179], v[160:163], v[96:99]\n"
    "v_exp_f32 v241, v153\n"
    "v_mfma_f32_16x16x32_bf16 v[100:103], a[176:179], v[164:167], v[100:103]\n"
    "s_waitcnt lgkmcnt(10)\n"
    "buffer_load_dwordx4 v194, s[40:43], s55 offen offset:2048 lds\n"
    "v_mfma_f32_16x16x32_bf16 v[108:111], a[180:183], v[164:167], v[108:111]\n"
    "v_add_f32 v216, v216, v241\n"
    "v_cvt_pk_bf16_f32 v170, v240, v241\n"
    "v_mfma_f32_16x16x32_bf16 v[104:107], a[180:183], v[160:163], v[104:107]\n"
    "s_waitcnt lgkmcnt(8)\n"
    "v_mfma_f32_16x16x32_bf16 v[112:115], a[184:187], v[160:163], v[112:115]\n"
    "v_exp_f32 v242, v154\n"
    "v_mfma_f32_16x16x32_bf16 v[116:119], a[184:187], v[164:167], v[116:119]\n"
    "s_waitcnt lgkmcnt(6)\n"
    "buffer_load_dwordx4 v195, s[40:43], s55 offen offset:3072 lds\n"
    "v_mfma_f32_16x16x32_bf16 v[124:127], a[188:191], v[164:167], v[124:127]\n"
    "s_add_u32 s65, s65, 1\n"
    "v_readlane_b32 s55, v211, s59\n"
    "v_mfma_f32_16x16x32_bf16 v[120:123], a[188:191], v[160:163], v[120:123]\n"
    "s_cbranch_scc1 .LX3\n"
    ".LG0:\n"
    "v_mfma_f32_16x16x32_bf16 v[128:131], a[128:131], a[0:3], v[236:239]\n"
    "v_exp_f32 v243, v155\n"
    "v_mfma_f32_16x16x32_bf16 v[128:131], a[132:135], a[8:11], v[128:131]\n"
    "s_waitcnt lgkmcnt(4)\n"
    "ds_read_b64_tr_b16 a[160:161], v180 offset:49152\n"
    "v_add_f32 v216, v216, v242\n"
    "v_mfma_f32_16x16x32_bf16 v[128:131], a[136:139], a[16:19], v[128:131]\n"
    "s_add_u32 s59, s59, 1\n"
    "ds_read_b64_tr_b16 a[162:163], v180 offset:57344\n"
    "v_add_f32 v216, v216, v243\n"
    "v_mfma_f32_16x16x32_bf16 v[128:131], a[140:143], a[24:27], v[128:131]\n"
    "ds_read_b64_tr_b16 a[164:165], v181 offset:49152\n"
    "v_cvt_pk_bf16_f32 v171, v242, v243\n"
    "v_mfma_f32_16x16x32_bf16 v[132:135], a[140:143], a[28:31], v[244:247]\n"
    "s_add_u32 m0, s58, 49152\n"
    "v_exp_f32 v240, v156\n"
    "v_mfma_f32_16x16x32_bf16 v[132:135], a[128:131], a[4:7], v[132:135]\n"
    "buffer_load_dwordx4 v188, s[36:39], s55 offen offset:0 lds\n"
    "v_mfma_f32_16x16x32_bf16 v[132:135], a[132:135], a[12:15], v[132:135]\n"
    "ds_read_b128 a[140:143], v179 offset:8192\n"
    "v_add_f32 v217, v217, v240\n"
    "v_mfma_f32_16x16x32_bf16 v[132:135], a[136:139], a[20:23], v[132:135]\n"
    "s_waitcnt lgkmcnt(6)\n"
    "ds_read_b128 a[128:131], v176 offset:8192\n"
    "ds_read_b128 a[132:135], v177 offset:8192\n"
    "v_mfma_f32_16x16x32_bf16 v[132:135], a[144:147], a[36:39], v[132:135]\n"
    "v_exp_f32 v241, v157\n"
    "v_mfma_f32_16x16x32_bf16 v[132:135], a[148:151], a[44:47], v[132:135]\n"
    "s_waitcnt lgkmcnt(6)\n"
    "ds_read_b128 a[136:139], v178 offset:8192\n"
    "v_add_f32 v217, v217, v241\n"
    "v_mfma_f32_16x16x32_bf16 v[132:135], a[152:155], a[52:55], v[132:135]\n"
    "ds_read_b64_tr_b16 a[166:167], v181 offset:57344\n"
    "v_cvt_pk_bf16_f32 v174, v240, v241\n"
    "v_mfma_f32_16x16x32_bf16 v[132:135], a[156:159], a[60:63], v[132:135]\n"
    "ds_read_b64_tr_b16 a[168:169], v182 offset:49152\n"
    "ds_read_b64_tr_b16 a[170:171], v182 offset:57344\n"
    "v_mfma_f32_16x16x32_bf16 v[128:131], a[156:159], a[56:59], v[128:131]\n"
    "v_exp_f32 v242, v158\n"
    "v_mfma_f32_16x16x32_bf16 v[128:131], a[144:147], a[32:35], v[128:131]\n"
    "buffer_load_dwordx4 v189, s[36:39], s55 offen offset:1024 lds\n"
    "v_mfma_f32_16x16x32_bf16 v[128:131], a[148:151], a[40:43], v[128:131]\n"
    "ds_read_b128 a[156:159], v179 offset:8448\n"
    "v_add_f32 v217, v217, v242\n"
    "v_mfma_f32_16x16x32_bf16 v[128:131], a[152:155], a[48:51], v[128:131]\n"
    "s_waitcnt lgkmcnt(5)\n"
    "ds_read_b128 a[144:147], v176 offset:8448\n"
    "ds_read_b128 a[148:151], v177 offset:8448\n"
    "v_mfma_f32_16x16x32_bf16 v[136:139], a[128:131], a[0:3], v[248:251]\n"
    "v_exp_f32 v243, v159\n"
    "v_mfma_f32_16x16x32_bf16 v[136:139], a[132:135], a[8:11], v[136:139]\n"
    "s_waitcnt lgkmcnt(6)\n"
    "ds_read_b128 a[152:155], v178 offset:8448\n"
    "v_add_f32 v217, v217, v243\n"
    "v_mfma_f32_16x16x32_bf16 v[136:139], a[136:139], a[16:19], v[136:139]\n"
    "ds_read_b64_tr_b16 a[172:173], v183 offset:49152\n"
    "v_add_f32 v215, v216, v217\n"
    "v_mfma_f32_16x16x32_bf16 v[136:139], a[140:143], a[24:27], v[136:139]\n"
    "ds_read_b64_tr_b16 a[174:175], v183 offset:57344\n"
    "v_cmp_lt_f32 vcc, s29, v215\n"
    "v_mfma_f32_16x16x32_bf16 v[140:143], a[140:143], a[28:31], v[252:255]\n"
    "ds_read_b64_tr_b16 a[176:177], v184 offset:49152\n"
    "v_cvt_pk_bf16_f32 v175, v242, v243\n"
    "v_mfma_f32_16x16x32_bf16 v[140:143], a[128:131], a[4:7], v[140:143]\n"
    "s_cbranch_vccnz .Lstub_12\n"
    ".Lstubb_13:\n"
    "buffer_load_dwordx4 v190, s[36:39], s55 offen offset:2048 lds\n"
    "v_mfma_f32_16x16x32_bf16 v[140:143], a[132:135], a[12:15], v[140:143]\n"
    "ds_read_b64_tr_b16 a[178:179], v184 offset:57344\n"
    "v_add_f32 v198, v198, v216\n"
    "v_mfma_f32_16x16x32_bf16 v[140:143], a[136:139], a[20:23], v[140:143]\n"
    "s_waitcnt lgkmcnt(5)\n"
    "v_exp_f32 v240, v128\n"
    "v_mfma_f32_16x16x32_bf16 v[140:143], a[144:147], a[36:39], v[140:143]\n"
    "ds_read_b64_tr_b16 a[180:181], v185 offset:49152\n"
    "v_add_f32 v199, v199, v217\n"
    "v_mfma_f32_16x16x32_bf16 v[140:143], a[148:151], a[44:47], v[140:143]\n"
    "s_waitcnt lgkmcnt(5)\n"
    "ds_read_b64_tr_b16 a[182:183], v185 offset:57344\n"
    "ds_read_b64_tr_b16 a[184:185], v186 offset:49152\n"
    "v_mfma_f32_16x16x32_bf16 v[140:143], a[152:155], a[52:55], v[140:143]\n"
    "v_exp_f32 v241, v129\n"
    "v_mfma_f32_16x16x32_bf16 v[140:143], a[156:159], a[60:63], v[140:143]\n"
    "ds_read_b64_tr_b16 a[186:187], v186 offset:57344\n"
    "v_add_f32 v216, v240, v241\n"
    "v_mfma_f32_16x16x32_bf16 v[136:139], a[156:159], a[56:59], v[136:139]\n"
    "ds_read_b64_tr_b16 a[188:189], v187 offset:49152\n"
    "v_cvt_pk_bf16_f32 v160, v240, v241\n"
    "v_mfma_f32_16x16x32_bf16 v[136:139], a[144:147], a[32:35], v[136:139]\n"
    "v_exp_f32 v242, v130\n"
    "v_mfma_f32_16x16x32_bf16 v[136:139], a[148:151], a[40:43], v[136:139]\n"
    "buffer_load_dwordx4 v191, s[36:39], s55 offen offset:3072 lds\n"
    "v_mfma_f32_16x16x32_bf16 v[136:139], a[152:155], a[48:51], v[136:139]\n"
    "ds_read_b64_tr_b16 a[190:191], v187 offset:57344\n"
    "v_add_f32 v216, v216, v242\n"
    "v_mfma_f32_16x16x32_bf16 v[0:3], a[160:163], v[168:171], v[0:3]\n"
    "v_exp_f32 v243, v131\n"
    "v_mfma_f32_16x16x32_bf16 v[4:7], a[160:163], v[172:175], v[4:7]\n"
    "ds_read_b64_tr_b16 a[160:161], v180 offset:49408\n"
    "v_add_f32 v216, v216, v243\n"
    "v_mfma_f32_16x16x32_bf16 v[12:15], a[164:167], v[172:175], v[12:15]\n"
    "s_waitcnt vmcnt(12)\n"
    "s_barrier\n"
    "ds_read_b64_tr_b16 a[162:163], v180 offset:57600\n"
    "v_cvt_pk_bf16_f32 v161, v242, v243\n"
    "v_mfma_f32_16x16x32_bf16 v[8:11], a[164:167], v[168:171], v[8:11]\n"
    "ds_read_b64_tr_b16 a[164:165], v181 offset:49408\n"
    "ds_read_b64_tr_b16 a[166:167], v181 offset:57600\n"
    "v_mfma_f32_16x16x32_bf16 v[16:19], a[168:171], v[168:171], v[16:19]\n"
    "v_exp_f32 v240, v132\n"
    "v_mfma_f32_16x16x32_bf16 v[20:23], a[168:171], v[172:175], v[20:23]\n"
    "s_waitcnt lgkmcnt(12)\n"
    "ds_read_b64_tr_b16 a[168:169], v182 offset:49408\n"
    "ds_read_b64_tr_b16 a[170:171], v182 offset:57600\n"
    "v_mfma_f32_16x16x32_bf16 v[28:31], a[172:175], v[172:175], v[28:31]\n"
    "ds_read_b128 a[128:131], v176 offset:16384\n"
    "s_waitcnt lgkmcnt(11)\n"
    "ds_read_b128 a[132:135], v177 offset:16384\n"
    "v_mfma_f32_16x16x32_bf16 v[24:27], a[172:175], v[168:171], v[24:27]\n"
    "ds_read_b64_tr_b16 a[172:173], v183 offset:49408\n"
    "ds_read_b64_tr_b16 a[174:175], v183 offset:57600\n"
    "v_mfma_f32_16x16x32_bf16 v[32:35], a[176:179], v[168:171], v[32:35]\n"
    "s_add_u32 m0, s58, 98304\n"
    "v_exp_f32 v241, v133\n"
    "v_mfma_f32_16x16x32_bf16 v[36:39], a[176:179], v[172:175], v[36:39]\n"
    "s_waitcnt lgkmcnt(10)\n"
    "buffer_load_dwordx4 v192, s[40:43], s54 offen offset:0 lds\n"
    "v_mfma_f32_16x16x32_bf16 v[44:47], a[180:183], v[172:175], v[44:47]\n"
    "ds_read_b64_tr_b16 a[176:177], v184 offset:49408\n"
    "v_add_f32 v217, v240, v241\n"
    "v_mfma_f32_16x16x32_bf16 v[40:43], a[180:183], v[168:171], v[40:43]\n"
    "ds_read_b64_tr_b16 a[178:179], v184 offset:57600\n"
    "v_cvt_pk_bf16_f32 v164, v240, v241\n"
    "v_mfma_f32_16x16x32_bf16 v[48:51], a[184:187], v[168:171], v[48:51]\n"
    "v_exp_f32 v242, v134\n"
    "v_mfma_f32_16x16x32_bf16 v[52:55], a[184:187], v[172:175], v[52:55]\n"
    "ds_read_b64_tr_b16 a[180:181], v185 offset:49408\n"
    "v_add_f32 v217, v217, v242\n"
    "v_mfma_f32_16x16x32_bf16 v[60:63], a[188:191], v[172:175], v[60:63]\n"
    "ds_read_b64_tr_b16 a[182:183], v185 offset:57600\n"
    "ds_read_b64_tr_b16 a[184:185], v186 offset:49408\n"
    "v_mfma_f32_16x16x32_bf16 v[56:59], a[188:191], v[168:171], v[56:59]\n"
    "s_waitcnt lgkmcnt(13)\n"
    "ds_read_b64_tr_b16 a[186:187], v186 offset:57600\n"
    "ds_read_b64_tr_b16 a[188:189], v187 offset:49408\n"
    "v_mfma_f32_16x16x32_bf16 v[64:67], a[160:163], v[168:171], v[64:67]\n"
    "v_exp_f32 v243, v135\n"
    "v_mfma_f32_16x16x32_bf16 v[68:71], a[160:163], v[172:175], v[68:71]\n"
    "s_waitcnt lgkmcnt(13)\n"
    "buffer_load_dwordx4 v193, s[40:43], s54 offen offset:1024 lds\n"
    "v_mfma_f32_16x16x32_bf16 v[76:79], a[164:167], v[172:175], v[76:79]\n"
    "ds_read_b64_tr_b16 a[190:191], v187 offset:57600\n"
    "v_add_f32 v217, v217, v243\n"
    "v_mfma_f32_16x16x32_bf16 v[72:75], a[164:167], v[168:171], v[72:75]\n"
    "s_waitcnt lgkmcnt(12)\n"
    "ds_read_b128 a[136:139], v178 offset:16384\n"
    "v_cvt_pk_bf16_f32 v165, v242, v243\n"
    "v_mfma_f32_16x16x32_bf16 v[80:83], a[168:171], v[168:171], v[80:83]\n"
    "v_exp_f32 v240, v136\n"
    "v_mfma_f32_16x16x32_bf16 v[84:87], a[168:171], v[172:175], v[84:87]\n"
    "s_waitcnt lgkmcnt(9)\n"
    "ds_read_b128 a[140:143], v179 offset:16384\n"
    "v_add_f32 v216, v216, v240\n"
    "v_mfma_f32_16x16x32_bf16 v[92:95], a[172:175], v[172:175], v[92:95]\n"
    "ds_read_b128 a[144:147], v176 offset:16640\n"
    "ds_read_b128 a[148:151], v177 offset:16640\n"
    "v_mfma_f32_16x16x32_bf16 v[88:91], a[172:175], v[168:171], v[88:91]\n"
    "s_waitcnt lgkmcnt(10)\n"
    "ds_read_b128 a[152:155], v178 offset:16640\n"
    "ds_read_b128 a[156:159], v179 offset:16640\n"
    "v_mfma_f32_16x16x32_bf16 v[96:99], a[176:179], v[168:171], v[96:99]\n"
    "v_exp_f32 v241, v137\n"
    "v_mfma_f32_16x16x32_bf16 v[100:103], a[176:179], v[172:175], v[100:103]\n"
    "s_waitcnt lgkmcnt(10)\n"
    "buffer_load_dwordx4 v194, s[40:43], s54 offen offset:2048 lds\n"
    "v_mfma_f32_16x16x32_bf16 v[108:111], a[180:183], v[172:175], v[108:111]\n"
    "v_add_f32 v216, v216, v241\n"
    "v_cvt_pk_bf16_f32 v162, v240, v241\n"
    "v_mfma_f32_16x16x32_bf16 v[104:107], a[180:183], v[168:171], v[104:107]\n"
    "s_waitcnt lgkmcnt(8)\n"
    "v_mfma_f32_16x16x32_bf16 v[112:115], a[184:187], v[168:171], v[112:115]\n"
    "v_exp_f32 v242, v138\n"
    "v_mfma_f32_16x16x32_bf16 v[116:119], a[184:187], v[172:175], v[116:119]\n"
    "s_waitcnt lgkmcnt(6)\n"
    "buffer_load_dwordx4 v195, s[40:43], s54 offen offset:3072 lds\n"
    "v_mfma_f32_16x16x32_bf16 v[124:127], a[188:191], v[172:175], v[124:127]\n"
    "s_add_u32 s65, s65, 1\n"
    "v_readlane_b32 s54, v211, s59\n"
    "v_mfma_f32_16x16x32_bf16 v[120:123], a[188:191], v[168:171], v[120:123]\n"
    "s_cbranch_scc0 .LG1\n"
    ".LX0:\n"
    "s_cmp_lg_u32 s63, 0\n"
    "s_cbranch_scc1 .Lev_14\n"
    "s_cmp_lg_u32 s35, 0\n"
    "s_cbranch_scc1 .Lev_15\n"
    "global_load_dword v212, v213, s[52:53] offset:0 sc1\n"
    "s_waitcnt lgkmcnt(0)\n"
    "ds_read_b64_tr_b16 a[160:161], v180 offset:0\n"
    "ds_read_b64_tr_b16 a[162:163], v180 offset:8192\n"
    "ds_read_b64_tr_b16 a[164:165], v181 offset:0\n"
    "ds_read_b64_tr_b16 a[166:167], v181 offset:8192\n"
    "ds_read_b64_tr_b16 a[168:169], v182 offset:0\n"
    "ds_read_b64_tr_b16 a[170:171], v182 offset:8192\n"
    "ds_read_b64_tr_b16 a[172:173], v183 offset:0\n"
    "ds_read_b64_tr_b16 a[174:175], v183 offset:8192\n"
    "ds_read_b64_tr_b16 a[176:177], v184 offset:0\n"
    "ds_read_b64_tr_b16 a[178:179], v184 offset:8192\n"
    "ds_read_b64_tr_b16 a[180:181], v185 offset:0\n"
    "ds_read_b64_tr_b16 a[182:183], v185 offset:8192\n"
    "ds_read_b64_tr_b16 a[184:185], v186 offset:0\n"
    "ds_read_b64_tr_b16 a[186:187], v186 offset:8192\n"
    "v_exp_f32 v243, v139\n"
    "v_add_f32 v216, v216, v242\n"
    "v_exp_f32 v240, v140\n"
    "v_add_f32 v216, v216, v243\n"
    "v_cvt_pk_bf16_f32 v163, v242, v243\n"
    "v_exp_f32 v241, v141\n"
    "v_add_f32 v217, v217, v240\n"
    "v_exp_f32 v242, v142\n"
    "v_add_f32 v217, v217, v241\n"
    "v_exp_f32 v243, v143\n"
    "v_add_f32 v217, v217, v242\n"
    "v_cvt_pk_bf16_f32 v166, v240, v241\n"
    "v_add_f32 v217, v217, v243\n"
    "v_add_f32 v215, v216, v217\n"
    "v_cmp_lt_f32 vcc, s29, v215\n"
    "v_cvt_pk_bf16_f32 v167, v242, v243\n"
    "s_cbranch_vccnz .Lstub_16\n"
    ".Lstubb_17:\n"
    "v_add_f32 v198, v198, v216\n"
    "v_add_f32 v199, v199, v217\n"
    "ds_read_b64_tr_b16 a[188:189], v187 offset:0\n"
    "s_waitcnt lgkmcnt(11)\n"
    "ds_read_b64_tr_b16 a[190:191], v187 offset:8192\n"
    "v_mfma_f32_16x16x32_bf16 v[0:3], a[160:163], v[160:163], v[0:3]\n"
    "v_mfma_f32_16x16x32_bf16 v[4:7], a[160:163], v[164:167], v[4:7]\n"
    "ds_read_b64_tr_b16 a[160:161], v180 offset:256\n"
    "v_mfma_f32_16x16x32_bf16 v[8:11], a[164:167], v[160:163], v[8:11]\n"
    "ds_read_b64_tr_b16 a[162:163], v180 offset:8448\n"
    "v_mfma_f32_16x16x32_bf16 v[12:15], a[164:167], v[164:167], v[12:15]\n"
    "ds_read_b64_tr_b16 a[164:165], v181 offset:256\n"
    "s_waitcnt lgkmcnt(13)\n"
    "v_mfma_f32_16x16x32_bf16 v[16:19], a[168:171], v[160:163], v[16:19]\n"
    "ds_read_b64_tr_b16 a[166:167], v181 offset:8448\n"
    "v_mfma_f32_16x16x32_bf16 v[20:23], a[168:171], v[164:167], v[20:23]\n"
    "ds_read_b64_tr_b16 a[168:169], v182 offset:256\n"
    "v_cvt_pk_bf16_f32 v0, v0, v1\n"
    "s_waitcnt lgkmcnt(13)\n"
    "v_mfma_f32_16x16x32_bf16 v[24:27], a[172:175], v[160:163], v[24:27]\n"
    "ds_read_b64_tr_b16 a[170:171], v182 offset:8448\n"
    "v_cvt_pk_bf16_f32 v1, v2, v3\n"
    "v_mfma_f32_16x16x32_bf16 v[28:31], a[172:175], v[164:167], v[28:31]\n"
    "ds_read_b64_tr_b16 a[172:173], v183 offset:256\n"
    "v_cvt_pk_bf16_f32 v2, v4, v5\n"
    "s_waitcnt lgkmcnt(13)\n"
    "v_mfma_f32_16x16x32_bf16 v[32:35], a[176:179], v[160:163], v[32:35]\n"
    "ds_read_b64_tr_b16 a[174:175], v183 offset:8448\n"
    "v_cvt_pk_bf16_f32 v3, v6, v7\n"
    "v_mfma_f32_16x16x32_bf16 v[36:39], a[176:179], v[164:167], v[36:39]\n"
    "ds_read_b64_tr_b16 a[176:177], v184 offset:256\n"
    "v_cvt_pk_bf16_f32 v8, v8, v9\n"
    "s_waitcnt lgkmcnt(13)\n"
    "v_mfma_f32_16x16x32_bf16 v[40:43], a[180:183], v[160:163], v[40:43]\n"
    "ds_read_b64_tr_b16 a[178:179], v184 offset:8448\n"
    "s_waitcnt vmcnt(9)\n"
    "ds_read_b128 v[128:131], v224 offset:0\n"
    "s_waitcnt lgkmcnt(11)\n"
    "ds_read_b128 v[132:135], v224 offset:4096\n"
    "ds_read_b128 v[136:139], v225 offset:0\n"
    "ds_read_b128 v[140:143], v225 offset:4096\n"
    "ds_read_b128 v[144:147], v226 offset:0\n"
    "s_waitcnt lgkmcnt(11)\n"
    "ds_read_b128 v[148:151], v226 offset:4096\n"
    "ds_read_b128 v[152:155], v227 offset:0\n"
    "ds_read_b128 v[156:159], v227 offset:4096\n"
    "v_cvt_pk_bf16_f32 v9, v10, v11\n"
    "v_mfma_f32_16x16x32_bf16 v[44:47], a[180:183], v[164:167], v[44:47]\n"
    "ds_read_b64_tr_b16 a[180:181], v185 offset:256\n"
    "v_cvt_pk_bf16_f32 v10, v12, v13\n"
    "v_mfma_f32_16x16x32_bf16 v[48:51], a[184:187], v[160:163], v[48:51]\n"
    "s_waitcnt lgkmcnt(11)\n"
    "ds_read_b64_tr_b16 a[182:183], v185 offset:8448\n"
    "v_cvt_pk_bf16_f32 v11, v14, v15\n"
    "v_mfma_f32_16x16x32_bf16 v[52:55], a[184:187], v[164:167], v[52:55]\n"
    "ds_read_b64_tr_b16 a[184:185], v186 offset:256\n"
    "v_cvt_pk_bf16_f32 v16, v16, v17\n"
    "v_mfma_f32_16x16x32_bf16 v[56:59], a[188:191], v[160:163], v[56:59]\n"
    "ds_read_b64_tr_b16 a[186:187], v186 offset:8448\n"
    "s_waitcnt vmcnt(9)\n"
    "s_barrier\n"
    "v_cvt_pk_bf16_f32 v17, v18, v19\n"
    "s_add_u32 m0, s58, 114688\n"
    "s_nop 0\n"
    "buffer_load_dwordx4 v192, s[40:43], s55 offen offset:0 lds\n"
    "v_mfma_f32_16x16x32_bf16 v[60:63], a[188:191], v[164:167], v[60:63]\n"
    "ds_read_b64_tr_b16 a[188:189], v187 offset:256\n"
    "buffer_load_dwordx4 v193, s[40:43], s55 offen offset:1024 lds\n"
    "v_cvt_pk_bf16_f32 v18, v20, v21\n"
    "v_mfma_f32_16x16x32_bf16 v[64:67], a[160:163], v[160:163], v[64:67]\n"
    "buffer_load_dwordx4 v194, s[40:43], s55 offen offset:2048 lds\n"
    "s_waitcnt lgkmcnt(11)\n"
    "ds_read_b64_tr_b16 a[190:191], v187 offset:8448\n"
    "s_waitcnt lgkmcnt(6)\n"
    "v_cvt_pk_bf16_f32 v19, v22, v23\n"
    "buffer_load_dwordx4 v195, s[40:43], s55 offen offset:3072 lds\n"
    "s_add_u32 s59, s59, 1\n"
    "v_readlane_b32 s55, v211, s59\n"
    "v_mfma_f32_16x16x32_bf16 v[68:71], a[160:163], v[164:167], v[68:71]\n"
    "v_cvt_pk_bf16_f32 v24, v24, v25\n"
    "s_add_u32 m0, s58, 0\n"
    "s_nop 0\n"
    "buffer_load_dwordx4 v188, s[36:39], s54 offen offset:0 lds\n"
    "v_cvt_pk_bf16_f32 v25, v26, v27\n"
    "v_mfma_f32_16x16x32_bf16 v[72:75], a[164:167], v[160:163], v[72:75]\n"
    "buffer_load_dwordx4 v189, s[36:39], s54 offen offset:1024 lds\n"
    "v_cvt_pk_bf16_f32 v26, v28, v29\n"
    "v_cvt_pk_bf16_f32 v27, v30, v31\n"
    "buffer_load_dwordx4 v190, s[36:39], s54 offen offset:2048 lds\n"
    "v_mfma_f32_16x16x32_bf16 v[76:79], a[164:167], v[164:167], v[76:79]\n"
    "v_cvt_pk_bf16_f32 v32, v32, v33\n"
    "buffer_load_dwordx4 v191, s[36:39], s54 offen offset:3072 lds\n"
    "v_cvt_pk_bf16_f32 v33, v34, v35\n"
    "v_mfma_f32_16x16x32_bf16 v[80:83], a[168:171], v[160:163], v[80:83]\n"
    "s_add_u32 m0, s58, 16384\n"
    "s_nop 0\n"
    "buffer_load_dwordx4 v188, s[36:39], s55 offen offset:0 lds\n"
    "v_cvt_pk_bf16_f32 v34, v36, v37\n"
    "v_cvt_pk_bf16_f32 v35, v38, v39\n"
    "buffer_load_dwordx4 v189, s[36:39], s55 offen offset:1024 lds\n"
    "v_mfma_f32_16x16x32_bf16 v[84:87], a[168:171], v[164:167], v[84:87]\n"
    "v_cvt_pk_bf16_f32 v40, v40, v41\n"
    "buffer_load_dwordx4 v190, s[36:39], s55 offen offset:2048 lds\n"
    "v_cvt_pk_bf16_f32 v41, v42, v43\n"
    "v_mfma_f32_16x16x32_bf16 v[88:91], a[172:175], v[160:163], v[88:91]\n"
    "buffer_load_dwordx4 v191, s[36:39], s55 offen offset:3072 lds\n"
    "v_cvt_pk_bf16_f32 v42, v44, v45\n"
    "v_cvt_pk_bf16_f32 v43, v46, v47\n"
    "s_add_u32 m0, s91, 0\n"
    "s_mov_b32 s75, 256\n"
    "buffer_load_dwordx4 v228, s[92:95], s75 offen offset:0 lds\n"
    "v_mfma_f32_16x16x32_bf16 v[92:95], a[172:175], v[164:167], v[92:95]\n"
    "v_cvt_pk_bf16_f32 v48, v48, v49\n"
    "buffer_load_dwordx4 v229, s[92:95], s75 offen offset:1024 lds\n"
    "v_cvt_pk_bf16_f32 v49, v50, v51\n"
    "v_mfma_f32_16x16x32_bf16 v[96:99], a[176:179], v[160:163], v[96:99]\n"
    "buffer_load_dwordx4 v230, s[92:95], s75 offen offset:2048 lds\n"
    "v_cvt_pk_bf16_f32 v50, v52, v53\n"
    "v_cvt_pk_bf16_f32 v51, v54, v55\n"
    "buffer_load_dwordx4 v231, s[92:95], s75 offen offset:3072 lds\n"
    "v_mfma_f32_16x16x32_bf16 v[100:103], a[176:179], v[164:167], v[100:103]\n"
    "v_cvt_pk_bf16_f32 v56, v56, v57\n"
    "s_add_u32 m0, s91, 4096\n"
    "s_mov_b32 s75, 8448\n"
    "buffer_load_dwordx4 v228, s[92:95], s75 offen offset:0 lds\n"
    "v_cvt_pk_bf16_f32 v57, v58, v59\n"
    "s_waitcnt lgkmcnt(4)\n"
    "v_mfma_f32_16x16x32_bf16 v[104:107], a[180:183], v[160:163], v[104:107]\n"
    "buffer_load_dwordx4 v229, s[92:95], s75 offen offset:1024 lds\n"
    "v_cvt_pk_bf16_f32 v58, v60, v61\n"
    "v_cvt_pk_bf16_f32 v59, v62, v63\n"
    "buffer_load_dwordx4 v230, s[92:95], s75 offen offset:2048 lds\n"
    "v_mfma_f32_16x16x32_bf16 v[108:111], a[180:183], v[164:167], v[108:111]\n"
    "v_cvt_pk_bf16_f32 v64, v64, v65\n"
    "buffer_load_dwordx4 v231, s[92:95], s75 offen offset:3072 lds\n"
    "v_cvt_pk_bf16_f32 v65, v66, v67\n"
    "s_waitcnt lgkmcnt(2)\n"
    "v_mfma_f32_16x16x32_bf16 v[112:115], a[184:187], v[160:163], v[112:115]\n"
    "v_cvt_pk_bf16_f32 v66, v68, v69\n"
    "v_cvt_pk_bf16_f32 v67, v70, v71\n"
    "v_mfma_f32_16x16x32_bf16 v[116:119], a[184:187], v[164:167], v[116:119]\n"
    "v_cvt_pk_bf16_f32 v72, v72, v73\n"
    "v_cvt_pk_bf16_f32 v73, v74, v75\n"
    "s_waitcnt lgkmcnt(0)\n"
    "v_mfma_f32_16x16x32_bf16 v[120:123], a[188:191], v[160:163], v[120:123]\n"
    "v_cvt_pk_bf16_f32 v74, v76, v77\n"
    "v_cvt_pk_bf16_f32 v75, v78, v79\n"
    "v_mfma_f32_16x16x32_bf16 v[124:127], a[188:191], v[164:167], v[124:127]\n"
    "v_cvt_pk_bf16_f32 v80, v80, v81\n"
    "v_cvt_pk_bf16_f32 v81, v82, v83\n"
    "s_nop 3\n"
    "v_cvt_pk_bf16_f32 v82, v84, v85\n"
    "v_cvt_pk_bf16_f32 v83, v86, v87\n"
    "global_store_dwordx4 v214, v[196:199], s[44:45] offset:0 sc1\n"
    "v_cvt_pk_bf16_f32 v88, v88, v89\n"
    "v_cvt_pk_bf16_f32 v89, v90, v91\n"
    "global_store_dwordx4 v232, v[0:3], s[50:51] offset:0 sc1\n"
    "v_cvt_pk_bf16_f32 v90, v92, v93\n"
    "v_cvt_pk_bf16_f32 v91, v94, v95\n"
    "global_store_dwordx4 v232, v[8:11], s[50:51] offset:2048 sc1\n"
    "v_cvt_pk_bf16_f32 v96, v96, v97\n"
    "v_cvt_pk_bf16_f32 v97, v98, v99\n"
    "global_store_dwordx4 v233, v[16:19], s[50:51] offset:0 sc1\n"
    "v_cvt_pk_bf16_f32 v98, v100, v101\n"
    "v_cvt_pk_bf16_f32 v99, v102, v103\n"
    "global_store_dwordx4 v233, v[24:27], s[50:51] offset:2048 sc1\n"
    "v_cvt_pk_bf16_f32 v104, v104, v105\n"
    "v_cvt_pk_bf16_f32 v105, v106, v107\n"
    "global_store_dwordx4 v234, v[32:35], s[50:51] offset:0 sc1\n"
    "v_cvt_pk_bf16_f32 v106, v108, v109\n"
    "v_cvt_pk_bf16_f32 v107, v110, v111\n"
    "global_store_dwordx4 v234, v[40:43], s[50:51] offset:2048 sc1\n"
    "v_cvt_pk_bf16_f32 v112, v112, v113\n"
    "v_cvt_pk_bf16_f32 v113, v114, v115\n"
    "global_store_dwordx4 v235, v[48:51], s[50:51] offset:0 sc1\n"
    "v_cvt_pk_bf16_f32 v114, v116, v117\n"
    "v_cvt_pk_bf16_f32 v115, v118, v119\n"
    "global_store_dwordx4 v235, v[56:59], s[50:51] offset:2048 sc1\n"
    "v_cvt_pk_bf16_f32 v120, v120, v121\n"
    "v_cvt_pk_bf16_f32 v121, v122, v123\n"
    "global_store_dwordx4 v232, v[64:67], s[50:51] offset:256 sc1\n"
    "v_cvt_pk_bf16_f32 v122, v124, v125\n"
    "v_cvt_pk_bf16_f32 v123, v126, v127\n"
    "global_store_dwordx4 v232, v[72:75], s[50:51] offset:2304 sc1\n"
    "v_mov_b32 v252, 0\n"
    "v_mov_b32 v253, 0\n"
    "v_mov_b32 v254, 0\n"
    "v_mov_b32 v255, 0\n"
    "s_nop 1\n"
    "global_store_dwordx4 v233, v[80:83], s[50:51] offset:256 sc1\n"
    "v_mfma_f32_32x32x16_bf16 v[0:15], v[252:255], v[252:255], 0\n"
    "global_store_dwordx4 v233, v[88:91], s[50:51] offset:2304 sc1\n"
    "v_mfma_f32_32x32x16_bf16 v[16:31], v[252:255], v[252:255], 0\n"
    "global_store_dwordx4 v234, v[96:99], s[50:51] offset:256 sc1\n"
    "v_mfma_f32_32x32x16_bf16 v[32:47], v[252:255], v[252:255], 0\n"
    "global_store_dwordx4 v234, v[104:107], s[50:51] offset:2304 sc1\n"
    "v_mfma_f32_32x32x16_bf16 v[48:63], v[252:255], v[252:255], 0\n"
    "global_store_dwordx4 v235, v[112:115], s[50:51] offset:256 sc1\n"
    "s_waitcnt vmcnt(37) lgkmcnt(0)\n"
    "s_barrier\n"
    "v_mfma_f32_32x32x16_bf16 v[64:79], v[252:255], v[252:255], 0\n"
    "s_add_u32 m0, s58, 65536\n"
    "s_nop 0\n"
    "buffer_load_dwordx4 v192, s[40:43], s54 offen offset:0 lds\n"
    "global_store_dwordx4 v235, v[120:123], s[50:51] offset:2304 sc1\n"
    "v_mfma_f32_32x32x16_bf16 v[80:95], v[252:255], v[252:255], 0\n"
    "buffer_load_dwordx4 v193, s[40:43], s54 offen offset:1024 lds\n"
    "s_nop 1\n"
    "v_mfma_f32_32x32x16_bf16 v[96:111], v[252:255], v[252:255], 0\n"
    "buffer_load_dwordx4 v194, s[40:43], s54 offen offset:2048 lds\n"
    "v_mfma_f32_32x32x16_bf16 v[112:127], v[252:255], v[252:255], 0\n"
    "v_lshlrev_b32 v240, 16, v128\n"
    "buffer_load_dwordx4 v195, s[40:43], s54 offen offset:3072 lds\n"
    "s_add_u32 s59, s59, 1\n"
    "v_readlane_b32 s54, v211, s59\n"
    "v_and_b32 v241, s64, v128\n"
    "v_lshlrev_b32 v242, 16, v129\n"
    "s_nop 3\n"
    "v_and_b32 v243, s64, v129\n"
    "v_pk_mul_f32 v[240:241], v[240:241], s[60:61] op_sel_hi:[1,0]\n"
    "s_add_u32 m0, s58, 32768\n"
    "s_nop 0\n"
    "buffer_load_dwordx4 v188, s[36:39], s54 offen offset:0 lds\n"
    "v_lshlrev_b32 v244, 16, v130\n"
    "v_and_b32 v245, s64, v130\n"
    "buffer_load_dwordx4 v189, s[36:39], s54 offen offset:1024 lds\n"
    "v_pk_mul_f32 v[242:243], v[242:243], s[60:61] op_sel_hi:[1,0]\n"
    "v_cvt_pk_bf16_f32 v128, v240, v241\n"
    "buffer_load_dwordx4 v190, s[36:39], s54 offen offset:2048 lds\n"
    "v_lshlrev_b32 v246, 16, v131\n"
    "v_and_b32 v247, s64, v131\n"
    "buffer_load_dwordx4 v191, s[36:39], s54 offen offset:3072 lds\n"
    "v_pk_mul_f32 v[244:245], v[244:245], s[60:61] op_sel_hi:[1,0]\n"
    "v_cvt_pk_bf16_f32 v129, v242, v243\n"
    "v_accvgpr_write_b32 a0, v128\n"
    "v_lshlrev_b32 v240, 16, v132\n"
    "v_and_b32 v241, s64, v132\n"
    "v_pk_mul_f32 v[246:247], v[246:247], s[60:61] op_sel_hi:[1,0]\n"
    "v_cvt_pk_bf16_f32 v130, v244, v245\n"
    "v_accvgpr_write_b32 a1, v129\n"
    "v_lshlrev_b32 v242, 16, v133\n"
    "v_and_b32 v243, s64, v133\n"
    "v_pk_mul_f32 v[240:241], v[240:241], s[60:61] op_sel_hi:[1,0]\n"
    "v_cvt_pk_bf16_f32 v131, v246, v247\n"
    "v_accvgpr_write_b32 a2, v130\n"
    "v_lshlrev_b32 v244, 16, v134\n"
    "v_and_b32 v245, s64, v134\n"
    "v_pk_mul_f32 v[242:243], v[242:243], s[60:61] op_sel_hi:[1,0]\n"
    "v_cvt_pk_bf16_f32 v132, v240, v241\n"
    "v_accvgpr_write_b32 a3, v131\n"
    "v_lshlrev_b32 v246, 16, v135\n"
    "v_and_b32 v247, s64, v135\n"
    "v_pk_mul_f32 v[244:245], v[244:245], s[60:61] op_sel_hi:[1,0]\n"
    "v_cvt_pk_bf16_f32 v133, v242, v243\n"
    "v_accvgpr_write_b32 a4, v132\n"
    "v_lshlrev_b32 v240, 16, v136\n"
    "v_and_b32 v241, s64, v136\n"
    "v_pk_mul_f32 v[246:247], v[246:247], s[60:61] op_sel_hi:[1,0]\n"
    "v_cvt_pk_bf16_f32 v134, v244, v245\n"
    "v_accvgpr_write_b32 a5, v133\n"
    "v_lshlrev_b32 v242, 16, v137\n"
    "v_and_b32 v243, s64, v137\n"
    "v_pk_mul_f32 v[240:241], v[240:241], s[60:61] op_sel_hi:[1,0]\n"
    "v_cvt_pk_bf16_f32 v135, v246, v247\n"
    "v_accvgpr_write_b32 a6, v134\n"
    "v_lshlrev_b32 v244, 16, v138\n"
    "v_and_b32 v245, s64, v138\n"
    "v_pk_mul_f32 v[242:243], v[242:243], s[60:61] op_sel_hi:[1,0]\n"
    "v_cvt_pk_bf16_f32 v136, v240, v241\n"
    "v_accvgpr_write_b32 a7, v135\n"
    "v_lshlrev_b32 v246, 16, v139\n"
    "v_and_b32 v247, s64, v139\n"
    "v_pk_mul_f32 v[244:245], v[244:245], s[60:61] op_sel_hi:[1,0]\n"
    "v_cvt_pk_bf16_f32 v137, v242, v243\n"
    "v_accvgpr_write_b32 a8, v136\n"
    "v_lshlrev_b32 v240, 16, v140\n"
    "v_and_b32 v241, s64, v140\n"
    "v_pk_mul_f32 v[246:247], v[246:247], s[60:61] op_sel_hi:[1,0]\n"
    "v_cvt_pk_bf16_f32 v138, v244, v245\n"
    "v_accvgpr_write_b32 a9, v137\n"
    "v_lshlrev_b32 v242, 16, v141\n"
    "v_and_b32 v243, s64, v141\n"
    "v_pk_mul_f32 v[240:241], v[240:241], s[60:61] op_sel_hi:[1,0]\n"
    "v_cvt_pk_bf16_f32 v139, v246, v247\n"
    "v_accvgpr_write_b32 a10, v138\n"
    "v_lshlrev_b32 v244, 16, v142\n"
    "v_and_b32 v245, s64, v142\n"
    "v_pk_mul_f32 v[242:243], v[242:243], s[60:61] op_sel_hi:[1,0]\n"
    "v_cvt_pk_bf16_f32 v140, v240, v241\n"
    "v_accvgpr_write_b32 a11, v139\n"
    "v_lshlrev_b32 v246, 16, v143\n"
    "v_and_b32 v247, s64, v143\n"
    "v_pk_mul_f32 v[244:245], v[244:245], s[60:61] op_sel_hi:[1,0]\n"
    "v_cvt_pk_bf16_f32 v141, v242, v243\n"
    "v_accvgpr_write_b32 a12, v140\n"
    "v_lshlrev_b32 v240, 16, v144\n"
    "v_and_b32 v241, s64, v144\n"
    "v_pk_mul_f32 v[246:247], v[246:247], s[60:61] op_sel_hi:[1,0]\n"
    "v_cvt_pk_bf16_f32 v142, v244, v245\n"
    "v_accvgpr_write_b32 a13, v141\n"
    "v_lshlrev_b32 v242, 16, v145\n"
    "v_and_b32 v243, s64, v145\n"
    "v_pk_mul_f32 v[240:241], v[240:241], s[60:61] op_sel_hi:[1,0]\n"
    "v_cvt_pk_bf16_f32 v143, v246, v247\n"
    "v_accvgpr_write_b32 a14, v142\n"
    "v_lshlrev_b32 v244, 16, v146\n"
    "v_and_b32 v245, s64, v146\n"
    "v_pk_mul_f32 v[242:243], v[242:243], s[60:61] op_sel_hi:[1,0]\n"
    "v_cvt_pk_bf16_f32 v144, v240, v241\n"
    "v_accvgpr_write_b32 a15, v143\n"
    "v_lshlrev_b32 v246, 16, v147\n"
    "v_and_b32 v247, s64, v147\n"
    "v_pk_mul_f32 v[244:245], v[244:245], s[60:61] op_sel_hi:[1,0]\n"
    "v_cvt_pk_bf16_f32 v145, v242, v243\n"
    "v_accvgpr_write_b32 a16, v144\n"
    "v_lshlrev_b32 v240, 16, v148\n"
    "v_and_b32 v241, s64, v148\n"
    "v_pk_mul_f32 v[246:247], v[246:247], s[60:61] op_sel_hi:[1,0]\n"
    "v_cvt_pk_bf16_f32 v146, v244, v245\n"
    "v_accvgpr_write_b32 a17, v145\n"
    "v_lshlrev_b32 v242, 16, v149\n"
    "v_and_b32 v243, s64, v149\n"
    "v_pk_mul_f32 v[240:241], v[240:241], s[60:61] op_sel_hi:[1,0]\n"
    "v_cvt_pk_bf16_f32 v147, v246, v247\n"
    "v_accvgpr_write_b32 a18, v146\n"
    "v_lshlrev_b32 v244, 16, v150\n"
    "v_and_b32 v245, s64, v150\n"
    "v_pk_mul_f32 v[242:243], v[242:243], s[60:61] op_sel_hi:[1,0]\n"
    "v_cvt_pk_bf16_f32 v148, v240, v241\n"
    "v_accvgpr_write_b32 a19, v147\n"
    "v_lshlrev_b32 v246, 16, v151\n"
    "v_and_b32 v247, s64, v151\n"
    "v_pk_mul_f32 v[244:245], v[244:245], s[60:61] op_sel_hi:[1,0]\n"
    "v_cvt_pk_bf16_f32 v149, v242, v243\n"
    "v_accvgpr_write_b32 a20, v148\n"
    "v_lshlrev_b32 v240, 16, v152\n"
    "v_and_b32 v241, s64, v152\n"
    "v_pk_mul_f32 v[246:247], v[246:247], s[60:61] op_sel_hi:[1,0]\n"
    "v_cvt_pk_bf16_f32 v150, v244, v245\n"
    "v_accvgpr_write_b32 a21, v149\n"
    "v_lshlrev_b32 v242, 16, v153\n"
    "v_and_b32 v243, s64, v153\n"
    "v_pk_mul_f32 v[240:241], v[240:241], s[60:61] op_sel_hi:[1,0]\n"
    "v_cvt_pk_bf16_f32 v151, v246, v247\n"
    "v_accvgpr_write_b32 a22, v150\n"
    "v_lshlrev_b32 v244, 16, v154\n"
    "v_and_b32 v245, s64, v154\n"
    "v_pk_mul_f32 v[242:243], v[242:243], s[60:61] op_sel_hi:[1,0]\n"
    "v_cvt_pk_bf16_f32 v152, v240, v241\n"
    "v_accvgpr_write_b32 a23, v151\n"
    "v_lshlrev_b32 v246, 16, v155\n"
    "v_and_b32 v247, s64, v155\n"
    "v_pk_mul_f32 v[244:245], v[244:245], s[60:61] op_sel_hi:[1,0]\n"
    "v_cvt_pk_bf16_f32 v153, v242, v243\n"
    "v_accvgpr_write_b32 a24, v152\n"
    "v_lshlrev_b32 v240, 16, v156\n"
    "v_and_b32 v241, s64, v156\n"
    "v_pk_mul_f32 v[246:247], v[246:247], s[60:61] op_sel_hi:[1,0]\n"
    "v_cvt_pk_bf16_f32 v154, v244, v245\n"
    "v_accvgpr_write_b32 a25, v153\n"
    "v_lshlrev_b32 v242, 16, v157\n"
    "v_and_b32 v243, s64, v157\n"
    "v_pk_mul_f32 v[240:241], v[240:241], s[60:61] op_sel_hi:[1,0]\n"
    "v_cvt_pk_bf16_f32 v155, v246, v247\n"
    "v_accvgpr_write_b32 a26, v154\n"
    "v_lshlrev_b32 v244, 16, v158\n"
    "v_and_b32 v245, s64, v158\n"
    "v_pk_mul_f32 v[242:243], v[242:243], s[60:61] op_sel_hi:[1,0]\n"
    "v_cvt_pk_bf16_f32 v156, v240, v241\n"
    "v_accvgpr_write_b32 a27, v155\n"
    "v_lshlrev_b32 v246, 16, v159\n"
    "v_and_b32 v247, s64, v159\n"
    "v_pk_mul_f32 v[244:245], v[244:245], s[60:61] op_sel_hi:[1,0]\n"
    "v_cvt_pk_bf16_f32 v157, v242, v243\n"
    "v_accvgpr_write_b32 a28, v156\n"
    "v_pk_mul_f32 v[246:247], v[246:247], s[60:61] op_sel_hi:[1,0]\n"
    "v_cvt_pk_bf16_f32 v158, v244, v245\n"
    "v_accvgpr_write_b32 a29, v157\n"
    "v_cvt_pk_bf16_f32 v159, v246, v247\n"
    "v_accvgpr_write_b32 a30, v158\n"
    "v_accvgpr_write_b32 a31, v159\n"
    "s_waitcnt vmcnt(25)\n"
    "ds_read_b128 v[128:131], v224 offset:0\n"
    "ds_read_b128 v[132:135], v224 offset:4096\n"
    "ds_read_b128 v[136:139], v225 offset:0\n"
    "ds_read_b128 v[140:143], v225 offset:4096\n"
    "ds_read_b128 v[144:147], v226 offset:0\n"
    "ds_read_b128 v[148:151], v226 offset:4096\n"
    "ds_read_b128 v[152:155], v227 offset:0\n"
    "ds_read_b128 v[156:159], v227 offset:4096\n"
    "s_waitcnt lgkmcnt(7)\n"
    "v_lshlrev_b32 v240, 16, v128\n"
    "v_and_b32 v241, s64, v128\n"
    "v_lshlrev_b32 v242, 16, v129\n"
    "v_and_b32 v243, s64, v129\n"
    "v_pk_mul_f32 v[240:241], v[240:241], s[60:61] op_sel_hi:[1,0]\n"
    "v_lshlrev_b32 v244, 16, v130\n"
    "v_and_b32 v245, s64, v130\n"
    "v_pk_mul_f32 v[242:243], v[242:243], s[60:61] op_sel_hi:[1,0]\n"
    "v_cvt_pk_bf16_f32 v128, v240, v241\n"
    "v_lshlrev_b32 v246, 16, v131\n"
    "v_and_b32 v247, s64, v131\n"
    "v_pk_mul_f32 v[244:245], v[244:245], s[60:61] op_sel_hi:[1,0]\n"
    "v_cvt_pk_bf16_f32 v129, v242, v243\n"
    "v_accvgpr_write_b32 a32, v128\n"
    "s_waitcnt lgkmcnt(6)\n"
    "v_lshlrev_b32 v240, 16, v132\n"
    "v_and_b32 v241, s64, v132\n"
    "v_pk_mul_f32 v[246:247], v[246:247], s[60:61] op_sel_hi:[1,0]\n"
    "v_cvt_pk_bf16_f32 v130, v244, v245\n"
    "v_accvgpr_write_b32 a33, v129\n"
    "v_lshlrev_b32 v242, 16, v133\n"
    "v_and_b32 v243, s64, v133\n"
    "v_pk_mul_f32 v[240:241], v[240:241], s[60:61] op_sel_hi:[1,0]\n"
    "v_cvt_pk_bf16_f32 v131, v246, v247\n"
    "v_accvgpr_write_b32 a34, v130\n"
    "s_waitcnt vmcnt(37) lgkmcnt(0)\n"
    "s_barrier\n"
    "v_lshlrev_b32 v244, 16, v134\n"
    "s_add_u32 m0, s58, 81920\n"
    "s_nop 0\n"
    "buffer_load_dwordx4 v192, s[40:43], s55 offen offset:0 lds\n"
    "v_and_b32 v245, s64, v134\n"
    "v_pk_mul_f32 v[242:243], v[242:243], s[60:61] op_sel_hi:[1,0]\n"
    "buffer_load_dwordx4 v193, s[40:43], s55 offen offset:1024 lds\n"
    "v_cvt_pk_bf16_f32 v132, v240, v241\n"
    "v_accvgpr_write_b32 a35, v131\n"
    "buffer_load_dwordx4 v194, s[40:43], s55 offen offset:2048 lds\n"
    "v_lshlrev_b32 v246, 16, v135\n"
    "v_and_b32 v247, s64, v135\n"
    "buffer_load_dwordx4 v195, s[40:43], s55 offen offset:3072 lds\n"
    "s_add_u32 s59, s59, 1\n"
    "v_readlane_b32 s55, v211, s59\n"
    "v_pk_mul_f32 v[244:245], v[244:245], s[60:61] op_sel_hi:[1,0]\n"
    "v_cvt_pk_bf16_f32 v133, v242, v243\n"
    "v_accvgpr_write_b32 a36, v132\n"
    "v_lshlrev_b32 v240, 16, v136\n"
    "v_and_b32 v241, s64, v136\n"
    "v_pk_mul_f32 v[246:247], v[246:247], s[60:61] op_sel_hi:[1,0]\n"
    "v_cvt_pk_bf16_f32 v134, v244, v245\n"
    "v_accvgpr_write_b32 a37, v133\n"
    "v_lshlrev_b32 v242, 16, v137\n"
    "v_and_b32 v243, s64, v137\n"
    "v_pk_mul_f32 v[240:241], v[240:241], s[60:61] op_sel_hi:[1,0]\n"
    "v_cvt_pk_bf16_f32 v135, v246, v247\n"
    "v_accvgpr_write_b32 a38, v134\n"
    "v_lshlrev_b32 v244, 16, v138\n"
    "v_and_b32 v245, s64, v138\n"
    "v_pk_mul_f32 v[242:243], v[242:243], s[60:61] op_sel_hi:[1,0]\n"
    "v_cvt_pk_bf16_f32 v136, v240, v241\n"
    "v_accvgpr_write_b32 a39, v135\n"
    "v_lshlrev_b32 v246, 16, v139\n"
    "v_and_b32 v247, s64, v139\n"
    "v_pk_mul_f32 v[244:245], v[244:245], s[60:61] op_sel_hi:[1,0]\n"
    "v_cvt_pk_bf16_f32 v137, v242, v243\n"
    "v_accvgpr_write_b32 a40, v136\n"
    "v_lshlrev_b32 v240, 16, v140\n"
    "v_and_b32 v241, s64, v140\n"
    "v_pk_mul_f32 v[246:247], v[246:247], s[60:61] op_sel_hi:[1,0]\n"
    "v_cvt_pk_bf16_f32 v138, v244, v245\n"
    "v_accvgpr_write_b32 a41, v137\n"
    "v_lshlrev_b32 v242, 16, v141\n"
    "v_and_b32 v243, s64, v141\n"
    "v_pk_mul_f32 v[240:241], v[240:241], s[60:61] op_sel_hi:[1,0]\n"
    "v_cvt_pk_bf16_f32 v139, v246, v247\n"
    "v_accvgpr_write_b32 a42, v138\n"
    "v_lshlrev_b32 v244, 16, v142\n"
    "v_and_b32 v245, s64, v142\n"
    "v_pk_mul_f32 v[242:243], v[242:243], s[60:61] op_sel_hi:[1,0]\n"
    "v_cvt_pk_bf16_f32 v140, v240, v241\n"
    "v_accvgpr_write_b32 a43, v139\n"
    "v_lshlrev_b32 v246, 16, v143\n"
    "v_and_b32 v247, s64, v143\n"
    "v_pk_mul_f32 v[244:245], v[244:245], s[60:61] op_sel_hi:[1,0]\n"
    "v_cvt_pk_bf16_f32 v141, v242, v243\n"
    "v_accvgpr_write_b32 a44, v140\n"
    "v_lshlrev_b32 v240, 16, v144\n"
    "v_and_b32 v241, s64, v144\n"
    "v_pk_mul_f32 v[246:247], v[246:247], s[60:61] op_sel_hi:[1,0]\n"
    "v_cvt_pk_bf16_f32 v142, v244, v245\n"
    "v_accvgpr_write_b32 a45, v141\n"
    "v_lshlrev_b32 v242, 16, v145\n"
    "v_and_b32 v243, s64, v145\n"
    "v_pk_mul_f32 v[240:241], v[240:241], s[60:61] op_sel_hi:[1,0]\n"
    "v_cvt_pk_bf16_f32 v143, v246, v247\n"
    "v_accvgpr_write_b32 a46, v142\n"
    "v_lshlrev_b32 v244, 16, v146\n"
    "v_and_b32 v245, s64, v146\n"
    "v_pk_mul_f32 v[242:243], v[242:243], s[60:61] op_sel_hi:[1,0]\n"
    "v_cvt_pk_bf16_f32 v144, v240, v241\n"
    "v_accvgpr_write_b32 a47, v143\n"
    "v_lshlrev_b32 v246, 16, v147\n"
    "v_and_b32 v247, s64, v147\n"
    "v_pk_mul_f32 v[244:245], v[244:245], s[60:61] op_sel_hi:[1,0]\n"
    "v_cvt_pk_bf16_f32 v145, v242, v243\n"
    "v_accvgpr_write_b32 a48, v144\n"
    "v_lshlrev_b32 v240, 16, v148\n"
    "v_and_b32 v241, s64, v148\n"
    "v_pk_mul_f32 v[246:247], v[246:247], s[60:61] op_sel_hi:[1,0]\n"
    "v_cvt_pk_bf16_f32 v146, v244, v245\n"
    "v_accvgpr_write_b32 a49, v145\n"
    "v_lshlrev_b32 v242, 16, v149\n"
    "v_and_b32 v243, s64, v149\n"
    "v_pk_mul_f32 v[240:241], v[240:241], s[60:61] op_sel_hi:[1,0]\n"
    "v_cvt_pk_bf16_f32 v147, v246, v247\n"
    "v_accvgpr_write_b32 a50, v146\n"
    "v_lshlrev_b32 v244, 16, v150\n"
    "v_and_b32 v245, s64, v150\n"
    "v_pk_mul_f32 v[242:243], v[242:243], s[60:61] op_sel_hi:[1,0]\n"
    "v_cvt_pk_bf16_f32 v148, v240, v241\n"
    "v_accvgpr_write_b32 a51, v147\n"
    "v_lshlrev_b32 v246, 16, v151\n"
    "v_and_b32 v247, s64, v151\n"
    "v_pk_mul_f32 v[244:245], v[244:245], s[60:61] op_sel_hi:[1,0]\n"
    "v_cvt_pk_bf16_f32 v149, v242, v243\n"
    "v_accvgpr_write_b32 a52, v148\n"
    "v_lshlrev_b32 v240, 16, v152\n"
    "v_and_b32 v241, s64, v152\n"
    "v_pk_mul_f32 v[246:247], v[246:247], s[60:61] op_sel_hi:[1,0]\n"
    "v_cvt_pk_bf16_f32 v150, v244, v245\n"
    "v_accvgpr_write_b32 a53, v149\n"
    "v_lshlrev_b32 v242, 16, v153\n"
    "v_and_b32 v243, s64, v153\n"
    "v_pk_mul_f32 v[240:241], v[240:241], s[60:61] op_sel_hi:[1,0]\n"
    "v_cvt_pk_bf16_f32 v151, v246, v247\n"
    "v_accvgpr_write_b32 a54, v150\n"
    "v_lshlrev_b32 v244, 16, v154\n"
    "v_and_b32 v245, s64, v154\n"
    "v_pk_mul_f32 v[242:243], v[242:243], s[60:61] op_sel_hi:[1,0]\n"
    "v_cvt_pk_bf16_f32 v152, v240, v241\n"
    "v_accvgpr_write_b32 a55, v151\n"
    "v_lshlrev_b32 v246, 16, v155\n"
    "v_and_b32 v247, s64, v155\n"
    "v_pk_mul_f32 v[244:245], v[244:245], s[60:61] op_sel_hi:[1,0]\n"
    "v_cvt_pk_bf16_f32 v153, v242, v243\n"
    "v_accvgpr_write_b32 a56, v152\n"
    "v_lshlrev_b32 v240, 16, v156\n"
    "v_and_b32 v241, s64, v156\n"
    "v_pk_mul_f32 v[246:247], v[246:247], s[60:61] op_sel_hi:[1,0]\n"
    "v_cvt_pk_bf16_f32 v154, v244, v245\n"
    "v_accvgpr_write_b32 a57, v153\n"
    "v_lshlrev_b32 v242, 16, v157\n"
    "v_and_b32 v243, s64, v157\n"
    "v_pk_mul_f32 v[240:241], v[240:241], s[60:61] op_sel_hi:[1,0]\n"
    "v_cvt_pk_bf16_f32 v155, v246, v247\n"
    "v_accvgpr_write_b32 a58, v154\n"
    "v_lshlrev_b32 v244, 16, v158\n"
    "v_and_b32 v245, s64, v158\n"
    "v_pk_mul_f32 v[242:243], v[242:243], s[60:61] op_sel_hi:[1,0]\n"
    "v_cvt_pk_bf16_f32 v156, v240, v241\n"
    "v_accvgpr_write_b32 a59, v155\n"
    "v_lshlrev_b32 v246, 16, v159\n"
    "v_and_b32 v247, s64, v159\n"
    "v_pk_mul_f32 v[244:245], v[244:245], s[60:61] op_sel_hi:[1,0]\n"
    "v_cvt_pk_bf16_f32 v157, v242, v243\n"
    "v_accvgpr_write_b32 a60, v156\n"
    "v_pk_mul_f32 v[246:247], v[246:247], s[60:61] op_sel_hi:[1,0]\n"
    "v_cvt_pk_bf16_f32 v158, v244, v245\n"
    "v_accvgpr_write_b32 a61, v157\n"
    "v_cvt_pk_bf16_f32 v159, v246, v247\n"
    "v_accvgpr_write_b32 a62, v158\n"
    "v_accvgpr_write_b32 a63, v159\n"
    "v_mov_b32 v244, 0xc1500000\n"
    "v_mov_b32 v245, 0xc1500000\n"
    "v_mov_b32 v246, 0xc1500000\n"
    "v_mov_b32 v247, 0xc1500000\n"
    "v_cndmask_b32 v236, v244, v248, s[80:81]\n"
    "v_cndmask_b32 v237, v244, v248, s[82:83]\n"
    "v_cndmask_b32 v238, v244, v248, s[84:85]\n"
    "v_cndmask_b32 v239, v244, v248, s[86:87]\n"
    "s_mov_b32 s35, 1\n"
    "s_sub_u32 s65, 0, s70\n"
    "s_waitcnt lgkmcnt(0)\n"
    "ds_read_b128 a[128:131], v176 offset:0\n"
    "ds_read_b128 a[132:135], v177 offset:0\n"
    "ds_read_b128 a[136:139], v178 offset:0\n"
    "ds_read_b128 a[140:143], v179 offset:0\n"
    "ds_read_b128 a[144:147], v176 offset:256\n"
    "ds_read_b128 a[148:151], v177 offset:256\n"
    "ds_read_b128 a[152:155], v178 offset:256\n"
    "ds_read_b128 a[156:159], v179 offset:256\n"
    "s_waitcnt lgkmcnt(6)\n"
    "v_mfma_f32_16x16x32_bf16 v[128:131], a[128:131], a[0:3], v[236:239]\n"
    "v_mov_b32 v252, 0xc1500000\n"
    "v_mov_b32 v253, 0xc1500000\n"
    "v_mfma_f32_16x16x32_bf16 v[132:135], a[128:131], a[4:7], v[244:247]\n"
    "ds_read_b128 a[128:131], v176 offset:8192\n"
    "v_mov_b32 v254, 0xc1500000\n"
    "v_mfma_f32_16x16x32_bf16 v[128:131], a[132:135], a[8:11], v[128:131]\n"
    "v_mov_b32 v255, 0xc1500000\n"
    "v_mov_b32 v196, 0x41500000\n"
    "v_mfma_f32_16x16x32_bf16 v[132:135], a[132:135], a[12:15], v[132:135]\n"
    "s_waitcnt lgkmcnt(6)\n"
    "ds_read_b128 a[132:135], v177 offset:8192\n"
    "v_mov_b32 v197, 0x41500000\n"
    "v_mfma_f32_16x16x32_bf16 v[128:131], a[136:139], a[16:19], v[128:131]\n"
    "v_mov_b32 v198, 0\n"
    "v_mov_b32 v199, 0\n"
    "v_mfma_f32_16x16x32_bf16 v[132:135], a[136:139], a[20:23], v[132:135]\n"
    "s_waitcnt lgkmcnt(6)\n"
    "ds_read_b128 a[136:139], v178 offset:8192\n"
    "v_mfma_f32_16x16x32_bf16 v[128:131], a[140:143], a[24:27], v[128:131]\n"
    "v_mfma_f32_16x16x32_bf16 v[132:135], a[140:143], a[28:31], v[132:135]\n"
    "s_waitcnt lgkmcnt(6)\n"
    "ds_read_b128 a[140:143], v179 offset:8192\n"
    "v_mfma_f32_16x16x32_bf16 v[128:131], a[144:147], a[32:35], v[128:131]\n"
    "v_mfma_f32_16x16x32_bf16 v[132:135], a[144:147], a[36:39], v[132:135]\n"
    "s_waitcnt lgkmcnt(6)\n"
    "ds_read_b128 a[144:147], v176 offset:8448\n"
    "v_mfma_f32_16x16x32_bf16 v[128:131], a[148:151], a[40:43], v[128:131]\n"
    "v_mfma_f32_16x16x32_bf16 v[132:135], a[148:151], a[44:47], v[132:135]\n"
    "s_waitcnt lgkmcnt(6)\n"
    "ds_read_b128 a[148:151], v177 offset:8448\n"
    "v_mfma_f32_16x16x32_bf16 v[128:131], a[152:155], a[48:51], v[128:131]\n"
    "v_mfma_f32_16x16x32_bf16 v[132:135], a[152:155], a[52:55], v[132:135]\n"
    "s_waitcnt lgkmcnt(6)\n"
    "ds_read_b128 a[152:155], v178 offset:8448\n"
    "v_mfma_f32_16x16x32_bf16 v[128:131], a[156:159], a[56:59], v[128:131]\n"
    "v_mfma_f32_16x16x32_bf16 v[132:135], a[156:159], a[60:63], v[132:135]\n"
    "s_waitcnt lgkmcnt(6)\n"
    "ds_read_b128 a[156:159], v179 offset:8448\n"
    "v_mfma_f32_16x16x32_bf16 v[136:139], a[128:131], a[0:3], v[248:251]\n"
    "v_mfma_f32_16x16x32_bf16 v[140:143], a[128:131], a[4:7], v[236:239]\n"
    "s_waitcnt lgkmcnt(6)\n"
    "s_add_u32 m0, s58, 49152\n"
    "s_nop 0\n"
    "buffer_load_dwordx4 v188, s[36:39], s55 offen offset:0 lds\n"
    "v_mfma_f32_16x16x32_bf16 v[136:139], a[132:135], a[8:11], v[136:139]\n"
    "v_mfma_f32_16x16x32_bf16 v[140:143], a[132:135], a[12:15], v[140:143]\n"
    "s_waitcnt lgkmcnt(5)\n"
    "buffer_load_dwordx4 v189, s[36:39], s55 offen offset:1024 lds\n"
    "v_mfma_f32_16x16x32_bf16 v[136:139], a[136:139], a[16:19], v[136:139]\n"
    "v_mov_b32 v236, 0xc1500000\n"
    "v_mov_b32 v237, 0xc1500000\n"
    "v_mfma_f32_16x16x32_bf16 v[140:143], a[136:139], a[20:23], v[140:143]\n"
    "s_waitcnt lgkmcnt(4)\n"
    "buffer_load_dwordx4 v190, s[36:39], s55 offen offset:2048 lds\n"
    "v_mfma_f32_16x16x32_bf16 v[136:139], a[140:143], a[24:27], v[136:139]\n"
    "v_mov_b32 v238, 0xc1500000\n"
    "v_mov_b32 v239, 0xc1500000\n"
    "v_mfma_f32_16x16x32_bf16 v[140:143], a[140:143], a[28:31], v[140:143]\n"
    "s_waitcnt lgkmcnt(3)\n"
    "buffer_load_dwordx4 v191, s[36:39], s55 offen offset:3072 lds\n"
    "v_mfma_f32_16x16x32_bf16 v[136:139], a[144:147], a[32:35], v[136:139]\n"
    "v_mov_b32 v248, 0xc1500000\n"
    "v_mov_b32 v249, 0xc1500000\n"
    "v_mfma_f32_16x16x32_bf16 v[140:143], a[144:147], a[36:39], v[140:143]\n"
    "s_waitcnt lgkmcnt(2)\n"
    "v_mov_b32 v250, 0xc1500000\n"
    "v_mov_b32 v251, 0xc1500000\n"
    "v_mfma_f32_16x16x32_bf16 v[136:139], a[148:151], a[40:43], v[136:139]\n"
    "v_min_f32 v204, v128, v132\n"
    "v_cmp_gt_f32 vcc, 0xc2540000, v204\n"
    "v_mfma_f32_16x16x32_bf16 v[140:143], a[148:151], a[44:47], v[140:143]\n"
    "s_waitcnt lgkmcnt(1)\n"
    "v_exp_f32 v240, v128\n"
    "v_mfma_f32_16x16x32_bf16 v[136:139], a[152:155], a[48:51], v[136:139]\n"
    "v_exp_f32 v241, v129\n"
    "v_mfma_f32_16x16x32_bf16 v[140:143], a[152:155], a[52:55], v[140:143]\n"
    "s_waitcnt lgkmcnt(0)\n"
    "v_exp_f32 v242, v130\n"
    "v_mfma_f32_16x16x32_bf16 v[136:139], a[156:159], a[56:59], v[136:139]\n"
    "v_add_f32 v216, v240, v241\n"
    "v_cvt_pk_bf16_f32 v160, v240, v241\n"
    "v_mfma_f32_16x16x32_bf16 v[140:143], a[156:159], a[60:63], v[140:143]\n"
    "v_exp_f32 v243, v131\n"
    "s_waitcnt vmcnt(12)\n"
    "s_barrier\n"
    "global_store_dword v213, v212, s[52:53] offset:64 sc1\n"
    "s_add_u32 m0, s58, 98304\n"
    "s_nop 0\n"
    "buffer_load_dwordx4 v192, s[40:43], s54 offen offset:0 lds\n"
    "ds_read_b128 a[128:131], v176 offset:16384\n"
    "ds_read_b128 a[132:135], v177 offset:16384\n"
    "s_and_b32 s72, vcc_lo, 0xffff\n"
    "s_cbranch_scc1 .Llow_18\n"
    ".Llowb_19:\n"
    "v_add_f32 v216, v216, v242\n"
    "ds_read_b128 a[136:139], v178 offset:16384\n"
    "v_exp_f32 v240, v132\n"
    "buffer_load_dwordx4 v193, s[40:43], s54 offen offset:1024 lds\n"
    "v_add_f32 v216, v216, v243\n"
    "ds_read_b128 a[140:143], v179 offset:16384\n"
    "v_cvt_pk_bf16_f32 v161, v242, v243\n"
    "buffer_load_dwordx4 v194, s[40:43], s54 offen offset:2048 lds\n"
    "v_exp_f32 v241, v133\n"
    "ds_read_b128 a[144:147], v176 offset:16640\n"
    "v_exp_f32 v242, v134\n"
    "buffer_load_dwordx4 v195, s[40:43], s54 offen offset:3072 lds\n"
    "s_add_u32 s59, s59, 1\n"
    "v_readlane_b32 s54, v211, s59\n"
    "v_add_f32 v217, v240, v241\n"
    "ds_read_b128 a[148:151], v177 offset:16640\n"
    "v_cvt_pk_bf16_f32 v164, v240, v241\n"
    "ds_read_b128 a[152:155], v178 offset:16640\n"
    "v_exp_f32 v243, v135\n"
    "ds_read_b128 a[156:159], v179 offset:16640\n"
    "v_add_f32 v217, v217, v242\n"
    "v_exp_f32 v240, v136\n"
    "v_add_f32 v217, v217, v243\n"
    "v_cvt_pk_bf16_f32 v165, v242, v243\n"
    "v_exp_f32 v241, v137\n"
    "v_add_f32 v216, v216, v240\n"
    "v_exp_f32 v242, v138\n"
    "v_add_f32 v216, v216, v241\n"
    "v_cvt_pk_bf16_f32 v162, v240, v241\n"
    "s_waitcnt lgkmcnt(6)\n"
    "s_add_u32 s65, s65, 1\n"
    "s_cbranch_scc1 .LX0\n"
    "s_branch .LG1\n"
    ".LX1:\n"
    "s_cmp_lg_u32 s63, 0\n"
    "s_cbranch_scc1 .Lev_20\n"
    "s_cmp_lg_u32 s35, 0\n"
    "s_cbranch_scc1 .Lev_21\n"
    "global_load_dword v212, v213, s[52:53] offset:0 sc1\n"
    "s_waitcnt lgkmcnt(0)\n"
    "ds_read_b64_tr_b16 a[160:161], v180 offset:16384\n"
    "ds_read_b64_tr_b16 a[162:163], v180 offset:24576\n"
    "ds_read_b64_tr_b16 a[164:165], v181 offset:16384\n"
    "ds_read_b64_tr_b16 a[166:167], v181 offset:24576\n"
    "ds_read_b64_tr_b16 a[168:169], v182 offset:16384\n"
    "ds_read_b64_tr_b16 a[170:171], v182 offset:24576\n"
    "ds_read_b64_tr_b16 a[172:173], v183 offset:16384\n"
    "ds_read_b64_tr_b16 a[174:175], v183 offset:24576\n"
    "ds_read_b64_tr_b16 a[176:177], v184 offset:16384\n"
    "ds_read_b64_tr_b16 a[178:179], v184 offset:24576\n"
    "ds_read_b64_tr_b16 a[180:181], v185 offset:16384\n"
    "ds_read_b64_tr_b16 a[182:183], v185 offset:24576\n"
    "ds_read_b64_tr_b16 a[184:185], v186 offset:16384\n"
    "ds_read_b64_tr_b16 a[186:187], v186 offset:24576\n"
    "v_exp_f32 v243, v155\n"
    "v_add_f32 v216, v216, v242\n"
    "v_exp_f32 v240, v156\n"
    "v_add_f32 v216, v216, v243\n"
    "v_cvt_pk_bf16_f32 v171, v242, v243\n"
    "v_exp_f32 v241, v157\n"
    "v_add_f32 v217, v217, v240\n"
    "v_exp_f32 v242, v158\n"
    "v_add_f32 v217, v217, v241\n"
    "v_exp_f32 v243, v159\n"
    "v_add_f32 v217, v217, v242\n"
    "v_cvt_pk_bf16_f32 v174, v240, v241\n"
    "v_add_f32 v217, v217, v243\n"
    "v_add_f32 v215, v216, v217\n"
    "v_cmp_lt_f32 vcc, s29, v215\n"
    "v_cvt_pk_bf16_f32 v175, v242, v243\n"
    "s_cbranch_vccnz .Lstub_22\n"
    ".Lstubb_23:\n"
    "v_add_f32 v198, v198, v216\n"
    "v_add_f32 v199, v199, v217\n"
    "ds_read_b64_tr_b16 a[188:189], v187 offset:16384\n"
    "s_waitcnt lgkmcnt(11)\n"
    "ds_read_b64_tr_b16 a[190:191], v187 offset:24576\n"
    "v_mfma_f32_16x16x32_bf16 v[0:3], a[160:163], v[168:171], v[0:3]\n"
    "v_mfma_f32_16x16x32_bf16 v[4:7], a[160:163], v[172:175], v[4:7]\n"
    "ds_read_b64_tr_b16 a[160:161], v180 offset:16640\n"
    "v_mfma_f32_16x16x32_bf16 v[8:11], a[164:167], v[168:171], v[8:11]\n"
    "ds_read_b64_tr_b16 a[162:163], v180 offset:24832\n"
    "v_mfma_f32_16x16x32_bf16 v[12:15], a[164:167], v[172:175], v[12:15]\n"
    "ds_read_b64_tr_b16 a[164:165], v181 offset:16640\n"
    "s_waitcnt lgkmcnt(13)\n"
    "v_mfma_f32_16x16x32_bf16 v[16:19], a[168:171], v[168:171], v[16:19]\n"
    "ds_read_b64_tr_b16 a[166:167], v181 offset:24832\n"
    "v_mfma_f32_16x16x32_bf16 v[20:23], a[168:171], v[172:175], v[20:23]\n"
    "ds_read_b64_tr_b16 a[168:169], v182 offset:16640\n"
    "v_cvt_pk_bf16_f32 v0, v0, v1\n"
    "s_waitcnt lgkmcnt(13)\n"
    "v_mfma_f32_16x16x32_bf16 v[24:27], a[172:175], v[168:171], v[24:27]\n"
    "ds_read_b64_tr_b16 a[170:171], v182 offset:24832\n"
    "v_cvt_pk_bf16_f32 v1, v2, v3\n"
    "v_mfma_f32_16x16x32_bf16 v[28:31], a[172:175], v[172:175], v[28:31]\n"
    "ds_read_b64_tr_b16 a[172:173], v183 offset:16640\n"
    "v_cvt_pk_bf16_f32 v2, v4, v5\n"
    "s_waitcnt lgkmcnt(13)\n"
    "v_mfma_f32_16x16x32_bf16 v[32:35], a[176:179], v[168:171], v[32:35]\n"
    "ds_read_b64_tr_b16 a[174:175], v183 offset:24832\n"
    "v_cvt_pk_bf16_f32 v3, v6, v7\n"
    "v_mfma_f32_16x16x32_bf16 v[36:39], a[176:179], v[172:175], v[36:39]\n"
    "ds_read_b64_tr_b16 a[176:177], v184 offset:16640\n"
    "v_cvt_pk_bf16_f32 v8, v8, v9\n"
    "s_waitcnt lgkmcnt(13)\n"
    "v_mfma_f32_16x16x32_bf16 v[40:43], a[180:183], v[168:171], v[40:43]\n"
    "ds_read_b64_tr_b16 a[178:179], v184 offset:24832\n"
    "s_waitcnt vmcnt(9)\n"
    "ds_read_b128 v[128:131], v224 offset:0\n"
    "s_waitcnt lgkmcnt(11)\n"
    "ds_read_b128 v[132:135], v224 offset:4096\n"
    "ds_read_b128 v[136:139], v225 offset:0\n"
    "ds_read_b128 v[140:143], v225 offset:4096\n"
    "ds_read_b128 v[144:147], v226 offset:0\n"
    "s_waitcnt lgkmcnt(11)\n"
    "ds_read_b128 v[148:151], v226 offset:4096\n"
    "ds_read_b128 v[152:155], v227 offset:0\n"
    "ds_read_b128 v[156:159], v227 offset:4096\n"
    "v_cvt_pk_bf16_f32 v9, v10, v11\n"
    "v_mfma_f32_16x16x32_bf16 v[44:47], a[180:183], v[172:175], v[44:47]\n"
    "ds_read_b64_tr_b16 a[180:181], v185 offset:16640\n"
    "v_cvt_pk_bf16_f32 v10, v12, v13\n"
    "v_mfma_f32_16x16x32_bf16 v[48:51], a[184:187], v[168:171], v[48:51]\n"
    "s_waitcnt lgkmcnt(11)\n"
    "ds_read_b64_tr_b16 a[182:183], v185 offset:24832\n"
    "v_cvt_pk_bf16_f32 v11, v14, v15\n"
    "v_mfma_f32_16x16x32_bf16 v[52:55], a[184:187], v[172:175], v[52:55]\n"
    "ds_read_b64_tr_b16 a[184:185], v186 offset:16640\n"
    "v_cvt_pk_bf16_f32 v16, v16, v17\n"
    "v_mfma_f32_16x16x32_bf16 v[56:59], a[188:191], v[168:171], v[56:59]\n"
    "ds_read_b64_tr_b16 a[186:187], v186 offset:24832\n"
    "s_waitcnt vmcnt(9)\n"
    "s_barrier\n"
    "v_cvt_pk_bf16_f32 v17, v18, v19\n"
    "s_add_u32 m0, s58, 65536\n"
    "s_nop 0\n"
    "buffer_load_dwordx4 v192, s[40:43], s54 offen offset:0 lds\n"
    "v_mfma_f32_16x16x32_bf16 v[60:63], a[188:191], v[172:175], v[60:63]\n"
    "ds_read_b64_tr_b16 a[188:189], v187 offset:16640\n"
    "buffer_load_dwordx4 v193, s[40:43], s54 offen offset:1024 lds\n"
    "v_cvt_pk_bf16_f32 v18, v20, v21\n"
    "v_mfma_f32_16x16x32_bf16 v[64:67], a[160:163], v[168:171], v[64:67]\n"
    "buffer_load_dwordx4 v194, s[40:43], s54 offen offset:2048 lds\n"
    "s_waitcnt lgkmcnt(11)\n"
    "ds_read_b64_tr_b16 a[190:191], v187 offset:24832\n"
    "s_waitcnt lgkmcnt(6)\n"
    "v_cvt_pk_bf16_f32 v19, v22, v23\n"
    "buffer_load_dwordx4 v195, s[40:43], s54 offen offset:3072 lds\n"
    "s_add_u32 s59, s59, 1\n"
    "v_readlane_b32 s54, v211, s59\n"
    "v_mfma_f32_16x16x32_bf16 v[68:71], a[160:163], v[172:175], v[68:71]\n"
    "v_cvt_pk_bf16_f32 v24, v24, v25\n"
    "s_add_u32 m0, s58, 16384\n"
    "s_nop 0\n"
    "buffer_load_dwordx4 v188, s[36:39], s55 offen offset:0 lds\n"
    "v_cvt_pk_bf16_f32 v25, v26, v27\n"
    "v_mfma_f32_16x16x32_bf16 v[72:75], a[164:167], v[168:171], v[72:75]\n"
    "buffer_load_dwordx4 v189, s[36:39], s55 offen offset:1024 lds\n"
    "v_cvt_pk_bf16_f32 v26, v28, v29\n"
    "v_cvt_pk_bf16_f32 v27, v30, v31\n"
    "buffer_load_dwordx4 v190, s[36:39], s55 offen offset:2048 lds\n"
    "v_mfma_f32_16x16x32_bf16 v[76:79], a[164:167], v[172:175], v[76:79]\n"
    "v_cvt_pk_bf16_f32 v32, v32, v33\n"
    "buffer_load_dwordx4 v191, s[36:39], s55 offen offset:3072 lds\n"
    "v_cvt_pk_bf16_f32 v33, v34, v35\n"
    "v_mfma_f32_16x16x32_bf16 v[80:83], a[168:171], v[168:171], v[80:83]\n"
    "s_add_u32 m0, s58, 32768\n"
    "s_nop 0\n"
    "buffer_load_dwordx4 v188, s[36:39], s54 offen offset:0 lds\n"
    "v_cvt_pk_bf16_f32 v34, v36, v37\n"
    "v_cvt_pk_bf16_f32 v35, v38, v39\n"
    "buffer_load_dwordx4 v189, s[36:39], s54 offen offset:1024 lds\n"
    "v_mfma_f32_16x16x32_bf16 v[84:87], a[168:171], v[172:175], v[84:87]\n"
    "v_cvt_pk_bf16_f32 v40, v40, v41\n"
    "buffer_load_dwordx4 v190, s[36:39], s54 offen offset:2048 lds\n"
    "v_cvt_pk_bf16_f32 v41, v42, v43\n"
    "v_mfma_f32_16x16x32_bf16 v[88:91], a[172:175], v[168:171], v[88:91]\n"
    "buffer_load_dwordx4 v191, s[36:39], s54 offen offset:3072 lds\n"
    "v_cvt_pk_bf16_f32 v42, v44, v45\n"
    "v_cvt_pk_bf16_f32 v43, v46, v47\n"
    "s_add_u32 m0, s91, 0\n"
    "s_mov_b32 s75, 256\n"
    "buffer_load_dwordx4 v228, s[92:95], s75 offen offset:0 lds\n"
    "v_mfma_f32_16x16x32_bf16 v[92:95], a[172:175], v[172:175], v[92:95]\n"
    "v_cvt_pk_bf16_f32 v48, v48, v49\n"
    "buffer_load_dwordx4 v229, s[92:95], s75 offen offset:1024 lds\n"
    "v_cvt_pk_bf16_f32 v49, v50, v51\n"
    "v_mfma_f32_16x16x32_bf16 v[96:99], a[176:179], v[168:171], v[96:99]\n"
    "buffer_load_dwordx4 v230, s[92:95], s75 offen offset:2048 lds\n"
    "v_cvt_pk_bf16_f32 v50, v52, v53\n"
    "v_cvt_pk_bf16_f32 v51, v54, v55\n"
    "buffer_load_dwordx4 v231, s[92:95], s75 offen offset:3072 lds\n"
    "v_mfma_f32_16x16x32_bf16 v[100:103], a[176:179], v[172:175], v[100:103]\n"
    "v_cvt_pk_bf16_f32 v56, v56, v57\n"
    "s_add_u32 m0, s91, 4096\n"
    "s_mov_b32 s75, 8448\n"
    "buffer_load_dwordx4 v228, s[92:95], s75 offen offset:0 lds\n"
    "v_cvt_pk_bf16_f32 v57, v58, v59\n"
    "s_waitcnt lgkmcnt(4)\n"
    "v_mfma_f32_16x16x32_bf16 v[104:107], a[180:183], v[168:171], v[104:107]\n"
    "buffer_load_dwordx4 v229, s[92:95], s75 offen offset:1024 lds\n"
    "v_cvt_pk_bf16_f32 v58, v60, v61\n"
    "v_cvt_pk_bf16_f32 v59, v62, v63\n"
    "buffer_load_dwordx4 v230, s[92:95], s75 offen offset:2048 lds\n"
    "v_mfma_f32_16x16x32_bf16 v[108:111], a[180:183], v[172:175], v[108:111]\n"
    "v_cvt_pk_bf16_f32 v64, v64, v65\n"
    "buffer_load_dwordx4 v231, s[92:95], s75 offen offset:3072 lds\n"
    "v_cvt_pk_bf16_f32 v65, v66, v67\n"
    "s_waitcnt lgkmcnt(2)\n"
    "v_mfma_f32_16x16x32_bf16 v[112:115], a[184:187], v[168:171], v[112:115]\n"
    "v_cvt_pk_bf16_f32 v66, v68, v69\n"
    "v_cvt_pk_bf16_f32 v67, v70, v71\n"
    "v_mfma_f32_16x16x32_bf16 v[116:119], a[184:187], v[172:175], v[116:119]\n"
    "v_cvt_pk_bf16_f32 v72, v72, v73\n"
    "v_cvt_pk_bf16_f32 v73, v74, v75\n"
    "s_waitcnt lgkmcnt(0)\n"
    "v_mfma_f32_16x16x32_bf16 v[120:123], a[188:191], v[168:171], v[120:123]\n"
    "v_cvt_pk_bf16_f32 v74, v76, v77\n"
    "v_cvt_pk_bf16_f32 v75, v78, v79\n"
    "v_mfma_f32_16x16x32_bf16 v[124:127], a[188:191], v[172:175], v[124:127]\n"
    "v_cvt_pk_bf16_f32 v80, v80, v81\n"
    "v_cvt_pk_bf16_f32 v81, v82, v83\n"
    "s_nop 3\n"
    "v_cvt_pk_bf16_f32 v82, v84, v85\n"
    "v_cvt_pk_bf16_f32 v83, v86, v87\n"
    "global_store_dwordx4 v214, v[196:199], s[44:45] offset:0 sc1\n"
    "v_cvt_pk_bf16_f32 v88, v88, v89\n"
    "v_cvt_pk_bf16_f32 v89, v90, v91\n"
    "global_store_dwordx4 v232, v[0:3], s[50:51] offset:0 sc1\n"
    "v_cvt_pk_bf16_f32 v90, v92, v93\n"
    "v_cvt_pk_bf16_f32 v91, v94, v95\n"
    "global_store_dwordx4 v232, v[8:11], s[50:51] offset:2048 sc1\n"
    "v_cvt_pk_bf16_f32 v96, v96, v97\n"
    "v_cvt_pk_bf16_f32 v97, v98, v99\n"
    "global_store_dwordx4 v233, v[16:19], s[50:51] offset:0 sc1\n"
    "v_cvt_pk_bf16_f32 v98, v100, v101\n"
    "v_cvt_pk_bf16_f32 v99, v102, v103\n"
    "global_store_dwordx4 v233, v[24:27], s[50:51] offset:2048 sc1\n"
    "v_cvt_pk_bf16_f32 v104, v104, v105\n"
    "v_cvt_pk_bf16_f32 v105, v106, v107\n"
    "global_store_dwordx4 v234, v[32:35], s[50:51] offset:0 sc1\n"
    "v_cvt_pk_bf16_f32 v106, v108, v109\n"
    "v_cvt_pk_bf16_f32 v107, v110, v111\n"
    "global_store_dwordx4 v234, v[40:43], s[50:51] offset:2048 sc1\n"
    "v_cvt_pk_bf16_f32 v112, v112, v113\n"
    "v_cvt_pk_bf16_f32 v113, v114, v115\n"
    "global_store_dwordx4 v235, v[48:51], s[50:51] offset:0 sc1\n"
    "v_cvt_pk_bf16_f32 v114, v116, v117\n"
    "v_cvt_pk_bf16_f32 v115, v118, v119\n"
    "global_store_dwordx4 v235, v[56:59], s[50:51] offset:2048 sc1\n"
    "v_cvt_pk_bf16_f32 v120, v120, v121\n"
    "v_cvt_pk_bf16_f32 v121, v122, v123\n"
    "global_store_dwordx4 v232, v[64:67], s[50:51] offset:256 sc1\n"
    "v_cvt_pk_bf16_f32 v122, v124, v125\n"
    "v_cvt_pk_bf16_f32 v123, v126, v127\n"
    "global_store_dwordx4 v232, v[72:75], s[50:51] offset:2304 sc1\n"
    "v_mov_b32 v252, 0\n"
    "v_mov_b32 v253, 0\n"
    "v_mov_b32 v254, 0\n"
    "v_mov_b32 v255, 0\n"
    "s_nop 1\n"
    "global_store_dwordx4 v233, v[80:83], s[50:51] offset:256 sc1\n"
    "v_mfma_f32_32x32x16_bf16 v[0:15], v[252:255], v[252:255], 0\n"
    "global_store_dwordx4 v233, v[88:91], s[50:51] offset:2304 sc1\n"
    "v_mfma_f32_32x32x16_bf16 v[16:31], v[252:255], v[252:255], 0\n"
    "global_store_dwordx4 v234, v[96:99], s[50:51] offset:256 sc1\n"
    "v_mfma_f32_32x32x16_bf16 v[32:47], v[252:255], v[252:255], 0\n"
    "global_store_dwordx4 v234, v[104:107], s[50:51] offset:2304 sc1\n"
    "v_mfma_f32_32x32x16_bf16 v[48:63], v[252:255], v[252:255], 0\n"
    "global_store_dwordx4 v235, v[112:115], s[50:51] offset:256 sc1\n"
    "s_waitcnt vmcnt(37) lgkmcnt(0)\n"
    "s_barrier\n"
    "v_mfma_f32_32x32x16_bf16 v[64:79], v[252:255], v[252:255], 0\n"
    "s_add_u32 m0, s58, 81920\n"
    "s_nop 0\n"
    "buffer_load_dwordx4 v192, s[40:43], s55 offen offset:0 lds\n"
    "global_store_dwordx4 v235, v[120:123], s[50:51] offset:2304 sc1\n"
    "v_mfma_f32_32x32x16_bf16 v[80:95], v[252:255], v[252:255], 0\n"
    "buffer_load_dwordx4 v193, s[40:43], s55 offen offset:1024 lds\n"
    "s_nop 1\n"
    "v_mfma_f32_32x32x16_bf16 v[96:111], v[252:255], v[252:255], 0\n"
    "buffer_load_dwordx4 v194, s[40:43], s55 offen offset:2048 lds\n"
    "v_mfma_f32_32x32x16_bf16 v[112:127], v[252:255], v[252:255], 0\n"
    "v_lshlrev_b32 v240, 16, v128\n"
    "buffer_load_dwordx4 v195, s[40:43], s55 offen offset:3072 lds\n"
    "s_add_u32 s59, s59, 1\n"
    "v_readlane_b32 s55, v211, s59\n"
    "v_and_b32 v241, s64, v128\n"
    "v_lshlrev_b32 v242, 16, v129\n"
    "s_nop 3\n"
    "v_and_b32 v243, s64, v129\n"
    "v_pk_mul_f32 v[240:241], v[240:241], s[60:61] op_sel_hi:[1,0]\n"
    "s_add_u32 m0, s58, 49152\n"
    "s_nop 0\n"
    "buffer_load_dwordx4 v188, s[36:39], s55 offen offset:0 lds\n"
    "v_lshlrev_b32 v244, 16, v130\n"
    "v_and_b32 v245, s64, v130\n"
    "buffer_load_dwordx4 v189, s[36:39], s55 offen offset:1024 lds\n"
    "v_pk_mul_f32 v[242:243], v[242:243], s[60:61] op_sel_hi:[1,0]\n"
    "v_cvt_pk_bf16_f32 v128, v240, v241\n"
    "buffer_load_dwordx4 v190, s[36:39], s55 offen offset:2048 lds\n"
    "v_lshlrev_b32 v246, 16, v131\n"
    "v_and_b32 v247, s64, v131\n"
    "buffer_load_dwordx4 v191, s[36:39], s55 offen offset:3072 lds\n"
    "v_pk_mul_f32 v[244:245], v[244:245], s[60:61] op_sel_hi:[1,0]\n"
    "v_cvt_pk_bf16_f32 v129, v242, v243\n"
    "v_accvgpr_write_b32 a0, v128\n"
    "v_lshlrev_b32 v240, 16, v132\n"
    "v_and_b32 v241, s64, v132\n"
    "v_pk_mul_f32 v[246:247], v[246:247], s[60:61] op_sel_hi:[1,0]\n"
    "v_cvt_pk_bf16_f32 v130, v244, v245\n"
    "v_accvgpr_write_b32 a1, v129\n"
    "v_lshlrev_b32 v242, 16, v133\n"
    "v_and_b32 v243, s64, v133\n"
    "v_pk_mul_f32 v[240:241], v[240:241], s[60:61] op_sel_hi:[1,0]\n"
    "v_cvt_pk_bf16_f32 v131, v246, v247\n"
    "v_accvgpr_write_b32 a2, v130\n"
    "v_lshlrev_b32 v244, 16, v134\n"
    "v_and_b32 v245, s64, v134\n"
    "v_pk_mul_f32 v[242:243], v[242:243], s[60:61] op_sel_hi:[1,0]\n"
    "v_cvt_pk_bf16_f32 v132, v240, v241\n"
    "v_accvgpr_write_b32 a3, v131\n"
    "v_lshlrev_b32 v246, 16, v135\n"
    "v_and_b32 v247, s64, v135\n"
    "v_pk_mul_f32 v[244:245], v[244:245], s[60:61] op_sel_hi:[1,0]\n"
    "v_cvt_pk_bf16_f32 v133, v242, v243\n"
    "v_accvgpr_write_b32 a4, v132\n"
    "v_lshlrev_b32 v240, 16, v136\n"
    "v_and_b32 v241, s64, v136\n"
    "v_pk_mul_f32 v[246:247], v[246:247], s[60:61] op_sel_hi:[1,0]\n"
    "v_cvt_pk_bf16_f32 v134, v244, v245\n"
    "v_accvgpr_write_b32 a5, v133\n"
    "v_lshlrev_b32 v242, 16, v137\n"
    "v_and_b32 v243, s64, v137\n"
    "v_pk_mul_f32 v[240:241], v[240:241], s[60:61] op_sel_hi:[1,0]\n"
    "v_cvt_pk_bf16_f32 v135, v246, v247\n"
    "v_accvgpr_write_b32 a6, v134\n"
    "v_lshlrev_b32 v244, 16, v138\n"
    "v_and_b32 v245, s64, v138\n"
    "v_pk_mul_f32 v[242:243], v[242:243], s[60:61] op_sel_hi:[1,0]\n"
    "v_cvt_pk_bf16_f32 v136, v240, v241\n"
    "v_accvgpr_write_b32 a7, v135\n"
    "v_lshlrev_b32 v246, 16, v139\n"
    "v_and_b32 v247, s64, v139\n"
    "v_pk_mul_f32 v[244:245], v[244:245], s[60:61] op_sel_hi:[1,0]\n"
    "v_cvt_pk_bf16_f32 v137, v242, v243\n"
    "v_accvgpr_write_b32 a8, v136\n"
    "v_lshlrev_b32 v240, 16, v140\n"
    "v_and_b32 v241, s64, v140\n"
    "v_pk_mul_f32 v[246:247], v[246:247], s[60:61] op_sel_hi:[1,0]\n"
    "v_cvt_pk_bf16_f32 v138, v244, v245\n"
    "v_accvgpr_write_b32 a9, v137\n"
    "v_lshlrev_b32 v242, 16, v141\n"
    "v_and_b32 v243, s64, v141\n"
    "v_pk_mul_f32 v[240:241], v[240:241], s[60:61] op_sel_hi:[1,0]\n"
    "v_cvt_pk_bf16_f32 v139, v246, v247\n"
    "v_accvgpr_write_b32 a10, v138\n"
    "v_lshlrev_b32 v244, 16, v142\n"
    "v_and_b32 v245, s64, v142\n"
    "v_pk_mul_f32 v[242:243], v[242:243], s[60:61] op_sel_hi:[1,0]\n"
    "v_cvt_pk_bf16_f32 v140, v240, v241\n"
    "v_accvgpr_write_b32 a11, v139\n"
    "v_lshlrev_b32 v246, 16, v143\n"
    "v_and_b32 v247, s64, v143\n"
    "v_pk_mul_f32 v[244:245], v[244:245], s[60:61] op_sel_hi:[1,0]\n"
    "v_cvt_pk_bf16_f32 v141, v242, v243\n"
    "v_accvgpr_write_b32 a12, v140\n"
    "v_lshlrev_b32 v240, 16, v144\n"
    "v_and_b32 v241, s64, v144\n"
    "v_pk_mul_f32 v[246:247], v[246:247], s[60:61] op_sel_hi:[1,0]\n"
    "v_cvt_pk_bf16_f32 v142, v244, v245\n"
    "v_accvgpr_write_b32 a13, v141\n"
    "v_lshlrev_b32 v242, 16, v145\n"
    "v_and_b32 v243, s64, v145\n"
    "v_pk_mul_f32 v[240:241], v[240:241], s[60:61] op_sel_hi:[1,0]\n"
    "v_cvt_pk_bf16_f32 v143, v246, v247\n"
    "v_accvgpr_write_b32 a14, v142\n"
    "v_lshlrev_b32 v244, 16, v146\n"
    "v_and_b32 v245, s64, v146\n"
    "v_pk_mul_f32 v[242:243], v[242:243], s[60:61] op_sel_hi:[1,0]\n"
    "v_cvt_pk_bf16_f32 v144, v240, v241\n"
    "v_accvgpr_write_b32 a15, v143\n"
    "v_lshlrev_b32 v246, 16, v147\n"
    "v_and_b32 v247, s64, v147\n"
    "v_pk_mul_f32 v[244:245], v[244:245], s[60:61] op_sel_hi:[1,0]\n"
    "v_cvt_pk_bf16_f32 v145, v242, v243\n"
    "v_accvgpr_write_b32 a16, v144\n"
    "v_lshlrev_b32 v240, 16, v148\n"
    "v_and_b32 v241, s64, v148\n"
    "v_pk_mul_f32 v[246:247], v[246:247], s[60:61] op_sel_hi:[1,0]\n"
    "v_cvt_pk_bf16_f32 v146, v244, v245\n"
    "v_accvgpr_write_b32 a17, v145\n"
    "v_lshlrev_b32 v242, 16, v149\n"
    "v_and_b32 v243, s64, v149\n"
    "v_pk_mul_f32 v[240:241], v[240:241], s[60:61] op_sel_hi:[1,0]\n"
    "v_cvt_pk_bf16_f32 v147, v246, v247\n"
    "v_accvgpr_write_b32 a18, v146\n"
    "v_lshlrev_b32 v244, 16, v150\n"
    "v_and_b32 v245, s64, v150\n"
    "v_pk_mul_f32 v[242:243], v[242:243], s[60:61] op_sel_hi:[1,0]\n"
    "v_cvt_pk_bf16_f32 v148, v240, v241\n"
    "v_accvgpr_write_b32 a19, v147\n"
    "v_lshlrev_b32 v246, 16, v151\n"
    "v_and_b32 v247, s64, v151\n"
    "v_pk_mul_f32 v[244:245], v[244:245], s[60:61] op_sel_hi:[1,0]\n"
    "v_cvt_pk_bf16_f32 v149, v242, v243\n"
    "v_accvgpr_write_b32 a20, v148\n"
    "v_lshlrev_b32 v240, 16, v152\n"
    "v_and_b32 v241, s64, v152\n"
    "v_pk_mul_f32 v[246:247], v[246:247], s[60:61] op_sel_hi:[1,0]\n"
    "v_cvt_pk_bf16_f32 v150, v244, v245\n"
    "v_accvgpr_write_b32 a21, v149\n"
    "v_lshlrev_b32 v242, 16, v153\n"
    "v_and_b32 v243, s64, v153\n"
    "v_pk_mul_f32 v[240:241], v[240:241], s[60:61] op_sel_hi:[1,0]\n"
    "v_cvt_pk_bf16_f32 v151, v246, v247\n"
    "v_accvgpr_write_b32 a22, v150\n"
    "v_lshlrev_b32 v244, 16, v154\n"
    "v_and_b32 v245, s64, v154\n"
    "v_pk_mul_f32 v[242:243], v[242:243], s[60:61] op_sel_hi:[1,0]\n"
    "v_cvt_pk_bf16_f32 v152, v240, v241\n"
    "v_accvgpr_write_b32 a23, v151\n"
    "v_lshlrev_b32 v246, 16, v155\n"
    "v_and_b32 v247, s64, v155\n"
    "v_pk_mul_f32 v[244:245], v[244:245], s[60:61] op_sel_hi:[1,0]\n"
    "v_cvt_pk_bf16_f32 v153, v242, v243\n"
    "v_accvgpr_write_b32 a24, v152\n"
    "v_lshlrev_b32 v240, 16, v156\n"
    "v_and_b32 v241, s64, v156\n"
    "v_pk_mul_f32 v[246:247], v[246:247], s[60:61] op_sel_hi:[1,0]\n"
    "v_cvt_pk_bf16_f32 v154, v244, v245\n"
    "v_accvgpr_write_b32 a25, v153\n"
    "v_lshlrev_b32 v242, 16, v157\n"
    "v_and_b32 v243, s64, v157\n"
    "v_pk_mul_f32 v[240:241], v[240:241], s[60:61] op_sel_hi:[1,0]\n"
    "v_cvt_pk_bf16_f32 v155, v246, v247\n"
    "v_accvgpr_write_b32 a26, v154\n"
    "v_lshlrev_b32 v244, 16, v158\n"
    "v_and_b32 v245, s64, v158\n"
    "v_pk_mul_f32 v[242:243], v[242:243], s[60:61] op_sel_hi:[1,0]\n"
    "v_cvt_pk_bf16_f32 v156, v240, v241\n"
    "v_accvgpr_write_b32 a27, v155\n"
    "v_lshlrev_b32 v246, 16, v159\n"
    "v_and_b32 v247, s64, v159\n"
    "v_pk_mul_f32 v[244:245], v[244:245], s[60:61] op_sel_hi:[1,0]\n"
    "v_cvt_pk_bf16_f32 v157, v242, v243\n"
    "v_accvgpr_write_b32 a28, v156\n"
    "v_pk_mul_f32 v[246:247], v[246:247], s[60:61] op_sel_hi:[1,0]\n"
    "v_cvt_pk_bf16_f32 v158, v244, v245\n"
    "v_accvgpr_write_b32 a29, v157\n"
    "v_cvt_pk_bf16_f32 v159, v246, v247\n"
    "v_accvgpr_write_b32 a30, v158\n"
    "v_accvgpr_write_b32 a31, v159\n"
    "s_waitcnt vmcnt(25)\n"
    "ds_read_b128 v[128:131], v224 offset:0\n"
    "ds_read_b128 v[132:135], v224 offset:4096\n"
    "ds_read_b128 v[136:139], v225 offset:0\n"
    "ds_read_b128 v[140:143], v225 offset:4096\n"
    "ds_read_b128 v[144:147], v226 offset:0\n"
    "ds_read_b128 v[148:151], v226 offset:4096\n"
    "ds_read_b128 v[152:155], v227 offset:0\n"
    "ds_read_b128 v[156:159], v227 offset:4096\n"
    "s_waitcnt lgkmcnt(7)\n"
    "v_lshlrev_b32 v240, 16, v128\n"
    "v_and_b32 v241, s64, v128\n"
    "v_lshlrev_b32 v242, 16, v129\n"
    "v_and_b32 v243, s64, v129\n"
    "v_pk_mul_f32 v[240:241], v[240:241], s[60:61] op_sel_hi:[1,0]\n"
    "v_lshlrev_b32 v244, 16, v130\n"
    "v_and_b32 v245, s64, v130\n"
    "v_pk_mul_f32 v[242:243], v[242:243], s[60:61] op_sel_hi:[1,0]\n"
    "v_cvt_pk_bf16_f32 v128, v240, v241\n"
    "v_lshlrev_b32 v246, 16, v131\n"
    "v_and_b32 v247, s64, v131\n"
    "v_pk_mul_f32 v[244:245], v[244:245], s[60:61] op_sel_hi:[1,0]\n"
    "v_cvt_pk_bf16_f32 v129, v242, v243\n"
    "v_accvgpr_write_b32 a32, v128\n"
    "s_waitcnt lgkmcnt(6)\n"
    "v_lshlrev_b32 v240, 16, v132\n"
    "v_and_b32 v241, s64, v132\n"
    "v_pk_mul_f32 v[246:247], v[246:247], s[60:61] op_sel_hi:[1,0]\n"
    "v_cvt_pk_bf16_f32 v130, v244, v245\n"
    "v_accvgpr_write_b32 a33, v129\n"
    "v_lshlrev_b32 v242, 16, v133\n"
    "v_and_b32 v243, s64, v133\n"
    "v_pk_mul_f32 v[240:241], v[240:241], s[60:61] op_sel_hi:[1,0]\n"
    "v_cvt_pk_bf16_f32 v131, v246, v247\n"
    "v_accvgpr_write_b32 a34, v130\n"
    "s_waitcnt vmcnt(37) lgkmcnt(0)\n"
    "s_barrier\n"
    "v_lshlrev_b32 v244, 16, v134\n"
    "s_add_u32 m0, s58, 98304\n"
    "s_nop 0\n"
    "buffer_load_dwordx4 v192, s[40:43], s54 offen offset:0 lds\n"
    "v_and_b32 v245, s64, v134\n"
    "v_pk_mul_f32 v[242:243], v[242:243], s[60:61] op_sel_hi:[1,0]\n"
    "buffer_load_dwordx4 v193, s[40:43], s54 offen offset:1024 lds\n"
    "v_cvt_pk_bf16_f32 v132, v240, v241\n"
    "v_accvgpr_write_b32 a35, v131\n"
    "buffer_load_dwordx4 v194, s[40:43], s54 offen offset:2048 lds\n"
    "v_lshlrev_b32 v246, 16, v135\n"
    "v_and_b32 v247, s64, v135\n"
    "buffer_load_dwordx4 v195, s[40:43], s54 offen offset:3072 lds\n"
    "s_add_u32 s59, s59, 1\n"
    "v_readlane_b32 s54, v211, s59\n"
    "v_pk_mul_f32 v[244:245], v[244:245], s[60:61] op_sel_hi:[1,0]\n"
    "v_cvt_pk_bf16_f32 v133, v242, v243\n"
    "v_accvgpr_write_b32 a36, v132\n"
    "v_lshlrev_b32 v240, 16, v136\n"
    "v_and_b32 v241, s64, v136\n"
    "v_pk_mul_f32 v[246:247], v[246:247], s[60:61] op_sel_hi:[1,0]\n"
    "v_cvt_pk_bf16_f32 v134, v244, v245\n"
    "v_accvgpr_write_b32 a37, v133\n"
    "v_lshlrev_b32 v242, 16, v137\n"
    "v_and_b32 v243, s64, v137\n"
    "v_pk_mul_f32 v[240:241], v[240:241], s[60:61] op_sel_hi:[1,0]\n"
    "v_cvt_pk_bf16_f32 v135, v246, v247\n"
    "v_accvgpr_write_b32 a38, v134\n"
    "v_lshlrev_b32 v244, 16, v138\n"
    "v_and_b32 v245, s64, v138\n"
    "v_pk_mul_f32 v[242:243], v[242:243], s[60:61] op_sel_hi:[1,0]\n"
    "v_cvt_pk_bf16_f32 v136, v240, v241\n"
    "v_accvgpr_write_b32 a39, v135\n"
    "v_lshlrev_b32 v246, 16, v139\n"
    "v_and_b32 v247, s64, v139\n"
    "v_pk_mul_f32 v[244:245], v[244:245], s[60:61] op_sel_hi:[1,0]\n"
    "v_cvt_pk_bf16_f32 v137, v242, v243\n"
    "v_accvgpr_write_b32 a40, v136\n"
    "v_lshlrev_b32 v240, 16, v140\n"
    "v_and_b32 v241, s64, v140\n"
    "v_pk_mul_f32 v[246:247], v[246:247], s[60:61] op_sel_hi:[1,0]\n"
    "v_cvt_pk_bf16_f32 v138, v244, v245\n"
    "v_accvgpr_write_b32 a41, v137\n"
    "v_lshlrev_b32 v242, 16, v141\n"
    "v_and_b32 v243, s64, v141\n"
    "v_pk_mul_f32 v[240:241], v[240:241], s[60:61] op_sel_hi:[1,0]\n"
    "v_cvt_pk_bf16_f32 v139, v246, v247\n"
    "v_accvgpr_write_b32 a42, v138\n"
    "v_lshlrev_b32 v244, 16, v142\n"
    "v_and_b32 v245, s64, v142\n"
    "v_pk_mul_f32 v[242:243], v[242:243], s[60:61] op_sel_hi:[1,0]\n"
    "v_cvt_pk_bf16_f32 v140, v240, v241\n"
    "v_accvgpr_write_b32 a43, v139\n"
    "v_lshlrev_b32 v246, 16, v143\n"
    "v_and_b32 v247, s64, v143\n"
    "v_pk_mul_f32 v[244:245], v[244:245], s[60:61] op_sel_hi:[1,0]\n"
    "v_cvt_pk_bf16_f32 v141, v242, v243\n"
    "v_accvgpr_write_b32 a44, v140\n"
    "v_lshlrev_b32 v240, 16, v144\n"
    "v_and_b32 v241, s64, v144\n"
    "v_pk_mul_f32 v[246:247], v[246:247], s[60:61] op_sel_hi:[1,0]\n"
    "v_cvt_pk_bf16_f32 v142, v244, v245\n"
    "v_accvgpr_write_b32 a45, v141\n"
    "v_lshlrev_b32 v242, 16, v145\n"
    "v_and_b32 v243, s64, v145\n"
    "v_pk_mul_f32 v[240:241], v[240:241], s[60:61] op_sel_hi:[1,0]\n"
    "v_cvt_pk_bf16_f32 v143, v246, v247\n"
    "v_accvgpr_write_b32 a46, v142\n"
    "v_lshlrev_b32 v244, 16, v146\n"
    "v_and_b32 v245, s64, v146\n"
    "v_pk_mul_f32 v[242:243], v[242:243], s[60:61] op_sel_hi:[1,0]\n"
    "v_cvt_pk_bf16_f32 v144, v240, v241\n"
    "v_accvgpr_write_b32 a47, v143\n"
    "v_lshlrev_b32 v246, 16, v147\n"
    "v_and_b32 v247, s64, v147\n"
    "v_pk_mul_f32 v[244:245], v[244:245], s[60:61] op_sel_hi:[1,0]\n"
    "v_cvt_pk_bf16_f32 v145, v242, v243\n"
    "v_accvgpr_write_b32 a48, v144\n"
    "v_lshlrev_b32 v240, 16, v148\n"
    "v_and_b32 v241, s64, v148\n"
    "v_pk_mul_f32 v[246:247], v[246:247], s[60:61] op_sel_hi:[1,0]\n"
    "v_cvt_pk_bf16_f32 v146, v244, v245\n"
    "v_accvgpr_write_b32 a49, v145\n"
    "v_lshlrev_b32 v242, 16, v149\n"
    "v_and_b32 v243, s64, v149\n"
    "v_pk_mul_f32 v[240:241], v[240:241], s[60:61] op_sel_hi:[1,0]\n"
    "v_cvt_pk_bf16_f32 v147, v246, v247\n"
    "v_accvgpr_write_b32 a50, v146\n"
    "v_lshlrev_b32 v244, 16, v150\n"
    "v_and_b32 v245, s64, v150\n"
    "v_pk_mul_f32 v[242:243], v[242:243], s[60:61] op_sel_hi:[1,0]\n"
    "v_cvt_pk_bf16_f32 v148, v240, v241\n"
    "v_accvgpr_write_b32 a51, v147\n"
    "v_lshlrev_b32 v246, 16, v151\n"
    "v_and_b32 v247, s64, v151\n"
    "v_pk_mul_f32 v[244:245], v[244:245], s[60:61] op_sel_hi:[1,0]\n"
    "v_cvt_pk_bf16_f32 v149, v242, v243\n"
    "v_accvgpr_write_b32 a52, v148\n"
    "v_lshlrev_b32 v240, 16, v152\n"
    "v_and_b32 v241, s64, v152\n"
    "v_pk_mul_f32 v[246:247], v[246:247], s[60:61] op_sel_hi:[1,0]\n"
    "v_cvt_pk_bf16_f32 v150, v244, v245\n"
    "v_accvgpr_write_b32 a53, v149\n"
    "v_lshlrev_b32 v242, 16, v153\n"
    "v_and_b32 v243, s64, v153\n"
    "v_pk_mul_f32 v[240:241], v[240:241], s[60:61] op_sel_hi:[1,0]\n"
    "v_cvt_pk_bf16_f32 v151, v246, v247\n"
    "v_accvgpr_write_b32 a54, v150\n"
    "v_lshlrev_b32 v244, 16, v154\n"
    "v_and_b32 v245, s64, v154\n"
    "v_pk_mul_f32 v[242:243], v[242:243], s[60:61] op_sel_hi:[1,0]\n"
    "v_cvt_pk_bf16_f32 v152, v240, v241\n"
    "v_accvgpr_write_b32 a55, v151\n"
    "v_lshlrev_b32 v246, 16, v155\n"
    "v_and_b32 v247, s64, v155\n"
    "v_pk_mul_f32 v[244:245], v[244:245], s[60:61] op_sel_hi:[1,0]\n"
    "v_cvt_pk_bf16_f32 v153, v242, v243\n"
    "v_accvgpr_write_b32 a56, v152\n"
    "v_lshlrev_b32 v240, 16, v156\n"
    "v_and_b32 v241, s64, v156\n"
    "v_pk_mul_f32 v[246:247], v[246:247], s[60:61] op_sel_hi:[1,0]\n"
    "v_cvt_pk_bf16_f32 v154, v244, v245\n"
    "v_accvgpr_write_b32 a57, v153\n"
    "v_lshlrev_b32 v242, 16, v157\n"
    "v_and_b32 v243, s64, v157\n"
    "v_pk_mul_f32 v[240:241], v[240:241], s[60:61] op_sel_hi:[1,0]\n"
    "v_cvt_pk_bf16_f32 v155, v246, v247\n"
    "v_accvgpr_write_b32 a58, v154\n"
    "v_lshlrev_b32 v244, 16, v158\n"
    "v_and_b32 v245, s64, v158\n"
    "v_pk_mul_f32 v[242:243], v[242:243], s[60:61] op_sel_hi:[1,0]\n"
    "v_cvt_pk_bf16_f32 v156, v240, v241\n"
    "v_accvgpr_write_b32 a59, v155\n"
    "v_lshlrev_b32 v246, 16, v159\n"
    "v_and_b32 v247, s64, v159\n"
    "v_pk_mul_f32 v[244:245], v[244:245], s[60:61] op_sel_hi:[1,0]\n"
    "v_cvt_pk_bf16_f32 v157, v242, v243\n"
    "v_accvgpr_write_b32 a60, v156\n"
    "v_pk_mul_f32 v[246:247], v[246:247], s[60:61] op_sel_hi:[1,0]\n"
    "v_cvt_pk_bf16_f32 v158, v244, v245\n"
    "v_accvgpr_write_b32 a61, v157\n"
    "v_cvt_pk_bf16_f32 v159, v246, v247\n"
    "v_accvgpr_write_b32 a62, v158\n"
    "v_accvgpr_write_b32 a63, v159\n"
    "v_mov_b32 v244, 0xc1500000\n"
    "v_mov_b32 v245, 0xc1500000\n"
    "v_mov_b32 v246, 0xc1500000\n"
    "v_mov_b32 v247, 0xc1500000\n"
    "v_cndmask_b32 v236, v244, v248, s[80:81]\n"
    "v_cndmask_b32 v237, v244, v248, s[82:83]\n"
    "v_cndmask_b32 v238, v244, v248, s[84:85]\n"
    "v_cndmask_b32 v239, v244, v248, s[86:87]\n"
    "s_mov_b32 s35, 1\n"
    "s_sub_u32 s65, 0, s70\n"
    "s_waitcnt lgkmcnt(0)\n"
    "ds_read_b128 a[128:131], v176 offset:16384\n"
    "ds_read_b128 a[132:135], v177 offset:16384\n"
    "ds_read_b128 a[136:139], v178 offset:16384\n"
    "ds_read_b128 a[140:143], v179 offset:16384\n"
    "ds_read_b128 a[144:147], v176 offset:16640\n"
    "ds_read_b128 a[148:151], v177 offset:16640\n"
    "ds_read_b128 a[152:155], v178 offset:16640\n"
    "ds_read_b128 a[156:159], v179 offset:16640\n"
    "s_waitcnt lgkmcnt(6)\n"
    "v_mfma_f32_16x16x32_bf16 v[144:147], a[128:131], a[0:3], v[236:239]\n"
    "v_mov_b32 v252, 0xc1500000\n"
    "v_mov_b32 v253, 0xc1500000\n"
    "v_mfma_f32_16x16x32_bf16 v[148:151], a[128:131], a[4:7], v[244:247]\n"
    "ds_read_b128 a[128:131], v176 offset:24576\n"
    "v_mov_b32 v254, 0xc1500000\n"
    "v_mfma_f32_16x16x32_bf16 v[144:147], a[132:135], a[8:11], v[144:147]\n"
    "v_mov_b32 v255, 0xc1500000\n"
    "v_mov_b32 v196, 0x41500000\n"
    "v_mfma_f32_16x16x32_bf16 v[148:151], a[132:135], a[12:15], v[148:151]\n"
    "s_waitcnt lgkmcnt(6)\n"
    "ds_read_b128 a[132:135], v177 offset:24576\n"
    "v_mov_b32 v197, 0x41500000\n"
    "v_mfma_f32_16x16x32_bf16 v[144:147], a[136:139], a[16:19], v[144:147]\n"
    "v_mov_b32 v198, 0\n"
    "v_mov_b32 v199, 0\n"
    "v_mfma_f32_16x16x32_bf16 v[148:151], a[136:139], a[20:23], v[148:151]\n"
    "s_waitcnt lgkmcnt(6)\n"
    "ds_read_b128 a[136:139], v178 offset:24576\n"
    "v_mfma_f32_16x16x32_bf16 v[144:147], a[140:143], a[24:27], v[144:147]\n"
    "v_mfma_f32_16x16x32_bf16 v[148:151], a[140:143], a[28:31], v[148:151]\n"
    "s_waitcnt lgkmcnt(6)\n"
    "ds_read_b128 a[140:143], v179 offset:24576\n"
    "v_mfma_f32_16x16x32_bf16 v[144:147], a[144:147], a[32:35], v[144:147]\n"
    "v_mfma_f32_16x16x32_bf16 v[148:151], a[144:147], a[36:39], v[148:151]\n"
    "s_waitcnt lgkmcnt(6)\n"
    "ds_read_b128 a[144:147], v176 offset:24832\n"
    "v_mfma_f32_16x16x32_bf16 v[144:147], a[148:151], a[40:43], v[144:147]\n"
    "v_mfma_f32_16x16x32_bf16 v[148:151], a[148:151], a[44:47], v[148:151]\n"
    "s_waitcnt lgkmcnt(6)\n"
    "ds_read_b128 a[148:151], v177 offset:24832\n"
    "v_mfma_f32_16x16x32_bf16 v[144:147], a[152:155], a[48:51], v[144:147]\n"
    "v_mfma_f32_16x16x32_bf16 v[148:151], a[152:155], a[52:55], v[148:151]\n"
    "s_waitcnt lgkmcnt(6)\n"
    "ds_read_b128 a[152:155], v178 offset:24832\n"
    "v_mfma_f32_16x16x32_bf16 v[144:147], a[156:159], a[56:59], v[144:147]\n"
    "v_mfma_f32_16x16x32_bf16 v[148:151], a[156:159], a[60:63], v[148:151]\n"
    "s_waitcnt lgkmcnt(6)\n"
    "ds_read_b128 a[156:159], v179 offset:24832\n"
    "v_mfma_f32_16x16x32_bf16 v[152:155], a[128:131], a[0:3], v[248:251]\n"
    "v_mfma_f32_16x16x32_bf16 v[156:159], a[128:131], a[4:7], v[236:239]\n"
    "s_waitcnt lgkmcnt(6)\n"
    "s_add_u32 m0, s58, 0\n"
    "s_nop 0\n"
    "buffer_load_dwordx4 v188, s[36:39], s54 offen offset:0 lds\n"
    "v_mfma_f32_16x16x32_bf16 v[152:155], a[132:135], a[8:11], v[152:155]\n"
    "v_mfma_f32_16x16x32_bf16 v[156:159], a[132:135], a[12:15], v[156:159]\n"
    "s_waitcnt lgkmcnt(5)\n"
    "buffer_load_dwordx4 v189, s[36:39], s54 offen offset:1024 lds\n"
    "v_mfma_f32_16x16x32_bf16 v[152:155], a[136:139], a[16:19], v[152:155]\n"
    "v_mov_b32 v236, 0xc1500000\n"
    "v_mov_b32 v237, 0xc1500000\n"
    "v_mfma_f32_16x16x32_bf16 v[156:159], a[136:139], a[20:23], v[156:159]\n"
    "s_waitcnt lgkmcnt(4)\n"
    "buffer_load_dwordx4 v190, s[36:39], s54 offen offset:2048 lds\n"
    "v_mfma_f32_16x16x32_bf16 v[152:155], a[140:143], a[24:27], v[152:155]\n"
    "v_mov_b32 v238, 0xc1500000\n"
    "v_mov_b32 v239, 0xc1500000\n"
    "v_mfma_f32_16x16x32_bf16 v[156:159], a[140:143], a[28:31], v[156:159]\n"
    "s_waitcnt lgkmcnt(3)\n"
    "buffer_load_dwordx4 v191, s[36:39], s54 offen offset:3072 lds\n"
    "v_mfma_f32_16x16x32_bf16 v[152:155], a[144:147], a[32:35], v[152:155]\n"
    "v_mov_b32 v248, 0xc1500000\n"
    "v_mov_b32 v249, 0xc1500000\n"
    "v_mfma_f32_16x16x32_bf16 v[156:159], a[144:147], a[36:39], v[156:159]\n"
    "s_waitcnt lgkmcnt(2)\n"
    "v_mov_b32 v250, 0xc1500000\n"
    "v_mov_b32 v251, 0xc1500000\n"
    "v_mfma_f32_16x16x32_bf16 v[152:155], a[148:151], a[40:43], v[152:155]\n"
    "v_min_f32 v204, v144, v148\n"
    "v_cmp_gt_f32 vcc, 0xc2540000, v204\n"
    "v_mfma_f32_16x16x32_bf16 v[156:159], a[148:151], a[44:47], v[156:159]\n"
    "s_waitcnt lgkmcnt(1)\n"
    "v_exp_f32 v240, v144\n"
    "v_mfma_f32_16x16x32_bf16 v[152:155], a[152:155], a[48:51], v[152:155]\n"
    "v_exp_f32 v241, v145\n"
    "v_mfma_f32_16x16x32_bf16 v[156:159], a[152:155], a[52:55], v[156:159]\n"
    "s_waitcnt lgkmcnt(0)\n"
    "v_exp_f32 v242, v146\n"
    "v_mfma_f32_16x16x32_bf16 v[152:155], a[156:159], a[56:59], v[152:155]\n"
    "v_add_f32 v216, v240, v241\n"
    "v_cvt_pk_bf16_f32 v168, v240, v241\n"
    "v_mfma_f32_16x16x32_bf16 v[156:159], a[156:159], a[60:63], v[156:159]\n"
    "v_exp_f32 v243, v147\n"
    "s_waitcnt vmcnt(12)\n"
    "s_barrier\n"
    "global_store_dword v213, v212, s[52:53] offset:64 sc1\n"
    "s_add_u32 m0, s58, 114688\n"
    "s_nop 0\n"
    "buffer_load_dwordx4 v192, s[40:43], s55 offen offset:0 lds\n"
    "ds_read_b128 a[128:131], v176 offset:32768\n"
    "ds_read_b128 a[132:135], v177 offset:32768\n"
    "s_and_b32 s72, vcc_lo, 0xffff\n"
    "s_cbranch_scc1 .Llow_24\n"
    ".Llowb_25:\n"
    "v_add_f32 v216, v216, v242\n"
    "ds_read_b128 a[136:139], v178 offset:32768\n"
    "v_exp_f32 v240, v148\n"
    "buffer_load_dwordx4 v193, s[40:43], s55 offen offset:1024 lds\n"
    "v_add_f32 v216, v216, v243\n"
    "ds_read_b128 a[140:143], v179 offset:32768\n"
    "v_cvt_pk_bf16_f32 v169, v242, v243\n"
    "buffer_load_dwordx4 v194, s[40:43], s55 offen offset:2048 lds\n"
    "v_exp_f32 v241, v149\n"
    "ds_read_b128 a[144:147], v176 offset:33024\n"
    "v_exp_f32 v242, v150\n"
    "buffer_load_dwordx4 v195, s[40:43], s55 offen offset:3072 lds\n"
    "s_add_u32 s59, s59, 1\n"
    "v_readlane_b32 s55, v211, s59\n"
    "v_add_f32 v217, v240, v241\n"
    "ds_read_b128 a[148:151], v177 offset:33024\n"
    "v_cvt_pk_bf16_f32 v172, v240, v241\n"
    "ds_read_b128 a[152:155], v178 offset:33024\n"
    "v_exp_f32 v243, v151\n"
    "ds_read_b128 a[156:159], v179 offset:33024\n"
    "v_add_f32 v217, v217, v242\n"
    "v_exp_f32 v240, v152\n"
    "v_add_f32 v217, v217, v243\n"
    "v_cvt_pk_bf16_f32 v173, v242, v243\n"
    "v_exp_f32 v241, v153\n"
    "v_add_f32 v216, v216, v240\n"
    "v_exp_f32 v242, v154\n"
    "v_add_f32 v216, v216, v241\n"
    "v_cvt_pk_bf16_f32 v170, v240, v241\n"
    "s_waitcnt lgkmcnt(6)\n"
    "s_add_u32 s65, s65, 1\n"
    "s_cbranch_scc1 .LX1\n"
    "s_branch .LG2\n"
    ".LX2:\n"
    "s_cmp_lg_u32 s63, 0\n"
    "s_cbranch_scc1 .Lev_26\n"
    "s_cmp_lg_u32 s35, 0\n"
    "s_cbranch_scc1 .Lev_27\n"
    "global_load_dword v212, v213, s[52:53] offset:0 sc1\n"
    "s_waitcnt lgkmcnt(0)\n"
    "ds_read_b64_tr_b16 a[160:161], v180 offset:32768\n"
    "ds_read_b64_tr_b16 a[162:163], v180 offset:40960\n"
    "ds_read_b64_tr_b16 a[164:165], v181 offset:32768\n"
    "ds_read_b64_tr_b16 a[166:167], v181 offset:40960\n"
    "ds_read_b64_tr_b16 a[168:169], v182 offset:32768\n"
    "ds_read_b64_tr_b16 a[170:171], v182 offset:40960\n"
    "ds_read_b64_tr_b16 a[172:173], v183 offset:32768\n"
    "ds_read_b64_tr_b16 a[174:175], v183 offset:40960\n"
    "ds_read_b64_tr_b16 a[176:177], v184 offset:32768\n"
    "ds_read_b64_tr_b16 a[178:179], v184 offset:40960\n"
    "ds_read_b64_tr_b16 a[180:181], v185 offset:32768\n"
    "ds_read_b64_tr_b16 a[182:183], v185 offset:40960\n"
    "ds_read_b64_tr_b16 a[184:185], v186 offset:32768\n"
    "ds_read_b64_tr_b16 a[186:187], v186 offset:40960\n"
    "v_exp_f32 v243, v139\n"
    "v_add_f32 v216, v216, v242\n"
    "v_exp_f32 v240, v140\n"
    "v_add_f32 v216, v216, v243\n"
    "v_cvt_pk_bf16_f32 v163, v242, v243\n"
    "v_exp_f32 v241, v141\n"
    "v_add_f32 v217, v217, v240\n"
    "v_exp_f32 v242, v142\n"
    "v_add_f32 v217, v217, v241\n"
    "v_exp_f32 v243, v143\n"
    "v_add_f32 v217, v217, v242\n"
    "v_cvt_pk_bf16_f32 v166, v240, v241\n"
    "v_add_f32 v217, v217, v243\n"
    "v_add_f32 v215, v216, v217\n"
    "v_cmp_lt_f32 vcc, s29, v215\n"
    "v_cvt_pk_bf16_f32 v167, v242, v243\n"
    "s_cbranch_vccnz .Lstub_28\n"
    ".Lstubb_29:\n"
    "v_add_f32 v198, v198, v216\n"
    "v_add_f32 v199, v199, v217\n"
    "ds_read_b64_tr_b16 a[188:189], v187 offset:32768\n"
    "s_waitcnt lgkmcnt(11)\n"
    "ds_read_b64_tr_b16 a[190:191], v187 offset:40960\n"
    "v_mfma_f32_16x16x32_bf16 v[0:3], a[160:163], v[160:163], v[0:3]\n"
    "v_mfma_f32_16x16x32_bf16 v[4:7], a[160:163], v[164:167], v[4:7]\n"
    "ds_read_b64_tr_b16 a[160:161], v180 offset:33024\n"
    "v_mfma_f32_16x16x32_bf16 v[8:11], a[164:167], v[160:163], v[8:11]\n"
    "ds_read_b64_tr_b16 a[162:163], v180 offset:41216\n"
    "v_mfma_f32_16x16x32_bf16 v[12:15], a[164:167], v[164:167], v[12:15]\n"
    "ds_read_b64_tr_b16 a[164:165], v181 offset:33024\n"
    "s_waitcnt lgkmcnt(13)\n"
    "v_mfma_f32_16x16x32_bf16 v[16:19], a[168:171], v[160:163], v[16:19]\n"
    "ds_read_b64_tr_b16 a[166:167], v181 offset:41216\n"
    "v_mfma_f32_16x16x32_bf16 v[20:23], a[168:171], v[164:167], v[20:23]\n"
    "ds_read_b64_tr_b16 a[168:169], v182 offset:33024\n"
    "v_cvt_pk_bf16_f32 v0, v0, v1\n"
    "s_waitcnt lgkmcnt(13)\n"
    "v_mfma_f32_16x16x32_bf16 v[24:27], a[172:175], v[160:163], v[24:27]\n"
    "ds_read_b64_tr_b16 a[170:171], v182 offset:41216\n"
    "v_cvt_pk_bf16_f32 v1, v2, v3\n"
    "v_mfma_f32_16x16x32_bf16 v[28:31], a[172:175], v[164:167], v[28:31]\n"
    "ds_read_b64_tr_b16 a[172:173], v183 offset:33024\n"
    "v_cvt_pk_bf16_f32 v2, v4, v5\n"
    "s_waitcnt lgkmcnt(13)\n"
    "v_mfma_f32_16x16x32_bf16 v[32:35], a[176:179], v[160:163], v[32:35]\n"
    "ds_read_b64_tr_b16 a[174:175], v183 offset:41216\n"
    "v_cvt_pk_bf16_f32 v3, v6, v7\n"
    "v_mfma_f32_16x16x32_bf16 v[36:39], a[176:179], v[164:167], v[36:39]\n"
    "ds_read_b64_tr_b16 a[176:177], v184 offset:33024\n"
    "v_cvt_pk_bf16_f32 v8, v8, v9\n"
    "s_waitcnt lgkmcnt(13)\n"
    "v_mfma_f32_16x16x32_bf16 v[40:43], a[180:183], v[160:163], v[40:43]\n"
    "ds_read_b64_tr_b16 a[178:179], v184 offset:41216\n"
    "s_waitcnt vmcnt(9)\n"
    "ds_read_b128 v[128:131], v224 offset:0\n"
    "s_waitcnt lgkmcnt(11)\n"
    "ds_read_b128 v[132:135], v224 offset:4096\n"
    "ds_read_b128 v[136:139], v225 offset:0\n"
    "ds_read_b128 v[140:143], v225 offset:4096\n"
    "ds_read_b128 v[144:147], v226 offset:0\n"
    "s_waitcnt lgkmcnt(11)\n"
    "ds_read_b128 v[148:151], v226 offset:4096\n"
    "ds_read_b128 v[152:155], v227 offset:0\n"
    "ds_read_b128 v[156:159], v227 offset:4096\n"
    "v_cvt_pk_bf16_f32 v9, v10, v11\n"
    "v_mfma_f32_16x16x32_bf16 v[44:47], a[180:183], v[164:167], v[44:47]\n"
    "ds_read_b64_tr_b16 a[180:181], v185 offset:33024\n"
    "v_cvt_pk_bf16_f32 v10, v12, v13\n"
    "v_mfma_f32_16x16x32_bf16 v[48:51], a[184:187], v[160:163], v[48:51]\n"
    "s_waitcnt lgkmcnt(11)\n"
    "ds_read_b64_tr_b16 a[182:183], v185 offset:41216\n"
    "v_cvt_pk_bf16_f32 v11, v14, v15\n"
    "v_mfma_f32_16x16x32_bf16 v[52:55], a[184:187], v[164:167], v[52:55]\n"
    "ds_read_b64_tr_b16 a[184:185], v186 offset:33024\n"
    "v_cvt_pk_bf16_f32 v16, v16, v17\n"
    "v_mfma_f32_16x16x32_bf16 v[56:59], a[188:191], v[160:163], v[56:59]\n"
    "ds_read_b64_tr_b16 a[186:187], v186 offset:41216\n"
    "s_waitcnt vmcnt(9)\n"
    "s_barrier\n"
    "v_cvt_pk_bf16_f32 v17, v18, v19\n"
    "s_add_u32 m0, s58, 81920\n"
    "s_nop 0\n"
    "buffer_load_dwordx4 v192, s[40:43], s55 offen offset:0 lds\n"
    "v_mfma_f32_16x16x32_bf16 v[60:63], a[188:191], v[164:167], v[60:63]\n"
    "ds_read_b64_tr_b16 a[188:189], v187 offset:33024\n"
    "buffer_load_dwordx4 v193, s[40:43], s55 offen offset:1024 lds\n"
    "v_cvt_pk_bf16_f32 v18, v20, v21\n"
    "v_mfma_f32_16x16x32_bf16 v[64:67], a[160:163], v[160:163], v[64:67]\n"
    "buffer_load_dwordx4 v194, s[40:43], s55 offen offset:2048 lds\n"
    "s_waitcnt lgkmcnt(11)\n"
    "ds_read_b64_tr_b16 a[190:191], v187 offset:41216\n"
    "s_waitcnt lgkmcnt(6)\n"
    "v_cvt_pk_bf16_f32 v19, v22, v23\n"
    "buffer_load_dwordx4 v195, s[40:43], s55 offen offset:3072 lds\n"
    "s_add_u32 s59, s59, 1\n"
    "v_readlane_b32 s55, v211, s59\n"
    "v_mfma_f32_16x16x32_bf16 v[68:71], a[160:163], v[164:167], v[68:71]\n"
    "v_cvt_pk_bf16_f32 v24, v24, v25\n"
    "s_add_u32 m0, s58, 32768\n"
    "s_nop 0\n"
    "buffer_load_dwordx4 v188, s[36:39], s54 offen offset:0 lds\n"
    "v_cvt_pk_bf16_f32 v25, v26, v27\n"
    "v_mfma_f32_16x16x32_bf16 v[72:75], a[164:167], v[160:163], v[72:75]\n"
    "buffer_load_dwordx4 v189, s[36:39], s54 offen offset:1024 lds\n"
    "v_cvt_pk_bf16_f32 v26, v28, v29\n"
    "v_cvt_pk_bf16_f32 v27, v30, v31\n"
    "buffer_load_dwordx4 v190, s[36:39], s54 offen offset:2048 lds\n"
    "v_mfma_f32_16x16x32_bf16 v[76:79], a[164:167], v[164:167], v[76:79]\n"
    "v_cvt_pk_bf16_f32 v32, v32, v33\n"
    "buffer_load_dwordx4 v191, s[36:39], s54 offen offset:3072 lds\n"
    "v_cvt_pk_bf16_f32 v33, v34, v35\n"
    "v_mfma_f32_16x16x32_bf16 v[80:83], a[168:171], v[160:163], v[80:83]\n"
    "s_add_u32 m0, s58, 49152\n"
    "s_nop 0\n"
    "buffer_load_dwordx4 v188, s[36:39], s55 offen offset:0 lds\n"
    "v_cvt_pk_bf16_f32 v34, v36, v37\n"
    "v_cvt_pk_bf16_f32 v35, v38, v39\n"
    "buffer_load_dwordx4 v189, s[36:39], s55 offen offset:1024 lds\n"
    "v_mfma_f32_16x16x32_bf16 v[84:87], a[168:171], v[164:167], v[84:87]\n"
    "v_cvt_pk_bf16_f32 v40, v40, v41\n"
    "buffer_load_dwordx4 v190, s[36:39], s55 offen offset:2048 lds\n"
    "v_cvt_pk_bf16_f32 v41, v42, v43\n"
    "v_mfma_f32_16x16x32_bf16 v[88:91], a[172:175], v[160:163], v[88:91]\n"
    "buffer_load_dwordx4 v191, s[36:39], s55 offen offset:3072 lds\n"
    "v_cvt_pk_bf16_f32 v42, v44, v45\n"
    "v_cvt_pk_bf16_f32 v43, v46, v47\n"
    "s_add_u32 m0, s91, 0\n"
    "s_mov_b32 s75, 256\n"
    "buffer_load_dwordx4 v228, s[92:95], s75 offen offset:0 lds\n"
    "v_mfma_f32_16x16x32_bf16 v[92:95], a[172:175], v[164:167], v[92:95]\n"
    "v_cvt_pk_bf16_f32 v48, v48, v49\n"
    "buffer_load_dwordx4 v229, s[92:95], s75 offen offset:1024 lds\n"
    "v_cvt_pk_bf16_f32 v49, v50, v51\n"
    "v_mfma_f32_16x16x32_bf16 v[96:99], a[176:179], v[160:163], v[96:99]\n"
    "buffer_load_dwordx4 v230, s[92:95], s75 offen offset:2048 lds\n"
    "v_cvt_pk_bf16_f32 v50, v52, v53\n"
    "v_cvt_pk_bf16_f32 v51, v54, v55\n"
    "buffer_load_dwordx4 v231, s[92:95], s75 offen offset:3072 lds\n"
    "v_mfma_f32_16x16x32_bf16 v[100:103], a[176:179], v[164:167], v[100:103]\n"
    "v_cvt_pk_bf16_f32 v56, v56, v57\n"
    "s_add_u32 m0, s91, 4096\n"
    "s_mov_b32 s75, 8448\n"
    "buffer_load_dwordx4 v228, s[92:95], s75 offen offset:0 lds\n"
    "v_cvt_pk_bf16_f32 v57, v58, v59\n"
    "s_waitcnt lgkmcnt(4)\n"
    "v_mfma_f32_16x16x32_bf16 v[104:107], a[180:183], v[160:163], v[104:107]\n"
    "buffer_load_dwordx4 v229, s[92:95], s75 offen offset:1024 lds\n"
    "v_cvt_pk_bf16_f32 v58, v60, v61\n"
    "v_cvt_pk_bf16_f32 v59, v62, v63\n"
    "buffer_load_dwordx4 v230, s[92:95], s75 offen offset:2048 lds\n"
    "v_mfma_f32_16x16x32_bf16 v[108:111], a[180:183], v[164:167], v[108:111]\n"
    "v_cvt_pk_bf16_f32 v64, v64, v65\n"
    "buffer_load_dwordx4 v231, s[92:95], s75 offen offset:3072 lds\n"
    "v_cvt_pk_bf16_f32 v65, v66, v67\n"
    "s_waitcnt lgkmcnt(2)\n"
    "v_mfma_f32_16x16x32_bf16 v[112:115], a[184:187], v[160:163], v[112:115]\n"
    "v_cvt_pk_bf16_f32 v66, v68, v69\n"
    "v_cvt_pk_bf16_f32 v67, v70, v71\n"
    "v_mfma_f32_16x16x32_bf16 v[116:119], a[184:187], v[164:167], v[116:119]\n"
    "v_cvt_pk_bf16_f32 v72, v72, v73\n"
    "v_cvt_pk_bf16_f32 v73, v74, v75\n"
    "s_waitcnt lgkmcnt(0)\n"
    "v_mfma_f32_16x16x32_bf16 v[120:123], a[188:191], v[160:163], v[120:123]\n"
    "v_cvt_pk_bf16_f32 v74, v76, v77\n"
    "v_cvt_pk_bf16_f32 v75, v78, v79\n"
    "v_mfma_f32_16x16x32_bf16 v[124:127], a[188:191], v[164:167], v[124:127]\n"
    "v_cvt_pk_bf16_f32 v80, v80, v81\n"
    "v_cvt_pk_bf16_f32 v81, v82, v83\n"
    "s_nop 3\n"
    "v_cvt_pk_bf16_f32 v82, v84, v85\n"
    "v_cvt_pk_bf16_f32 v83, v86, v87\n"
    "global_store_dwordx4 v214, v[196:199], s[44:45] offset:0 sc1\n"
    "v_cvt_pk_bf16_f32 v88, v88, v89\n"
    "v_cvt_pk_bf16_f32 v89, v90, v91\n"
    "global_store_dwordx4 v232, v[0:3], s[50:51] offset:0 sc1\n"
    "v_cvt_pk_bf16_f32 v90, v92, v93\n"
    "v_cvt_pk_bf16_f32 v91, v94, v95\n"
    "global_store_dwordx4 v232, v[8:11], s[50:51] offset:2048 sc1\n"
    "v_cvt_pk_bf16_f32 v96, v96, v97\n"
    "v_cvt_pk_bf16_f32 v97, v98, v99\n"
    "global_store_dwordx4 v233, v[16:19], s[50:51] offset:0 sc1\n"
    "v_cvt_pk_bf16_f32 v98, v100, v101\n"
    "v_cvt_pk_bf16_f32 v99, v102, v103\n"
    "global_store_dwordx4 v233, v[24:27], s[50:51] offset:2048 sc1\n"
    "v_cvt_pk_bf16_f32 v104, v104, v105\n"
    "v_cvt_pk_bf16_f32 v105, v106, v107\n"
    "global_store_dwordx4 v234, v[32:35], s[50:51] offset:0 sc1\n"
    "v_cvt_pk_bf16_f32 v106, v108, v109\n"
    "v_cvt_pk_bf16_f32 v107, v110, v111\n"
    "global_store_dwordx4 v234, v[40:43], s[50:51] offset:2048 sc1\n"
    "v_cvt_pk_bf16_f32 v112, v112, v113\n"
    "v_cvt_pk_bf16_f32 v113, v114, v115\n"
    "global_store_dwordx4 v235, v[48:51], s[50:51] offset:0 sc1\n"
    "v_cvt_pk_bf16_f32 v114, v116, v117\n"
    "v_cvt_pk_bf16_f32 v115, v118, v119\n"
    "global_store_dwordx4 v235, v[56:59], s[50:51] offset:2048 sc1\n"
    "v_cvt_pk_bf16_f32 v120, v120, v121\n"
    "v_cvt_pk_bf16_f32 v121, v122, v123\n"
    "global_store_dwordx4 v232, v[64:67], s[50:51] offset:256 sc1\n"
    "v_cvt_pk_bf16_f32 v122, v124, v125\n"
    "v_cvt_pk_bf16_f32 v123, v126, v127\n"
    "global_store_dwordx4 v232, v[72:75], s[50:51] offset:2304 sc1\n"
    "v_mov_b32 v252, 0\n"
    "v_mov_b32 v253, 0\n"
    "v_mov_b32 v254, 0\n"
    "v_mov_b32 v255, 0\n"
    "s_nop 1\n"
    "global_store_dwordx4 v233, v[80:83], s[50:51] offset:256 sc1\n"
    "v_mfma_f32_32x32x16_bf16 v[0:15], v[252:255], v[252:255], 0\n"
    "global_store_dwordx4 v233, v[88:91], s[50:51] offset:2304 sc1\n"
    "v_mfma_f32_32x32x16_bf16 v[16:31], v[252:255], v[252:255], 0\n"
    "global_store_dwordx4 v234, v[96:99], s[50:51] offset:256 sc1\n"
    "v_mfma_f32_32x32x16_bf16 v[32:47], v[252:255], v[252:255], 0\n"
    "global_store_dwordx4 v234, v[104:107], s[50:51] offset:2304 sc1\n"
    "v_mfma_f32_32x32x16_bf16 v[48:63], v[252:255], v[252:255], 0\n"
    "global_store_dwordx4 v235, v[112:115], s[50:51] offset:256 sc1\n"
    "s_waitcnt vmcnt(37) lgkmcnt(0)\n"
    "s_barrier\n"
    "v_mfma_f32_32x32x16_bf16 v[64:79], v[252:255], v[252:255], 0\n"
    "s_add_u32 m0, s58, 98304\n"
    "s_nop 0\n"
    "buffer_load_dwordx4 v192, s[40:43], s54 offen offset:0 lds\n"
    "global_store_dwordx4 v235, v[120:123], s[50:51] offset:2304 sc1\n"
    "v_mfma_f32_32x32x16_bf16 v[80:95], v[252:255], v[252:255], 0\n"
    "buffer_load_dwordx4 v193, s[40:43], s54 offen offset:1024 lds\n"
    "s_nop 1\n"
    "v_mfma_f32_32x32x16_bf16 v[96:111], v[252:255], v[252:255], 0\n"
    "buffer_load_dwordx4 v194, s[40:43], s54 offen offset:2048 lds\n"
    "v_mfma_f32_32x32x16_bf16 v[112:127], v[252:255], v[252:255], 0\n"
    "v_lshlrev_b32 v240, 16, v128\n"
    "buffer_load_dwordx4 v195, s[40:43], s54 offen offset:3072 lds\n"
    "s_add_u32 s59, s59, 1\n"
    "v_readlane_b32 s54, v211, s59\n"
    "v_and_b32 v241, s64, v128\n"
    "v_lshlrev_b32 v242, 16, v129\n"
    "s_nop 3\n"
    "v_and_b32 v243, s64, v129\n"
    "v_pk_mul_f32 v[240:241], v[240:241], s[60:61] op_sel_hi:[1,0]\n"
    "s_add_u32 m0, s58, 0\n"
    "s_nop 0\n"
    "buffer_load_dwordx4 v188, s[36:39], s54 offen offset:0 lds\n"
    "v_lshlrev_b32 v244, 16, v130\n"
    "v_and_b32 v245, s64, v130\n"
    "buffer_load_dwordx4 v189, s[36:39], s54 offen offset:1024 lds\n"
    "v_pk_mul_f32 v[242:243], v[242:243], s[60:61] op_sel_hi:[1,0]\n"
    "v_cvt_pk_bf16_f32 v128, v240, v241\n"
    "buffer_load_dwordx4 v190, s[36:39], s54 offen offset:2048 lds\n"
    "v_lshlrev_b32 v246, 16, v131\n"
    "v_and_b32 v247, s64, v131\n"
    "buffer_load_dwordx4 v191, s[36:39], s54 offen offset:3072 lds\n"
    "v_pk_mul_f32 v[244:245], v[244:245], s[60:61] op_sel_hi:[1,0]\n"
    "v_cvt_pk_bf16_f32 v129, v242, v243\n"
    "v_accvgpr_write_b32 a0, v128\n"
    "v_lshlrev_b32 v240, 16, v132\n"
    "v_and_b32 v241, s64, v132\n"
    "v_pk_mul_f32 v[246:247], v[246:247], s[60:61] op_sel_hi:[1,0]\n"
    "v_cvt_pk_bf16_f32 v130, v244, v245\n"
    "v_accvgpr_write_b32 a1, v129\n"
    "v_lshlrev_b32 v242, 16, v133\n"
    "v_and_b32 v243, s64, v133\n"
    "v_pk_mul_f32 v[240:241], v[240:241], s[60:61] op_sel_hi:[1,0]\n"
    "v_cvt_pk_bf16_f32 v131, v246, v247\n"
    "v_accvgpr_write_b32 a2, v130\n"
    "v_lshlrev_b32 v244, 16, v134\n"
    "v_and_b32 v245, s64, v134\n"
    "v_pk_mul_f32 v[242:243], v[242:243], s[60:61] op_sel_hi:[1,0]\n"
    "v_cvt_pk_bf16_f32 v132, v240, v241\n"
    "v_accvgpr_write_b32 a3, v131\n"
    "v_lshlrev_b32 v246, 16, v135\n"
    "v_and_b32 v247, s64, v135\n"
    "v_pk_mul_f32 v[244:245], v[244:245], s[60:61] op_sel_hi:[1,0]\n"
    "v_cvt_pk_bf16_f32 v133, v242, v243\n"
    "v_accvgpr_write_b32 a4, v132\n"
    "v_lshlrev_b32 v240, 16, v136\n"
    "v_and_b32 v241, s64, v136\n"
    "v_pk_mul_f32 v[246:247], v[246:247], s[60:61] op_sel_hi:[1,0]\n"
    "v_cvt_pk_bf16_f32 v134, v244, v245\n"
    "v_accvgpr_write_b32 a5, v133\n"
    "v_lshlrev_b32 v242, 16, v137\n"
    "v_and_b32 v243, s64, v137\n"
    "v_pk_mul_f32 v[240:241], v[240:241], s[60:61] op_sel_hi:[1,0]\n"
    "v_cvt_pk_bf16_f32 v135, v246, v247\n"
    "v_accvgpr_write_b32 a6, v134\n"
    "v_lshlrev_b32 v244, 16, v138\n"
    "v_and_b32 v245, s64, v138\n"
    "v_pk_mul_f32 v[242:243], v[242:243], s[60:61] op_sel_hi:[1,0]\n"
    "v_cvt_pk_bf16_f32 v136, v240, v241\n"
    "v_accvgpr_write_b32 a7, v135\n"
    "v_lshlrev_b32 v246, 16, v139\n"
    "v_and_b32 v247, s64, v139\n"
    "v_pk_mul_f32 v[244:245], v[244:245], s[60:61] op_sel_hi:[1,0]\n"
    "v_cvt_pk_bf16_f32 v137, v242, v243\n"
    "v_accvgpr_write_b32 a8, v136\n"
    "v_lshlrev_b32 v240, 16, v140\n"
    "v_and_b32 v241, s64, v140\n"
    "v_pk_mul_f32 v[246:247], v[246:247], s[60:61] op_sel_hi:[1,0]\n"
    "v_cvt_pk_bf16_f32 v138, v244, v245\n"
    "v_accvgpr_write_b32 a9, v137\n"
    "v_lshlrev_b32 v242, 16, v141\n"
    "v_and_b32 v243, s64, v141\n"
    "v_pk_mul_f32 v[240:241], v[240:241], s[60:61] op_sel_hi:[1,0]\n"
    "v_cvt_pk_bf16_f32 v139, v246, v247\n"
    "v_accvgpr_write_b32 a10, v138\n"
    "v_lshlrev_b32 v244, 16, v142\n"
    "v_and_b32 v245, s64, v142\n"
    "v_pk_mul_f32 v[242:243], v[242:243], s[60:61] op_sel_hi:[1,0]\n"
    "v_cvt_pk_bf16_f32 v140, v240, v241\n"
    "v_accvgpr_write_b32 a11, v139\n"
    "v_lshlrev_b32 v246, 16, v143\n"
    "v_and_b32 v247, s64, v143\n"
    "v_pk_mul_f32 v[244:245], v[244:245], s[60:61] op_sel_hi:[1,0]\n"
    "v_cvt_pk_bf16_f32 v141, v242, v243\n"
    "v_accvgpr_write_b32 a12, v140\n"
    "v_lshlrev_b32 v240, 16, v144\n"
    "v_and_b32 v241, s64, v144\n"
    "v_pk_mul_f32 v[246:247], v[246:247], s[60:61] op_sel_hi:[1,0]\n"
    "v_cvt_pk_bf16_f32 v142, v244, v245\n"
    "v_accvgpr_write_b32 a13, v141\n"
    "v_lshlrev_b32 v242, 16, v145\n"
    "v_and_b32 v243, s64, v145\n"
    "v_pk_mul_f32 v[240:241], v[240:241], s[60:61] op_sel_hi:[1,0]\n"
    "v_cvt_pk_bf16_f32 v143, v246, v247\n"
    "v_accvgpr_write_b32 a14, v142\n"
    "v_lshlrev_b32 v244, 16, v146\n"
    "v_and_b32 v245, s64, v146\n"
    "v_pk_mul_f32 v[242:243], v[242:243], s[60:61] op_sel_hi:[1,0]\n"
    "v_cvt_pk_bf16_f32 v144, v240, v241\n"
    "v_accvgpr_write_b32 a15, v143\n"
    "v_lshlrev_b32 v246, 16, v147\n"
    "v_and_b32 v247, s64, v147\n"
    "v_pk_mul_f32 v[244:245], v[244:245], s[60:61] op_sel_hi:[1,0]\n"
    "v_cvt_pk_bf16_f32 v145, v242, v243\n"
    "v_accvgpr_write_b32 a16, v144\n"
    "v_lshlrev_b32 v240, 16, v148\n"
    "v_and_b32 v241, s64, v148\n"
    "v_pk_mul_f32 v[246:247], v[246:247], s[60:61] op_sel_hi:[1,0]\n"
    "v_cvt_pk_bf16_f32 v146, v244, v245\n"
    "v_accvgpr_write_b32 a17, v145\n"
    "v_lshlrev_b32 v242, 16, v149\n"
    "v_and_b32 v243, s64, v149\n"
    "v_pk_mul_f32 v[240:241], v[240:241], s[60:61] op_sel_hi:[1,0]\n"
    "v_cvt_pk_bf16_f32 v147, v246, v247\n"
    "v_accvgpr_write_b32 a18, v146\n"
    "v_lshlrev_b32 v244, 16, v150\n"
    "v_and_b32 v245, s64, v150\n"
    "v_pk_mul_f32 v[242:243], v[242:243], s[60:61] op_sel_hi:[1,0]\n"
    "v_cvt_pk_bf16_f32 v148, v240, v241\n"
    "v_accvgpr_write_b32 a19, v147\n"
    "v_lshlrev_b32 v246, 16, v151\n"
    "v_and_b32 v247, s64, v151\n"
    "v_pk_mul_f32 v[244:245], v[244:245], s[60:61] op_sel_hi:[1,0]\n"
    "v_cvt_pk_bf16_f32 v149, v242, v243\n"
    "v_accvgpr_write_b32 a20, v148\n"
    "v_lshlrev_b32 v240, 16, v152\n"
    "v_and_b32 v241, s64, v152\n"
    "v_pk_mul_f32 v[246:247], v[246:247], s[60:61] op_sel_hi:[1,0]\n"
    "v_cvt_pk_bf16_f32 v150, v244, v245\n"
    "v_accvgpr_write_b32 a21, v149\n"
    "v_lshlrev_b32 v242, 16, v153\n"
    "v_and_b32 v243, s64, v153\n"
    "v_pk_mul_f32 v[240:241], v[240:241], s[60:61] op_sel_hi:[1,0]\n"
    "v_cvt_pk_bf16_f32 v151, v246, v247\n"
    "v_accvgpr_write_b32 a22, v150\n"
    "v_lshlrev_b32 v244, 16, v154\n"
    "v_and_b32 v245, s64, v154\n"
    "v_pk_mul_f32 v[242:243], v[242:243], s[60:61] op_sel_hi:[1,0]\n"
    "v_cvt_pk_bf16_f32 v152, v240, v241\n"
    "v_accvgpr_write_b32 a23, v151\n"
    "v_lshlrev_b32 v246, 16, v155\n"
    "v_and_b32 v247, s64, v155\n"
    "v_pk_mul_f32 v[244:245], v[244:245], s[60:61] op_sel_hi:[1,0]\n"
    "v_cvt_pk_bf16_f32 v153, v242, v243\n"
    "v_accvgpr_write_b32 a24, v152\n"
    "v_lshlrev_b32 v240, 16, v156\n"
    "v_and_b32 v241, s64, v156\n"
    "v_pk_mul_f32 v[246:247], v[246:247], s[60:61] op_sel_hi:[1,0]\n"
    "v_cvt_pk_bf16_f32 v154, v244, v245\n"
    "v_accvgpr_write_b32 a25, v153\n"
    "v_lshlrev_b32 v242, 16, v157\n"
    "v_and_b32 v243, s64, v157\n"
    "v_pk_mul_f32 v[240:241], v[240:241], s[60:61] op_sel_hi:[1,0]\n"
    "v_cvt_pk_bf16_f32 v155, v246, v247\n"
    "v_accvgpr_write_b32 a26, v154\n"
    "v_lshlrev_b32 v244, 16, v158\n"
    "v_and_b32 v245, s64, v158\n"
    "v_pk_mul_f32 v[242:243], v[242:243], s[60:61] op_sel_hi:[1,0]\n"
    "v_cvt_pk_bf16_f32 v156, v240, v241\n"
    "v_accvgpr_write_b32 a27, v155\n"
    "v_lshlrev_b32 v246, 16, v159\n"
    "v_and_b32 v247, s64, v159\n"
    "v_pk_mul_f32 v[244:245], v[244:245], s[60:61] op_sel_hi:[1,0]\n"
    "v_cvt_pk_bf16_f32 v157, v242, v243\n"
    "v_accvgpr_write_b32 a28, v156\n"
    "v_pk_mul_f32 v[246:247], v[246:247], s[60:61] op_sel_hi:[1,0]\n"
    "v_cvt_pk_bf16_f32 v158, v244, v245\n"
    "v_accvgpr_write_b32 a29, v157\n"
    "v_cvt_pk_bf16_f32 v159, v246, v247\n"
    "v_accvgpr_write_b32 a30, v158\n"
    "v_accvgpr_write_b32 a31, v159\n"
    "s_waitcnt vmcnt(25)\n"
    "ds_read_b128 v[128:131], v224 offset:0\n"
    "ds_read_b128 v[132:135], v224 offset:4096\n"
    "ds_read_b128 v[136:139], v225 offset:0\n"
    "ds_read_b128 v[140:143], v225 offset:4096\n"
    "ds_read_b128 v[144:147], v226 offset:0\n"
    "ds_read_b128 v[148:151], v226 offset:4096\n"
    "ds_read_b128 v[152:155], v227 offset:0\n"
    "ds_read_b128 v[156:159], v227 offset:4096\n"
    "s_waitcnt lgkmcnt(7)\n"
    "v_lshlrev_b32 v240, 16, v128\n"
    "v_and_b32 v241, s64, v128\n"
    "v_lshlrev_b32 v242, 16, v129\n"
    "v_and_b32 v243, s64, v129\n"
    "v_pk_mul_f32 v[240:241], v[240:241], s[60:61] op_sel_hi:[1,0]\n"
    "v_lshlrev_b32 v244, 16, v130\n"
    "v_and_b32 v245, s64, v130\n"
    "v_pk_mul_f32 v[242:243], v[242:243], s[60:61] op_sel_hi:[1,0]\n"
    "v_cvt_pk_bf16_f32 v128, v240, v241\n"
    "v_lshlrev_b32 v246, 16, v131\n"
    "v_and_b32 v247, s64, v131\n"
    "v_pk_mul_f32 v[244:245], v[244:245], s[60:61] op_sel_hi:[1,0]\n"
    "v_cvt_pk_bf16_f32 v129, v242, v243\n"
    "v_accvgpr_write_b32 a32, v128\n"
    "s_waitcnt lgkmcnt(6)\n"
    "v_lshlrev_b32 v240, 16, v132\n"
    "v_and_b32 v241, s64, v132\n"
    "v_pk_mul_f32 v[246:247], v[246:247], s[60:61] op_sel_hi:[1,0]\n"
    "v_cvt_pk_bf16_f32 v130, v244, v245\n"
    "v_accvgpr_write_b32 a33, v129\n"
    "v_lshlrev_b32 v242, 16, v133\n"
    "v_and_b32 v243, s64, v133\n"
    "v_pk_mul_f32 v[240:241], v[240:241], s[60:61] op_sel_hi:[1,0]\n"
    "v_cvt_pk_bf16_f32 v131, v246, v247\n"
    "v_accvgpr_write_b32 a34, v130\n"
    "s_waitcnt vmcnt(37) lgkmcnt(0)\n"
    "s_barrier\n"
    "v_lshlrev_b32 v244, 16, v134\n"
    "s_add_u32 m0, s58, 114688\n"
    "s_nop 0\n"
    "buffer_load_dwordx4 v192, s[40:43], s55 offen offset:0 lds\n"
    "v_and_b32 v245, s64, v134\n"
    "v_pk_mul_f32 v[242:243], v[242:243], s[60:61] op_sel_hi:[1,0]\n"
    "buffer_load_dwordx4 v193, s[40:43], s55 offen offset:1024 lds\n"
    "v_cvt_pk_bf16_f32 v132, v240, v241\n"
    "v_accvgpr_write_b32 a35, v131\n"
    "buffer_load_dwordx4 v194, s[40:43], s55 offen offset:2048 lds\n"
    "v_lshlrev_b32 v246, 16, v135\n"
    "v_and_b32 v247, s64, v135\n"
    "buffer_load_dwordx4 v195, s[40:43], s55 offen offset:3072 lds\n"
    "s_add_u32 s59, s59, 1\n"
    "v_readlane_b32 s55, v211, s59\n"
    "v_pk_mul_f32 v[244:245], v[244:245], s[60:61] op_sel_hi:[1,0]\n"
    "v_cvt_pk_bf16_f32 v133, v242, v243\n"
    "v_accvgpr_write_b32 a36, v132\n"
    "v_lshlrev_b32 v240, 16, v136\n"
    "v_and_b32 v241, s64, v136\n"
    "v_pk_mul_f32 v[246:247], v[246:247], s[60:61] op_sel_hi:[1,0]\n"
    "v_cvt_pk_bf16_f32 v134, v244, v245\n"
    "v_accvgpr_write_b32 a37, v133\n"
    "v_lshlrev_b32 v242, 16, v137\n"
    "v_and_b32 v243, s64, v137\n"
    "v_pk_mul_f32 v[240:241], v[240:241], s[60:61] op_sel_hi:[1,0]\n"
    "v_cvt_pk_bf16_f32 v135, v246, v247\n"
    "v_accvgpr_write_b32 a38, v134\n"
    "v_lshlrev_b32 v244, 16, v138\n"
    "v_and_b32 v245, s64, v138\n"
    "v_pk_mul_f32 v[242:243], v[242:243], s[60:61] op_sel_hi:[1,0]\n"
    "v_cvt_pk_bf16_f32 v136, v240, v241\n"
    "v_accvgpr_write_b32 a39, v135\n"
    "v_lshlrev_b32 v246, 16, v139\n"
    "v_and_b32 v247, s64, v139\n"
    "v_pk_mul_f32 v[244:245], v[244:245], s[60:61] op_sel_hi:[1,0]\n"
    "v_cvt_pk_bf16_f32 v137, v242, v243\n"
    "v_accvgpr_write_b32 a40, v136\n"
    "v_lshlrev_b32 v240, 16, v140\n"
    "v_and_b32 v241, s64, v140\n"
    "v_pk_mul_f32 v[246:247], v[246:247], s[60:61] op_sel_hi:[1,0]\n"
    "v_cvt_pk_bf16_f32 v138, v244, v245\n"
    "v_accvgpr_write_b32 a41, v137\n"
    "v_lshlrev_b32 v242, 16, v141\n"
    "v_and_b32 v243, s64, v141\n"
    "v_pk_mul_f32 v[240:241], v[240:241], s[60:61] op_sel_hi:[1,0]\n"
    "v_cvt_pk_bf16_f32 v139, v246, v247\n"
    "v_accvgpr_write_b32 a42, v138\n"
    "v_lshlrev_b32 v244, 16, v142\n"
    "v_and_b32 v245, s64, v142\n"
    "v_pk_mul_f32 v[242:243], v[242:243], s[60:61] op_sel_hi:[1,0]\n"
    "v_cvt_pk_bf16_f32 v140, v240, v241\n"
    "v_accvgpr_write_b32 a43, v139\n"
    "v_lshlrev_b32 v246, 16, v143\n"
    "v_and_b32 v247, s64, v143\n"
    "v_pk_mul_f32 v[244:245], v[244:245], s[60:61] op_sel_hi:[1,0]\n"
    "v_cvt_pk_bf16_f32 v141, v242, v243\n"
    "v_accvgpr_write_b32 a44, v140\n"
    "v_lshlrev_b32 v240, 16, v144\n"
    "v_and_b32 v241, s64, v144\n"
    "v_pk_mul_f32 v[246:247], v[246:247], s[60:61] op_sel_hi:[1,0]\n"
    "v_cvt_pk_bf16_f32 v142, v244, v245\n"
    "v_accvgpr_write_b32 a45, v141\n"
    "v_lshlrev_b32 v242, 16, v145\n"
    "v_and_b32 v243, s64, v145\n"
    "v_pk_mul_f32 v[240:241], v[240:241], s[60:61] op_sel_hi:[1,0]\n"
    "v_cvt_pk_bf16_f32 v143, v246, v247\n"
    "v_accvgpr_write_b32 a46, v142\n"
    "v_lshlrev_b32 v244, 16, v146\n"
    "v_and_b32 v245, s64, v146\n"
    "v_pk_mul_f32 v[242:243], v[242:243], s[60:61] op_sel_hi:[1,0]\n"
    "v_cvt_pk_bf16_f32 v144, v240, v241\n"
    "v_accvgpr_write_b32 a47, v143\n"
    "v_lshlrev_b32 v246, 16, v147\n"
    "v_and_b32 v247, s64, v147\n"
    "v_pk_mul_f32 v[244:245], v[244:245], s[60:61] op_sel_hi:[1,0]\n"
    "v_cvt_pk_bf16_f32 v145, v242, v243\n"
    "v_accvgpr_write_b32 a48, v144\n"
    "v_lshlrev_b32 v240, 16, v148\n"
    "v_and_b32 v241, s64, v148\n"
    "v_pk_mul_f32 v[246:247], v[246:247], s[60:61] op_sel_hi:[1,0]\n"
    "v_cvt_pk_bf16_f32 v146, v244, v245\n"
    "v_accvgpr_write_b32 a49, v145\n"
    "v_lshlrev_b32 v242, 16, v149\n"
    "v_and_b32 v243, s64, v149\n"
    "v_pk_mul_f32 v[240:241], v[240:241], s[60:61] op_sel_hi:[1,0]\n"
    "v_cvt_pk_bf16_f32 v147, v246, v247\n"
    "v_accvgpr_write_b32 a50, v146\n"
    "v_lshlrev_b32 v244, 16, v150\n"
    "v_and_b32 v245, s64, v150\n"
    "v_pk_mul_f32 v[242:243], v[242:243], s[60:61] op_sel_hi:[1,0]\n"
    "v_cvt_pk_bf16_f32 v148, v240, v241\n"
    "v_accvgpr_write_b32 a51, v147\n"
    "v_lshlrev_b32 v246, 16, v151\n"
    "v_and_b32 v247, s64, v151\n"
    "v_pk_mul_f32 v[244:245], v[244:245], s[60:61] op_sel_hi:[1,0]\n"
    "v_cvt_pk_bf16_f32 v149, v242, v243\n"
    "v_accvgpr_write_b32 a52, v148\n"
    "v_lshlrev_b32 v240, 16, v152\n"
    "v_and_b32 v241, s64, v152\n"
    "v_pk_mul_f32 v[246:247], v[246:247], s[60:61] op_sel_hi:[1,0]\n"
    "v_cvt_pk_bf16_f32 v150, v244, v245\n"
    "v_accvgpr_write_b32 a53, v149\n"
    "v_lshlrev_b32 v242, 16, v153\n"
    "v_and_b32 v243, s64, v153\n"
    "v_pk_mul_f32 v[240:241], v[240:241], s[60:61] op_sel_hi:[1,0]\n"
    "v_cvt_pk_bf16_f32 v151, v246, v247\n"
    "v_accvgpr_write_b32 a54, v150\n"
    "v_lshlrev_b32 v244, 16, v154\n"
    "v_and_b32 v245, s64, v154\n"
    "v_pk_mul_f32 v[242:243], v[242:243], s[60:61] op_sel_hi:[1,0]\n"
    "v_cvt_pk_bf16_f32 v152, v240, v241\n"
    "v_accvgpr_write_b32 a55, v151\n"
    "v_lshlrev_b32 v246, 16, v155\n"
    "v_and_b32 v247, s64, v155\n"
    "v_pk_mul_f32 v[244:245], v[244:245], s[60:61] op_sel_hi:[1,0]\n"
    "v_cvt_pk_bf16_f32 v153, v242, v243\n"
    "v_accvgpr_write_b32 a56, v152\n"
    "v_lshlrev_b32 v240, 16, v156\n"
    "v_and_b32 v241, s64, v156\n"
    "v_pk_mul_f32 v[246:247], v[246:247], s[60:61] op_sel_hi:[1,0]\n"
    "v_cvt_pk_bf16_f32 v154, v244, v245\n"
    "v_accvgpr_write_b32 a57, v153\n"
    "v_lshlrev_b32 v242, 16, v157\n"
    "v_and_b32 v243, s64, v157\n"
    "v_pk_mul_f32 v[240:241], v[240:241], s[60:61] op_sel_hi:[1,0]\n"
    "v_cvt_pk_bf16_f32 v155, v246, v247\n"
    "v_accvgpr_write_b32 a58, v154\n"
    "v_lshlrev_b32 v244, 16, v158\n"
    "v_and_b32 v245, s64, v158\n"
    "v_pk_mul_f32 v[242:243], v[242:243], s[60:61] op_sel_hi:[1,0]\n"
    "v_cvt_pk_bf16_f32 v156, v240, v241\n"
    "v_accvgpr_write_b32 a59, v155\n"
    "v_lshlrev_b32 v246, 16, v159\n"
    "v_and_b32 v247, s64, v159\n"
    "v_pk_mul_f32 v[244:245], v[244:245], s[60:61] op_sel_hi:[1,0]\n"
    "v_cvt_pk_bf16_f32 v157, v242, v243\n"
    "v_accvgpr_write_b32 a60, v156\n"
    "v_pk_mul_f32 v[246:247], v[246:247], s[60:61] op_sel_hi:[1,0]\n"
    "v_cvt_pk_bf16_f32 v158, v244, v245\n"
    "v_accvgpr_write_b32 a61, v157\n"
    "v_cvt_pk_bf16_f32 v159, v246, v247\n"
    "v_accvgpr_write_b32 a62, v158\n"
    "v_accvgpr_write_b32 a63, v159\n"
    "v_mov_b32 v244, 0xc1500000\n"
    "v_mov_b32 v245, 0xc1500000\n"
    "v_mov_b32 v246, 0xc1500000\n"
    "v_mov_b32 v247, 0xc1500000\n"
    "v_cndmask_b32 v236, v244, v248, s[80:81]\n"
    "v_cndmask_b32 v237, v244, v248, s[82:83]\n"
    "v_cndmask_b32 v238, v244, v248, s[84:85]\n"
    "v_cndmask_b32 v239, v244, v248, s[86:87]\n"
    "s_mov_b32 s35, 1\n"
    "s_sub_u32 s65, 0, s70\n"
    "s_waitcnt lgkmcnt(0)\n"
    "ds_read_b128 a[128:131], v176 offset:32768\n"
    "ds_read_b128 a[132:135], v177 offset:32768\n"
    "ds_read_b128 a[136:139], v178 offset:32768\n"
    "ds_read_b128 a[140:143], v179 offset:32768\n"
    "ds_read_b128 a[144:147], v176 offset:33024\n"
    "ds_read_b128 a[148:151], v177 offset:33024\n"
    "ds_read_b128 a[152:155], v178 offset:33024\n"
    "ds_read_b128 a[156:159], v179 offset:33024\n"
    "s_waitcnt lgkmcnt(6)\n"
    "v_mfma_f32_16x16x32_bf16 v[128:131], a[128:131], a[0:3], v[236:239]\n"
    "v_mov_b32 v252, 0xc1500000\n"
    "v_mov_b32 v253, 0xc1500000\n"
    "v_mfma_f32_16x16x32_bf16 v[132:135], a[128:131], a[4:7], v[244:247]\n"
    "ds_read_b128 a[128:131], v176 offset:40960\n"
    "v_mov_b32 v254, 0xc1500000\n"
    "v_mfma_f32_16x16x32_bf16 v[128:131], a[132:135], a[8:11], v[128:131]\n"
    "v_mov_b32 v255, 0xc1500000\n"
    "v_mov_b32 v196, 0x41500000\n"
    "v_mfma_f32_16x16x32_bf16 v[132:135], a[132:135], a[12:15], v[132:135]\n"
    "s_waitcnt lgkmcnt(6)\n"
    "ds_read_b128 a[132:135], v177 offset:40960\n"
    "v_mov_b32 v197, 0x41500000\n"
    "v_mfma_f32_16x16x32_bf16 v[128:131], a[136:139], a[16:19], v[128:131]\n"
    "v_mov_b32 v198, 0\n"
    "v_mov_b32 v199, 0\n"
    "v_mfma_f32_16x16x32_bf16 v[132:135], a[136:139], a[20:23], v[132:135]\n"
    "s_waitcnt lgkmcnt(6)\n"
    "ds_read_b128 a[136:139], v178 offset:40960\n"
    "v_mfma_f32_16x16x32_bf16 v[128:131], a[140:143], a[24:27], v[128:131]\n"
    "v_mfma_f32_16x16x32_bf16 v[132:135], a[140:143], a[28:31], v[132:135]\n"
    "s_waitcnt lgkmcnt(6)\n"
    "ds_read_b128 a[140:143], v179 offset:40960\n"
    "v_mfma_f32_16x16x32_bf16 v[128:131], a[144:147], a[32:35], v[128:131]\n"
    "v_mfma_f32_16x16x32_bf16 v[132:135], a[144:147], a[36:39], v[132:135]\n"
    "s_waitcnt lgkmcnt(6)\n"
    "ds_read_b128 a[144:147], v176 offset:41216\n"
    "v_mfma_f32_16x16x32_bf16 v[128:131], a[148:151], a[40:43], v[128:131]\n"
    "v_mfma_f32_16x16x32_bf16 v[132:135], a[148:151], a[44:47], v[132:135]\n"
    "s_waitcnt lgkmcnt(6)\n"
    "ds_read_b128 a[148:151], v177 offset:41216\n"
    "v_mfma_f32_16x16x32_bf16 v[128:131], a[152:155], a[48:51], v[128:131]\n"
    "v_mfma_f32_16x16x32_bf16 v[132:135], a[152:155], a[52:55], v[132:135]\n"
    "s_waitcnt lgkmcnt(6)\n"
    "ds_read_b128 a[152:155], v178 offset:41216\n"
    "v_mfma_f32_16x16x32_bf16 v[128:131], a[156:159], a[56:59], v[128:131]\n"
    "v_mfma_f32_16x16x32_bf16 v[132:135], a[156:159], a[60:63], v[132:135]\n"
    "s_waitcnt lgkmcnt(6)\n"
    "ds_read_b128 a[156:159], v179 offset:41216\n"
    "v_mfma_f32_16x16x32_bf16 v[136:139], a[128:131], a[0:3], v[248:251]\n"
    "v_mfma_f32_16x16x32_bf16 v[140:143], a[128:131], a[4:7], v[236:239]\n"
    "s_waitcnt lgkmcnt(6)\n"
    "s_add_u32 m0, s58, 16384\n"
    "s_nop 0\n"
    "buffer_load_dwordx4 v188, s[36:39], s55 offen offset:0 lds\n"
    "v_mfma_f32_16x16x32_bf16 v[136:139], a[132:135], a[8:11], v[136:139]\n"
    "v_mfma_f32_16x16x32_bf16 v[140:143], a[132:135], a[12:15], v[140:143]\n"
    "s_waitcnt lgkmcnt(5)\n"
    "buffer_load_dwordx4 v189, s[36:39], s55 offen offset:1024 lds\n"
    "v_mfma_f32_16x16x32_bf16 v[136:139], a[136:139], a[16:19], v[136:139]\n"
    "v_mov_b32 v236, 0xc1500000\n"
    "v_mov_b32 v237, 0xc1500000\n"
    "v_mfma_f32_16x16x32_bf16 v[140:143], a[136:139], a[20:23], v[140:143]\n"
    "s_waitcnt lgkmcnt(4)\n"
    "buffer_load_dwordx4 v190, s[36:39], s55 offen offset:2048 lds\n"
    "v_mfma_f32_16x16x32_bf16 v[136:139], a[140:143], a[24:27], v[136:139]\n"
    "v_mov_b32 v238, 0xc1500000\n"
    "v_mov_b32 v239, 0xc1500000\n"
    "v_mfma_f32_16x16x32_bf16 v[140:143], a[140:143], a[28:31], v[140:143]\n"
    "s_waitcnt lgkmcnt(3)\n"
    "buffer_load_dwordx4 v191, s[36:39], s55 offen offset:3072 lds\n"
    "v_mfma_f32_16x16x32_bf16 v[136:139], a[144:147], a[32:35], v[136:139]\n"
    "v_mov_b32 v248, 0xc1500000\n"
    "v_mov_b32 v249, 0xc1500000\n"
    "v_mfma_f32_16x16x32_bf16 v[140:143], a[144:147], a[36:39], v[140:143]\n"
    "s_waitcnt lgkmcnt(2)\n"
    "v_mov_b32 v250, 0xc1500000\n"
    "v_mov_b32 v251, 0xc1500000\n"
    "v_mfma_f32_16x16x32_bf16 v[136:139], a[148:151], a[40:43], v[136:139]\n"
    "v_min_f32 v204, v128, v132\n"
    "v_cmp_gt_f32 vcc, 0xc2540000, v204\n"
    "v_mfma_f32_16x16x32_bf16 v[140:143], a[148:151], a[44:47], v[140:143]\n"
    "s_waitcnt lgkmcnt(1)\n"
    "v_exp_f32 v240, v128\n"
    "v_mfma_f32_16x16x32_bf16 v[136:139], a[152:155], a[48:51], v[136:139]\n"
    "v_exp_f32 v241, v129\n"
    "v_mfma_f32_16x16x32_bf16 v[140:143], a[152:155], a[52:55], v[140:143]\n"
    "s_waitcnt lgkmcnt(0)\n"
    "v_exp_f32 v242, v130\n"
    "v_mfma_f32_16x16x32_bf16 v[136:139], a[156:159], a[56:59], v[136:139]\n"
    "v_add_f32 v216, v240, v241\n"
    "v_cvt_pk_bf16_f32 v160, v240, v241\n"
    "v_mfma_f32_16x16x32_bf16 v[140:143], a[156:159], a[60:63], v[140:143]\n"
    "v_exp_f32 v243, v131\n"
    "s_waitcnt vmcnt(12)\n"
    "s_barrier\n"
    "global_store_dword v213, v212, s[52:53] offset:64 sc1\n"
    "s_add_u32 m0, s58, 65536\n"
    "s_nop 0\n"
    "buffer_load_dwordx4 v192, s[40:43], s54 offen offset:0 lds\n"
    "ds_read_b128 a[128:131], v176 offset:49152\n"
    "ds_read_b128 a[132:135], v177 offset:49152\n"
    "s_and_b32 s72, vcc_lo, 0xffff\n"
    "s_cbranch_scc1 .Llow_30\n"
    ".Llowb_31:\n"
    "v_add_f32 v216, v216, v242\n"
    "ds_read_b128 a[136:139], v178 offset:49152\n"
    "v_exp_f32 v240, v132\n"
    "buffer_load_dwordx4 v193, s[40:43], s54 offen offset:1024 lds\n"
    "v_add_f32 v216, v216, v243\n"
    "ds_read_b128 a[140:143], v179 offset:49152\n"
    "v_cvt_pk_bf16_f32 v161, v242, v243\n"
    "buffer_load_dwordx4 v194, s[40:43], s54 offen offset:2048 lds\n"
    "v_exp_f32 v241, v133\n"
    "ds_read_b128 a[144:147], v176 offset:49408\n"
    "v_exp_f32 v242, v134\n"
    "buffer_load_dwordx4 v195, s[40:43], s54 offen offset:3072 lds\n"
    "s_add_u32 s59, s59, 1\n"
    "v_readlane_b32 s54, v211, s59\n"
    "v_add_f32 v217, v240, v241\n"
    "ds_read_b128 a[148:151], v177 offset:49408\n"
    "v_cvt_pk_bf16_f32 v164, v240, v241\n"
    "ds_read_b128 a[152:155], v178 offset:49408\n"
    "v_exp_f32 v243, v135\n"
    "ds_read_b128 a[156:159], v179 offset:49408\n"
    "v_add_f32 v217, v217, v242\n"
    "v_exp_f32 v240, v136\n"
    "v_add_f32 v217, v217, v243\n"
    "v_cvt_pk_bf16_f32 v165, v242, v243\n"
    "v_exp_f32 v241, v137\n"
    "v_add_f32 v216, v216, v240\n"
    "v_exp_f32 v242, v138\n"
    "v_add_f32 v216, v216, v241\n"
    "v_cvt_pk_bf16_f32 v162, v240, v241\n"
    "s_waitcnt lgkmcnt(6)\n"
    "s_add_u32 s65, s65, 1\n"
    "s_cbranch_scc1 .LX2\n"
    "s_branch .LG3\n"
    ".LX3:\n"
    "s_cmp_lg_u32 s63, 0\n"
    "s_cbranch_scc1 .Lev_32\n"
    "s_cmp_lg_u32 s35, 0\n"
    "s_cbranch_scc1 .Lev_33\n"
    "global_load_dword v212, v213, s[52:53] offset:0 sc1\n"
    "s_waitcnt lgkmcnt(0)\n"
    "ds_read_b64_tr_b16 a[160:161], v180 offset:49152\n"
    "ds_read_b64_tr_b16 a[162:163], v180 offset:57344\n"
    "ds_read_b64_tr_b16 a[164:165], v181 offset:49152\n"
    "ds_read_b64_tr_b16 a[166:167], v181 offset:57344\n"
    "ds_read_b64_tr_b16 a[168:169], v182 offset:49152\n"
    "ds_read_b64_tr_b16 a[170:171], v182 offset:57344\n"
    "ds_read_b64_tr_b16 a[172:173], v183 offset:49152\n"
    "ds_read_b64_tr_b16 a[174:175], v183 offset:57344\n"
    "ds_read_b64_tr_b16 a[176:177], v184 offset:49152\n"
    "ds_read_b64_tr_b16 a[178:179], v184 offset:57344\n"
    "ds_read_b64_tr_b16 a[180:181], v185 offset:49152\n"
    "ds_read_b64_tr_b16 a[182:183], v185 offset:57344\n"
    "ds_read_b64_tr_b16 a[184:185], v186 offset:49152\n"
    "ds_read_b64_tr_b16 a[186:187], v186 offset:57344\n"
    "v_exp_f32 v243, v155\n"
    "v_add_f32 v216, v216, v242\n"
    "v_exp_f32 v240, v156\n"
    "v_add_f32 v216, v216, v243\n"
    "v_cvt_pk_bf16_f32 v171, v242, v243\n"
    "v_exp_f32 v241, v157\n"
    "v_add_f32 v217, v217, v240\n"
    "v_exp_f32 v242, v158\n"
    "v_add_f32 v217, v217, v241\n"
    "v_exp_f32 v243, v159\n"
    "v_add_f32 v217, v217, v242\n"
    "v_cvt_pk_bf16_f32 v174, v240, v241\n"
    "v_add_f32 v217, v217, v243\n"
    "v_add_f32 v215, v216, v217\n"
    "v_cmp_lt_f32 vcc, s29, v215\n"
    "v_cvt_pk_bf16_f32 v175, v242, v243\n"
    "s_cbranch_vccnz .Lstub_34\n"
    ".Lstubb_35:\n"
    "v_add_f32 v198, v198, v216\n"
    "v_add_f32 v199, v199, v217\n"
    "ds_read_b64_tr_b16 a[188:189], v187 offset:49152\n"
    "s_waitcnt lgkmcnt(11)\n"
    "ds_read_b64_tr_b16 a[190:191], v187 offset:57344\n"
    "v_mfma_f32_16x16x32_bf16 v[0:3], a[160:163], v[168:171], v[0:3]\n"
    "v_mfma_f32_16x16x32_bf16 v[4:7], a[160:163], v[172:175], v[4:7]\n"
    "ds_read_b64_tr_b16 a[160:161], v180 offset:49408\n"
    "v_mfma_f32_16x16x32_bf16 v[8:11], a[164:167], v[168:171], v[8:11]\n"
    "ds_read_b64_tr_b16 a[162:163], v180 offset:57600\n"
    "v_mfma_f32_16x16x32_bf16 v[12:15], a[164:167], v[172:175], v[12:15]\n"
    "ds_read_b64_tr_b16 a[164:165], v181 offset:49408\n"
    "s_waitcnt lgkmcnt(13)\n"
    "v_mfma_f32_16x16x32_bf16 v[16:19], a[168:171], v[168:171], v[16:19]\n"
    "ds_read_b64_tr_b16 a[166:167], v181 offset:57600\n"
    "v_mfma_f32_16x16x32_bf16 v[20:23], a[168:171], v[172:175], v[20:23]\n"
    "ds_read_b64_tr_b16 a[168:169], v182 offset:49408\n"
    "v_cvt_pk_bf16_f32 v0, v0, v1\n"
    "s_waitcnt lgkmcnt(13)\n"
    "v_mfma_f32_16x16x32_bf16 v[24:27], a[172:175], v[168:171], v[24:27]\n"
    "ds_read_b64_tr_b16 a[170:171], v182 offset:57600\n"
    "v_cvt_pk_bf16_f32 v1, v2, v3\n"
    "v_mfma_f32_16x16x32_bf16 v[28:31], a[172:175], v[172:175], v[28:31]\n"
    "ds_read_b64_tr_b16 a[172:173], v183 offset:49408\n"
    "v_cvt_pk_bf16_f32 v2, v4, v5\n"
    "s_waitcnt lgkmcnt(13)\n"
    "v_mfma_f32_16x16x32_bf16 v[32:35], a[176:179], v[168:171], v[32:35]\n"
    "ds_read_b64_tr_b16 a[174:175], v183 offset:57600\n"
    "v_cvt_pk_bf16_f32 v3, v6, v7\n"
    "v_mfma_f32_16x16x32_bf16 v[36:39], a[176:179], v[172:175], v[36:39]\n"
    "ds_read_b64_tr_b16 a[176:177], v184 offset:49408\n"
    "v_cvt_pk_bf16_f32 v8, v8, v9\n"
    "s_waitcnt lgkmcnt(13)\n"
    "v_mfma_f32_16x16x32_bf16 v[40:43], a[180:183], v[168:171], v[40:43]\n"
    "ds_read_b64_tr_b16 a[178:179], v184 offset:57600\n"
    "s_waitcnt vmcnt(9)\n"
    "ds_read_b128 v[128:131], v224 offset:0\n"
    "s_waitcnt lgkmcnt(11)\n"
    "ds_read_b128 v[132:135], v224 offset:4096\n"
    "ds_read_b128 v[136:139], v225 offset:0\n"
    "ds_read_b128 v[140:143], v225 offset:4096\n"
    "ds_read_b128 v[144:147], v226 offset:0\n"
    "s_waitcnt lgkmcnt(11)\n"
    "ds_read_b128 v[148:151], v226 offset:4096\n"
    "ds_read_b128 v[152:155], v227 offset:0\n"
    "ds_read_b128 v[156:159], v227 offset:4096\n"
    "v_cvt_pk_bf16_f32 v9, v10, v11\n"
    "v_mfma_f32_16x16x32_bf16 v[44:47], a[180:183], v[172:175], v[44:47]\n"
    "ds_read_b64_tr_b16 a[180:181], v185 offset:49408\n"
    "v_cvt_pk_bf16_f32 v10, v12, v13\n"
    "v_mfma_f32_16x16x32_bf16 v[48:51], a[184:187], v[168:171], v[48:51]\n"
    "s_waitcnt lgkmcnt(11)\n"
    "ds_read_b64_tr_b16 a[182:183], v185 offset:57600\n"
    "v_cvt_pk_bf16_f32 v11, v14, v15\n"
    "v_mfma_f32_16x16x32_bf16 v[52:55], a[184:187], v[172:175], v[52:55]\n"
    "ds_read_b64_tr_b16 a[184:185], v186 offset:49408\n"
    "v_cvt_pk_bf16_f32 v16, v16, v17\n"
    "v_mfma_f32_16x16x32_bf16 v[56:59], a[188:191], v[168:171], v[56:59]\n"
    "ds_read_b64_tr_b16 a[186:187], v186 offset:57600\n"
    "s_waitcnt vmcnt(9)\n"
    "s_barrier\n"
    "v_cvt_pk_bf16_f32 v17, v18, v19\n"
    "s_add_u32 m0, s58, 98304\n"
    "s_nop 0\n"
    "buffer_load_dwordx4 v192, s[40:43], s54 offen offset:0 lds\n"
    "v_mfma_f32_16x16x32_bf16 v[60:63], a[188:191], v[172:175], v[60:63]\n"
    "ds_read_b64_tr_b16 a[188:189], v187 offset:49408\n"
    "buffer_load_dwordx4 v193, s[40:43], s54 offen offset:1024 lds\n"
    "v_cvt_pk_bf16_f32 v18, v20, v21\n"
    "v_mfma_f32_16x16x32_bf16 v[64:67], a[160:163], v[168:171], v[64:67]\n"
    "buffer_load_dwordx4 v194, s[40:43], s54 offen offset:2048 lds\n"
    "s_waitcnt lgkmcnt(11)\n"
    "ds_read_b64_tr_b16 a[190:191], v187 offset:57600\n"
    "s_waitcnt lgkmcnt(6)\n"
    "v_cvt_pk_bf16_f32 v19, v22, v23\n"
    "buffer_load_dwordx4 v195, s[40:43], s54 offen offset:3072 lds\n"
    "s_add_u32 s59, s59, 1\n"
    "v_readlane_b32 s54, v211, s59\n"
    "v_mfma_f32_16x16x32_bf16 v[68:71], a[160:163], v[172:175], v[68:71]\n"
    "v_cvt_pk_bf16_f32 v24, v24, v25\n"
    "s_add_u32 m0, s58, 49152\n"
    "s_nop 0\n"
    "buffer_load_dwordx4 v188, s[36:39], s55 offen offset:0 lds\n"
    "v_cvt_pk_bf16_f32 v25, v26, v27\n"
    "v_mfma_f32_16x16x32_bf16 v[72:75], a[164:167], v[168:171], v[72:75]\n"
    "buffer_load_dwordx4 v189, s[36:39], s55 offen offset:1024 lds\n"
    "v_cvt_pk_bf16_f32 v26, v28, v29\n"
    "v_cvt_pk_bf16_f32 v27, v30, v31\n"
    "buffer_load_dwordx4 v190, s[36:39], s55 offen offset:2048 lds\n"
    "v_mfma_f32_16x16x32_bf16 v[76:79], a[164:167], v[172:175], v[76:79]\n"
    "v_cvt_pk_bf16_f32 v32, v32, v33\n"
    "buffer_load_dwordx4 v191, s[36:39], s55 offen offset:3072 lds\n"
    "v_cvt_pk_bf16_f32 v33, v34, v35\n"
    "v_mfma_f32_16x16x32_bf16 v[80:83], a[168:171], v[168:171], v[80:83]\n"
    "s_add_u32 m0, s58, 0\n"
    "s_nop 0\n"
    "buffer_load_dwordx4 v188, s[36:39], s54 offen offset:0 lds\n"
    "v_cvt_pk_bf16_f32 v34, v36, v37\n"
    "v_cvt_pk_bf16_f32 v35, v38, v39\n"
    "buffer_load_dwordx4 v189, s[36:39], s54 offen offset:1024 lds\n"
    "v_mfma_f32_16x16x32_bf16 v[84:87], a[168:171], v[172:175], v[84:87]\n"
    "v_cvt_pk_bf16_f32 v40, v40, v41\n"
    "buffer_load_dwordx4 v190, s[36:39], s54 offen offset:2048 lds\n"
    "v_cvt_pk_bf16_f32 v41, v42, v43\n"
    "v_mfma_f32_16x16x32_bf16 v[88:91], a[172:175], v[168:171], v[88:91]\n"
    "buffer_load_dwordx4 v191, s[36:39], s54 offen offset:3072 lds\n"
    "v_cvt_pk_bf16_f32 v42, v44, v45\n"
    "v_cvt_pk_bf16_f32 v43, v46, v47\n"
    "s_add_u32 m0, s91, 0\n"
    "s_mov_b32 s75, 256\n"
    "buffer_load_dwordx4 v228, s[92:95], s75 offen offset:0 lds\n"
    "v_mfma_f32_16x16x32_bf16 v[92:95], a[172:175], v[172:175], v[92:95]\n"
    "v_cvt_pk_bf16_f32 v48, v48, v49\n"
    "buffer_load_dwordx4 v229, s[92:95], s75 offen offset:1024 lds\n"
    "v_cvt_pk_bf16_f32 v49, v50, v51\n"
    "v_mfma_f32_16x16x32_bf16 v[96:99], a[176:179], v[168:171], v[96:99]\n"
    "buffer_load_dwordx4 v230, s[92:95], s75 offen offset:2048 lds\n"
    "v_cvt_pk_bf16_f32 v50, v52, v53\n"
    "v_cvt_pk_bf16_f32 v51, v54, v55\n"
    "buffer_load_dwordx4 v231, s[92:95], s75 offen offset:3072 lds\n"
    "v_mfma_f32_16x16x32_bf16 v[100:103], a[176:179], v[172:175], v[100:103]\n"
    "v_cvt_pk_bf16_f32 v56, v56, v57\n"
    "s_add_u32 m0, s91, 4096\n"
    "s_mov_b32 s75, 8448\n"
    "buffer_load_dwordx4 v228, s[92:95], s75 offen offset:0 lds\n"
    "v_cvt_pk_bf16_f32 v57, v58, v59\n"
    "s_waitcnt lgkmcnt(4)\n"
    "v_mfma_f32_16x16x32_bf16 v[104:107], a[180:183], v[168:171], v[104:107]\n"
    "buffer_load_dwordx4 v229, s[92:95], s75 offen offset:1024 lds\n"
    "v_cvt_pk_bf16_f32 v58, v60, v61\n"
    "v_cvt_pk_bf16_f32 v59, v62, v63\n"
    "buffer_load_dwordx4 v230, s[92:95], s75 offen offset:2048 lds\n"
    "v_mfma_f32_16x16x32_bf16 v[108:111], a[180:183], v[172:175], v[108:111]\n"
    "v_cvt_pk_bf16_f32 v64, v64, v65\n"
    "buffer_load_dwordx4 v231, s[92:95], s75 offen offset:3072 lds\n"
    "v_cvt_pk_bf16_f32 v65, v66, v67\n"
    "s_waitcnt lgkmcnt(2)\n"
    "v_mfma_f32_16x16x32_bf16 v[112:115], a[184:187], v[168:171], v[112:115]\n"
    "v_cvt_pk_bf16_f32 v66, v68, v69\n"
    "v_cvt_pk_bf16_f32 v67, v70, v71\n"
    "v_mfma_f32_16x16x32_bf16 v[116:119], a[184:187], v[172:175], v[116:119]\n"
    "v_cvt_pk_bf16_f32 v72, v72, v73\n"
    "v_cvt_pk_bf16_f32 v73, v74, v75\n"
    "s_waitcnt lgkmcnt(0)\n"
    "v_mfma_f32_16x16x32_bf16 v[120:123], a[188:191], v[168:171], v[120:123]\n"
    "v_cvt_pk_bf16_f32 v74, v76, v77\n"
    "v_cvt_pk_bf16_f32 v75, v78, v79\n"
    "v_mfma_f32_16x16x32_bf16 v[124:127], a[188:191], v[172:175], v[124:127]\n"
    "v_cvt_pk_bf16_f32 v80, v80, v81\n"
    "v_cvt_pk_bf16_f32 v81, v82, v83\n"
    "s_nop 3\n"
    "v_cvt_pk_bf16_f32 v82, v84, v85\n"
    "v_cvt_pk_bf16_f32 v83, v86, v87\n"
    "global_store_dwordx4 v214, v[196:199], s[44:45] offset:0 sc1\n"
    "v_cvt_pk_bf16_f32 v88, v88, v89\n"
    "v_cvt_pk_bf16_f32 v89, v90, v91\n"
    "global_store_dwordx4 v232, v[0:3], s[50:51] offset:0 sc1\n"
    "v_cvt_pk_bf16_f32 v90, v92, v93\n"
    "v_cvt_pk_bf16_f32 v91, v94, v95\n"
    "global_store_dwordx4 v232, v[8:11], s[50:51] offset:2048 sc1\n"
    "v_cvt_pk_bf16_f32 v96, v96, v97\n"
    "v_cvt_pk_bf16_f32 v97, v98, v99\n"
    "global_store_dwordx4 v233, v[16:19], s[50:51] offset:0 sc1\n"
    "v_cvt_pk_bf16_f32 v98, v100, v101\n"
    "v_cvt_pk_bf16_f32 v99, v102, v103\n"
    "global_store_dwordx4 v233, v[24:27], s[50:51] offset:2048 sc1\n"
    "v_cvt_pk_bf16_f32 v104, v104, v105\n"
    "v_cvt_pk_bf16_f32 v105, v106, v107\n"
    "global_store_dwordx4 v234, v[32:35], s[50:51] offset:0 sc1\n"
    "v_cvt_pk_bf16_f32 v106, v108, v109\n"
    "v_cvt_pk_bf16_f32 v107, v110, v111\n"
    "global_store_dwordx4 v234, v[40:43], s[50:51] offset:2048 sc1\n"
    "v_cvt_pk_bf16_f32 v112, v112, v113\n"
    "v_cvt_pk_bf16_f32 v113, v114, v115\n"
    "global_store_dwordx4 v235, v[48:51], s[50:51] offset:0 sc1\n"
    "v_cvt_pk_bf16_f32 v114, v116, v117\n"
    "v_cvt_pk_bf16_f32 v115, v118, v119\n"
    "global_store_dwordx4 v235, v[56:59], s[50:51] offset:2048 sc1\n"
    "v_cvt_pk_bf16_f32 v120, v120, v121\n"
    "v_cvt_pk_bf16_f32 v121, v122, v123\n"
    "global_store_dwordx4 v232, v[64:67], s[50:51] offset:256 sc1\n"
    "v_cvt_pk_bf16_f32 v122, v124, v125\n"
    "v_cvt_pk_bf16_f32 v123, v126, v127\n"
    "global_store_dwordx4 v232, v[72:75], s[50:51] offset:2304 sc1\n"
    "v_mov_b32 v252, 0\n"
    "v_mov_b32 v253, 0\n"
    "v_mov_b32 v254, 0\n"
    "v_mov_b32 v255, 0\n"
    "s_nop 1\n"
    "global_store_dwordx4 v233, v[80:83], s[50:51] offset:256 sc1\n"
    "v_mfma_f32_32x32x16_bf16 v[0:15], v[252:255], v[252:255], 0\n"
    "global_store_dwordx4 v233, v[88:91], s[50:51] offset:2304 sc1\n"
    "v_mfma_f32_32x32x16_bf16 v[16:31], v[252:255], v[252:255], 0\n"
    "global_store_dwordx4 v234, v[96:99], s[50:51] offset:256 sc1\n"
    "v_mfma_f32_32x32x16_bf16 v[32:47], v[252:255], v[252:255], 0\n"
    "global_store_dwordx4 v234, v[104:107], s[50:51] offset:2304 sc1\n"
    "v_mfma_f32_32x32x16_bf16 v[48:63], v[252:255], v[252:255], 0\n"
    "global_store_dwordx4 v235, v[112:115], s[50:51] offset:256 sc1\n"
    "s_waitcnt vmcnt(37) lgkmcnt(0)\n"
    "s_barrier\n"
    "v_mfma_f32_32x32x16_bf16 v[64:79], v[252:255], v[252:255], 0\n"
    "s_add_u32 m0, s58, 114688\n"
    "s_nop 0\n"
    "buffer_load_dwordx4 v192, s[40:43], s55 offen offset:0 lds\n"
    "global_store_dwordx4 v235, v[120:123], s[50:51] offset:2304 sc1\n"
    "v_mfma_f32_32x32x16_bf16 v[80:95], v[252:255], v[252:255], 0\n"
    "buffer_load_dwordx4 v193, s[40:43], s55 offen offset:1024 lds\n"
    "s_nop 1\n"
    "v_mfma_f32_32x32x16_bf16 v[96:111], v[252:255], v[252:255], 0\n"
    "buffer_load_dwordx4 v194, s[40:43], s55 offen offset:2048 lds\n"
    "v_mfma_f32_32x32x16_bf16 v[112:127], v[252:255], v[252:255], 0\n"
    "v_lshlrev_b32 v240, 16, v128\n"
    "buffer_load_dwordx4 v195, s[40:43], s55 offen offset:3072 lds\n"
    "s_add_u32 s59, s59, 1\n"
    "v_readlane_b32 s55, v211, s59\n"
    "v_and_b32 v241, s64, v128\n"
    "v_lshlrev_b32 v242, 16, v129\n"
    "s_nop 3\n"
    "v_and_b32 v243, s64, v129\n"
    "v_pk_mul_f32 v[240:241], v[240:241], s[60:61] op_sel_hi:[1,0]\n"
    "s_add_u32 m0, s58, 16384\n"
    "s_nop 0\n"
    "buffer_load_dwordx4 v188, s[36:39], s55 offen offset:0 lds\n"
    "v_lshlrev_b32 v244, 16, v130\n"
    "v_and_b32 v245, s64, v130\n"
    "buffer_load_dwordx4 v189, s[36:39], s55 offen offset:1024 lds\n"
    "v_pk_mul_f32 v[242:243], v[242:243], s[60:61] op_sel_hi:[1,0]\n"
    "v_cvt_pk_bf16_f32 v128, v240, v241\n"
    "buffer_load_dwordx4 v190, s[36:39], s55 offen offset:2048 lds\n"
    "v_lshlrev_b32 v246, 16, v131\n"
    "v_and_b32 v247, s64, v131\n"
    "buffer_load_dwordx4 v191, s[36:39], s55 offen offset:3072 lds\n"
    "v_pk_mul_f32 v[244:245], v[244:245], s[60:61] op_sel_hi:[1,0]\n"
    "v_cvt_pk_bf16_f32 v129, v242, v243\n"
    "v_accvgpr_write_b32 a0, v128\n"
    "v_lshlrev_b32 v240, 16, v132\n"
    "v_and_b32 v241, s64, v132\n"
    "v_pk_mul_f32 v[246:247], v[246:247], s[60:61] op_sel_hi:[1,0]\n"
    "v_cvt_pk_bf16_f32 v130, v244, v245\n"
    "v_accvgpr_write_b32 a1, v129\n"
    "v_lshlrev_b32 v242, 16, v133\n"
    "v_and_b32 v243, s64, v133\n"
    "v_pk_mul_f32 v[240:241], v[240:241], s[60:61] op_sel_hi:[1,0]\n"
    "v_cvt_pk_bf16_f32 v131, v246, v247\n"
    "v_accvgpr_write_b32 a2, v130\n"
    "v_lshlrev_b32 v244, 16, v134\n"
    "v_and_b32 v245, s64, v134\n"
    "v_pk_mul_f32 v[242:243], v[242:243], s[60:61] op_sel_hi:[1,0]\n"
    "v_cvt_pk_bf16_f32 v132, v240, v241\n"
    "v_accvgpr_write_b32 a3, v131\n"
    "v_lshlrev_b32 v246, 16, v135\n"
    "v_and_b32 v247, s64, v135\n"
    "v_pk_mul_f32 v[244:245], v[244:245], s[60:61] op_sel_hi:[1,0]\n"
    "v_cvt_pk_bf16_f32 v133, v242, v243\n"
    "v_accvgpr_write_b32 a4, v132\n"
    "v_lshlrev_b32 v240, 16, v136\n"
    "v_and_b32 v241, s64, v136\n"
    "v_pk_mul_f32 v[246:247], v[246:247], s[60:61] op_sel_hi:[1,0]\n"
    "v_cvt_pk_bf16_f32 v134, v244, v245\n"
    "v_accvgpr_write_b32 a5, v133\n"
    "v_lshlrev_b32 v242, 16, v137\n"
    "v_and_b32 v243, s64, v137\n"
    "v_pk_mul_f32 v[240:241], v[240:241], s[60:61] op_sel_hi:[1,0]\n"
    "v_cvt_pk_bf16_f32 v135, v246, v247\n"
    "v_accvgpr_write_b32 a6, v134\n"
    "v_lshlrev_b32 v244, 16, v138\n"
    "v_and_b32 v245, s64, v138\n"
    "v_pk_mul_f32 v[242:243], v[242:243], s[60:61] op_sel_hi:[1,0]\n"
    "v_cvt_pk_bf16_f32 v136, v240, v241\n"
    "v_accvgpr_write_b32 a7, v135\n"
    "v_lshlrev_b32 v246, 16, v139\n"
    "v_and_b32 v247, s64, v139\n"
    "v_pk_mul_f32 v[244:245], v[244:245], s[60:61] op_sel_hi:[1,0]\n"
    "v_cvt_pk_bf16_f32 v137, v242, v243\n"
    "v_accvgpr_write_b32 a8, v136\n"
    "v_lshlrev_b32 v240, 16, v140\n"
    "v_and_b32 v241, s64, v140\n"
    "v_pk_mul_f32 v[246:247], v[246:247], s[60:61] op_sel_hi:[1,0]\n"
    "v_cvt_pk_bf16_f32 v138, v244, v245\n"
    "v_accvgpr_write_b32 a9, v137\n"
    "v_lshlrev_b32 v242, 16, v141\n"
    "v_and_b32 v243, s64, v141\n"
    "v_pk_mul_f32 v[240:241], v[240:241], s[60:61] op_sel_hi:[1,0]\n"
    "v_cvt_pk_bf16_f32 v139, v246, v247\n"
    "v_accvgpr_write_b32 a10, v138\n"
    "v_lshlrev_b32 v244, 16, v142\n"
    "v_and_b32 v245, s64, v142\n"
    "v_pk_mul_f32 v[242:243], v[242:243], s[60:61] op_sel_hi:[1,0]\n"
    "v_cvt_pk_bf16_f32 v140, v240, v241\n"
    "v_accvgpr_write_b32 a11, v139\n"
    "v_lshlrev_b32 v246, 16, v143\n"
    "v_and_b32 v247, s64, v143\n"
    "v_pk_mul_f32 v[244:245], v[244:245], s[60:61] op_sel_hi:[1,0]\n"
    "v_cvt_pk_bf16_f32 v141, v242, v243\n"
    "v_accvgpr_write_b32 a12, v140\n"
    "v_lshlrev_b32 v240, 16, v144\n"
    "v_and_b32 v241, s64, v144\n"
    "v_pk_mul_f32 v[246:247], v[246:247], s[60:61] op_sel_hi:[1,0]\n"
    "v_cvt_pk_bf16_f32 v142, v244, v245\n"
    "v_accvgpr_write_b32 a13, v141\n"
    "v_lshlrev_b32 v242, 16, v145\n"
    "v_and_b32 v243, s64, v145\n"
    "v_pk_mul_f32 v[240:241], v[240:241], s[60:61] op_sel_hi:[1,0]\n"
    "v_cvt_pk_bf16_f32 v143, v246, v247\n"
    "v_accvgpr_write_b32 a14, v142\n"
    "v_lshlrev_b32 v244, 16, v146\n"
    "v_and_b32 v245, s64, v146\n"
    "v_pk_mul_f32 v[242:243], v[242:243], s[60:61] op_sel_hi:[1,0]\n"
    "v_cvt_pk_bf16_f32 v144, v240, v241\n"
    "v_accvgpr_write_b32 a15, v143\n"
    "v_lshlrev_b32 v246, 16, v147\n"
    "v_and_b32 v247, s64, v147\n"
    "v_pk_mul_f32 v[244:245], v[244:245], s[60:61] op_sel_hi:[1,0]\n"
    "v_cvt_pk_bf16_f32 v145, v242, v243\n"
    "v_accvgpr_write_b32 a16, v144\n"
    "v_lshlrev_b32 v240, 16, v148\n"
    "v_and_b32 v241, s64, v148\n"
    "v_pk_mul_f32 v[246:247], v[246:247], s[60:61] op_sel_hi:[1,0]\n"
    "v_cvt_pk_bf16_f32 v146, v244, v245\n"
    "v_accvgpr_write_b32 a17, v145\n"
    "v_lshlrev_b32 v242, 16, v149\n"
    "v_and_b32 v243, s64, v149\n"
    "v_pk_mul_f32 v[240:241], v[240:241], s[60:61] op_sel_hi:[1,0]\n"
    "v_cvt_pk_bf16_f32 v147, v246, v247\n"
    "v_accvgpr_write_b32 a18, v146\n"
    "v_lshlrev_b32 v244, 16, v150\n"
    "v_and_b32 v245, s64, v150\n"
    "v_pk_mul_f32 v[242:243], v[242:243], s[60:61] op_sel_hi:[1,0]\n"
    "v_cvt_pk_bf16_f32 v148, v240, v241\n"
    "v_accvgpr_write_b32 a19, v147\n"
    "v_lshlrev_b32 v246, 16, v151\n"
    "v_and_b32 v247, s64, v151\n"
    "v_pk_mul_f32 v[244:245], v[244:245], s[60:61] op_sel_hi:[1,0]\n"
    "v_cvt_pk_bf16_f32 v149, v242, v243\n"
    "v_accvgpr_write_b32 a20, v148\n"
    "v_lshlrev_b32 v240, 16, v152\n"
    "v_and_b32 v241, s64, v152\n"
    "v_pk_mul_f32 v[246:247], v[246:247], s[60:61] op_sel_hi:[1,0]\n"
    "v_cvt_pk_bf16_f32 v150, v244, v245\n"
    "v_accvgpr_write_b32 a21, v149\n"
    "v_lshlrev_b32 v242, 16, v153\n"
    "v_and_b32 v243, s64, v153\n"
    "v_pk_mul_f32 v[240:241], v[240:241], s[60:61] op_sel_hi:[1,0]\n"
    "v_cvt_pk_bf16_f32 v151, v246, v247\n"
    "v_accvgpr_write_b32 a22, v150\n"
    "v_lshlrev_b32 v244, 16, v154\n"
    "v_and_b32 v245, s64, v154\n"
    "v_pk_mul_f32 v[242:243], v[242:243], s[60:61] op_sel_hi:[1,0]\n"
    "v_cvt_pk_bf16_f32 v152, v240, v241\n"
    "v_accvgpr_write_b32 a23, v151\n"
    "v_lshlrev_b32 v246, 16, v155\n"
    "v_and_b32 v247, s64, v155\n"
    "v_pk_mul_f32 v[244:245], v[244:245], s[60:61] op_sel_hi:[1,0]\n"
    "v_cvt_pk_bf16_f32 v153, v242, v243\n"
    "v_accvgpr_write_b32 a24, v152\n"
    "v_lshlrev_b32 v240, 16, v156\n"
    "v_and_b32 v241, s64, v156\n"
    "v_pk_mul_f32 v[246:247], v[246:247], s[60:61] op_sel_hi:[1,0]\n"
    "v_cvt_pk_bf16_f32 v154, v244, v245\n"
    "v_accvgpr_write_b32 a25, v153\n"
    "v_lshlrev_b32 v242, 16, v157\n"
    "v_and_b32 v243, s64, v157\n"
    "v_pk_mul_f32 v[240:241], v[240:241], s[60:61] op_sel_hi:[1,0]\n"
    "v_cvt_pk_bf16_f32 v155, v246, v247\n"
    "v_accvgpr_write_b32 a26, v154\n"
    "v_lshlrev_b32 v244, 16, v158\n"
    "v_and_b32 v245, s64, v158\n"
    "v_pk_mul_f32 v[242:243], v[242:243], s[60:61] op_sel_hi:[1,0]\n"
    "v_cvt_pk_bf16_f32 v156, v240, v241\n"
    "v_accvgpr_write_b32 a27, v155\n"
    "v_lshlrev_b32 v246, 16, v159\n"
    "v_and_b32 v247, s64, v159\n"
    "v_pk_mul_f32 v[244:245], v[244:245], s[60:61] op_sel_hi:[1,0]\n"
    "v_cvt_pk_bf16_f32 v157, v242, v243\n"
    "v_accvgpr_write_b32 a28, v156\n"
    "v_pk_mul_f32 v[246:247], v[246:247], s[60:61] op_sel_hi:[1,0]\n"
    "v_cvt_pk_bf16_f32 v158, v244, v245\n"
    "v_accvgpr_write_b32 a29, v157\n"
    "v_cvt_pk_bf16_f32 v159, v246, v247\n"
    "v_accvgpr_write_b32 a30, v158\n"
    "v_accvgpr_write_b32 a31, v159\n"
    "s_waitcnt vmcnt(25)\n"
    "ds_read_b128 v[128:131], v224 offset:0\n"
    "ds_read_b128 v[132:135], v224 offset:4096\n"
    "ds_read_b128 v[136:139], v225 offset:0\n"
    "ds_read_b128 v[140:143], v225 offset:4096\n"
    "ds_read_b128 v[144:147], v226 offset:0\n"
    "ds_read_b128 v[148:151], v226 offset:4096\n"
    "ds_read_b128 v[152:155], v227 offset:0\n"
    "ds_read_b128 v[156:159], v227 offset:4096\n"
    "s_waitcnt lgkmcnt(7)\n"
    "v_lshlrev_b32 v240, 16, v128\n"
    "v_and_b32 v241, s64, v128\n"
    "v_lshlrev_b32 v242, 16, v129\n"
    "v_and_b32 v243, s64, v129\n"
    "v_pk_mul_f32 v[240:241], v[240:241], s[60:61] op_sel_hi:[1,0]\n"
    "v_lshlrev_b32 v244, 16, v130\n"
    "v_and_b32 v245, s64, v130\n"
    "v_pk_mul_f32 v[242:243], v[242:243], s[60:61] op_sel_hi:[1,0]\n"
    "v_cvt_pk_bf16_f32 v128, v240, v241\n"
    "v_lshlrev_b32 v246, 16, v131\n"
    "v_and_b32 v247, s64, v131\n"
    "v_pk_mul_f32 v[244:245], v[244:245], s[60:61] op_sel_hi:[1,0]\n"
    "v_cvt_pk_bf16_f32 v129, v242, v243\n"
    "v_accvgpr_write_b32 a32, v128\n"
    "s_waitcnt lgkmcnt(6)\n"
    "v_lshlrev_b32 v240, 16, v132\n"
    "v_and_b32 v241, s64, v132\n"
    "v_pk_mul_f32 v[246:247], v[246:247], s[60:61] op_sel_hi:[1,0]\n"
    "v_cvt_pk_bf16_f32 v130, v244, v245\n"
    "v_accvgpr_write_b32 a33, v129\n"
    "v_lshlrev_b32 v242, 16, v133\n"
    "v_and_b32 v243, s64, v133\n"
    "v_pk_mul_f32 v[240:241], v[240:241], s[60:61] op_sel_hi:[1,0]\n"
    "v_cvt_pk_bf16_f32 v131, v246, v247\n"
    "v_accvgpr_write_b32 a34, v130\n"
    "s_waitcnt vmcnt(37) lgkmcnt(0)\n"
    "s_barrier\n"
    "v_lshlrev_b32 v244, 16, v134\n"
    "s_add_u32 m0, s58, 65536\n"
    "s_nop 0\n"
    "buffer_load_dwordx4 v192, s[40:43], s54 offen offset:0 lds\n"
    "v_and_b32 v245, s64, v134\n"
    "v_pk_mul_f32 v[242:243], v[242:243], s[60:61] op_sel_hi:[1,0]\n"
    "buffer_load_dwordx4 v193, s[40:43], s54 offen offset:1024 lds\n"
    "v_cvt_pk_bf16_f32 v132, v240, v241\n"
    "v_accvgpr_write_b32 a35, v131\n"
    "buffer_load_dwordx4 v194, s[40:43], s54 offen offset:2048 lds\n"
    "v_lshlrev_b32 v246, 16, v135\n"
    "v_and_b32 v247, s64, v135\n"
    "buffer_load_dwordx4 v195, s[40:43], s54 offen offset:3072 lds\n"
    "s_add_u32 s59, s59, 1\n"
    "v_readlane_b32 s54, v211, s59\n"
    "v_pk_mul_f32 v[244:245], v[244:245], s[60:61] op_sel_hi:[1,0]\n"
    "v_cvt_pk_bf16_f32 v133, v242, v243\n"
    "v_accvgpr_write_b32 a36, v132\n"
    "v_lshlrev_b32 v240, 16, v136\n"
    "v_and_b32 v241, s64, v136\n"
    "v_pk_mul_f32 v[246:247], v[246:247], s[60:61] op_sel_hi:[1,0]\n"
    "v_cvt_pk_bf16_f32 v134, v244, v245\n"
    "v_accvgpr_write_b32 a37, v133\n"
    "v_lshlrev_b32 v242, 16, v137\n"
    "v_and_b32 v243, s64, v137\n"
    "v_pk_mul_f32 v[240:241], v[240:241], s[60:61] op_sel_hi:[1,0]\n"
    "v_cvt_pk_bf16_f32 v135, v246, v247\n"
    "v_accvgpr_write_b32 a38, v134\n"
    "v_lshlrev_b32 v244, 16, v138\n"
    "v_and_b32 v245, s64, v138\n"
    "v_pk_mul_f32 v[242:243], v[242:243], s[60:61] op_sel_hi:[1,0]\n"
    "v_cvt_pk_bf16_f32 v136, v240, v241\n"
    "v_accvgpr_write_b32 a39, v135\n"
    "v_lshlrev_b32 v246, 16, v139\n"
    "v_and_b32 v247, s64, v139\n"
    "v_pk_mul_f32 v[244:245], v[244:245], s[60:61] op_sel_hi:[1,0]\n"
    "v_cvt_pk_bf16_f32 v137, v242, v243\n"
    "v_accvgpr_write_b32 a40, v136\n"
    "v_lshlrev_b32 v240, 16, v140\n"
    "v_and_b32 v241, s64, v140\n"
    "v_pk_mul_f32 v[246:247], v[246:247], s[60:61] op_sel_hi:[1,0]\n"
    "v_cvt_pk_bf16_f32 v138, v244, v245\n"
    "v_accvgpr_write_b32 a41, v137\n"
    "v_lshlrev_b32 v242, 16, v141\n"
    "v_and_b32 v243, s64, v141\n"
    "v_pk_mul_f32 v[240:241], v[240:241], s[60:61] op_sel_hi:[1,0]\n"
    "v_cvt_pk_bf16_f32 v139, v246, v247\n"
    "v_accvgpr_write_b32 a42, v138\n"
    "v_lshlrev_b32 v244, 16, v142\n"
    "v_and_b32 v245, s64, v142\n"
    "v_pk_mul_f32 v[242:243], v[242:243], s[60:61] op_sel_hi:[1,0]\n"
    "v_cvt_pk_bf16_f32 v140, v240, v241\n"
    "v_accvgpr_write_b32 a43, v139\n"
    "v_lshlrev_b32 v246, 16, v143\n"
    "v_and_b32 v247, s64, v143\n"
    "v_pk_mul_f32 v[244:245], v[244:245], s[60:61] op_sel_hi:[1,0]\n"
    "v_cvt_pk_bf16_f32 v141, v242, v243\n"
    "v_accvgpr_write_b32 a44, v140\n"
    "v_lshlrev_b32 v240, 16, v144\n"
    "v_and_b32 v241, s64, v144\n"
    "v_pk_mul_f32 v[246:247], v[246:247], s[60:61] op_sel_hi:[1,0]\n"
    "v_cvt_pk_bf16_f32 v142, v244, v245\n"
    "v_accvgpr_write_b32 a45, v141\n"
    "v_lshlrev_b32 v242, 16, v145\n"
    "v_and_b32 v243, s64, v145\n"
    "v_pk_mul_f32 v[240:241], v[240:241], s[60:61] op_sel_hi:[1,0]\n"
    "v_cvt_pk_bf16_f32 v143, v246, v247\n"
    "v_accvgpr_write_b32 a46, v142\n"
    "v_lshlrev_b32 v244, 16, v146\n"
    "v_and_b32 v245, s64, v146\n"
    "v_pk_mul_f32 v[242:243], v[242:243], s[60:61] op_sel_hi:[1,0]\n"
    "v_cvt_pk_bf16_f32 v144, v240, v241\n"
    "v_accvgpr_write_b32 a47, v143\n"
    "v_lshlrev_b32 v246, 16, v147\n"
    "v_and_b32 v247, s64, v147\n"
    "v_pk_mul_f32 v[244:245], v[244:245], s[60:61] op_sel_hi:[1,0]\n"
    "v_cvt_pk_bf16_f32 v145, v242, v243\n"
    "v_accvgpr_write_b32 a48, v144\n"
    "v_lshlrev_b32 v240, 16, v148\n"
    "v_and_b32 v241, s64, v148\n"
    "v_pk_mul_f32 v[246:247], v[246:247], s[60:61] op_sel_hi:[1,0]\n"
    "v_cvt_pk_bf16_f32 v146, v244, v245\n"
    "v_accvgpr_write_b32 a49, v145\n"
    "v_lshlrev_b32 v242, 16, v149\n"
    "v_and_b32 v243, s64, v149\n"
    "v_pk_mul_f32 v[240:241], v[240:241], s[60:61] op_sel_hi:[1,0]\n"
    "v_cvt_pk_bf16_f32 v147, v246, v247\n"
    "v_accvgpr_write_b32 a50, v146\n"
    "v_lshlrev_b32 v244, 16, v150\n"
    "v_and_b32 v245, s64, v150\n"
    "v_pk_mul_f32 v[242:243], v[242:243], s[60:61] op_sel_hi:[1,0]\n"
    "v_cvt_pk_bf16_f32 v148, v240, v241\n"
    "v_accvgpr_write_b32 a51, v147\n"
    "v_lshlrev_b32 v246, 16, v151\n"
    "v_and_b32 v247, s64, v151\n"
    "v_pk_mul_f32 v[244:245], v[244:245], s[60:61] op_sel_hi:[1,0]\n"
    "v_cvt_pk_bf16_f32 v149, v242, v243\n"
    "v_accvgpr_write_b32 a52, v148\n"
    "v_lshlrev_b32 v240, 16, v152\n"
    "v_and_b32 v241, s64, v152\n"
    "v_pk_mul_f32 v[246:247], v[246:247], s[60:61] op_sel_hi:[1,0]\n"
    "v_cvt_pk_bf16_f32 v150, v244, v245\n"
    "v_accvgpr_write_b32 a53, v149\n"
    "v_lshlrev_b32 v242, 16, v153\n"
    "v_and_b32 v243, s64, v153\n"
    "v_pk_mul_f32 v[240:241], v[240:241], s[60:61] op_sel_hi:[1,0]\n"
    "v_cvt_pk_bf16_f32 v151, v246, v247\n"
    "v_accvgpr_write_b32 a54, v150\n"
    "v_lshlrev_b32 v244, 16, v154\n"
    "v_and_b32 v245, s64, v154\n"
    "v_pk_mul_f32 v[242:243], v[242:243], s[60:61] op_sel_hi:[1,0]\n"
    "v_cvt_pk_bf16_f32 v152, v240, v241\n"
    "v_accvgpr_write_b32 a55, v151\n"
    "v_lshlrev_b32 v246, 16, v155\n"
    "v_and_b32 v247, s64, v155\n"
    "v_pk_mul_f32 v[244:245], v[244:245], s[60:61] op_sel_hi:[1,0]\n"
    "v_cvt_pk_bf16_f32 v153, v242, v243\n"
    "v_accvgpr_write_b32 a56, v152\n"
    "v_lshlrev_b32 v240, 16, v156\n"
    "v_and_b32 v241, s64, v156\n"
    "v_pk_mul_f32 v[246:247], v[246:247], s[60:61] op_sel_hi:[1,0]\n"
    "v_cvt_pk_bf16_f32 v154, v244, v245\n"
    "v_accvgpr_write_b32 a57, v153\n"
    "v_lshlrev_b32 v242, 16, v157\n"
    "v_and_b32 v243, s64, v157\n"
    "v_pk_mul_f32 v[240:241], v[240:241], s[60:61] op_sel_hi:[1,0]\n"
    "v_cvt_pk_bf16_f32 v155, v246, v247\n"
    "v_accvgpr_write_b32 a58, v154\n"
    "v_lshlrev_b32 v244, 16, v158\n"
    "v_and_b32 v245, s64, v158\n"
    "v_pk_mul_f32 v[242:243], v[242:243], s[60:61] op_sel_hi:[1,0]\n"
    "v_cvt_pk_bf16_f32 v156, v240, v241\n"
    "v_accvgpr_write_b32 a59, v155\n"
    "v_lshlrev_b32 v246, 16, v159\n"
    "v_and_b32 v247, s64, v159\n"
    "v_pk_mul_f32 v[244:245], v[244:245], s[60:61] op_sel_hi:[1,0]\n"
    "v_cvt_pk_bf16_f32 v157, v242, v243\n"
    "v_accvgpr_write_b32 a60, v156\n"
    "v_pk_mul_f32 v[246:247], v[246:247], s[60:61] op_sel_hi:[1,0]\n"
    "v_cvt_pk_bf16_f32 v158, v244, v245\n"
    "v_accvgpr_write_b32 a61, v157\n"
    "v_cvt_pk_bf16_f32 v159, v246, v247\n"
    "v_accvgpr_write_b32 a62, v158\n"
    "v_accvgpr_write_b32 a63, v159\n"
    "v_mov_b32 v244, 0xc1500000\n"
    "v_mov_b32 v245, 0xc1500000\n"
    "v_mov_b32 v246, 0xc1500000\n"
    "v_mov_b32 v247, 0xc1500000\n"
    "v_cndmask_b32 v236, v244, v248, s[80:81]\n"
    "v_cndmask_b32 v237, v244, v248, s[82:83]\n"
    "v_cndmask_b32 v238, v244, v248, s[84:85]\n"
    "v_cndmask_b32 v239, v244, v248, s[86:87]\n"
    "s_mov_b32 s35, 1\n"
    "s_sub_u32 s65, 0, s70\n"
    "s_waitcnt lgkmcnt(0)\n"
    "ds_read_b128 a[128:131], v176 offset:49152\n"
    "ds_read_b128 a[132:135], v177 offset:49152\n"
    "ds_read_b128 a[136:139], v178 offset:49152\n"
    "ds_read_b128 a[140:143], v179 offset:49152\n"
    "ds_read_b128 a[144:147], v176 offset:49408\n"
    "ds_read_b128 a[148:151], v177 offset:49408\n"
    "ds_read_b128 a[152:155], v178 offset:49408\n"
    "ds_read_b128 a[156:159], v179 offset:49408\n"
    "s_waitcnt lgkmcnt(6)\n"
    "v_mfma_f32_16x16x32_bf16 v[144:147], a[128:131], a[0:3], v[236:239]\n"
    "v_mov_b32 v252, 0xc1500000\n"
    "v_mov_b32 v253, 0xc1500000\n"
    "v_mfma_f32_16x16x32_bf16 v[148:151], a[128:131], a[4:7], v[244:247]\n"
    "ds_read_b128 a[128:131], v176 offset:57344\n"
    "v_mov_b32 v254, 0xc1500000\n"
    "v_mfma_f32_16x16x32_bf16 v[144:147], a[132:135], a[8:11], v[144:147]\n"
    "v_mov_b32 v255, 0xc1500000\n"
    "v_mov_b32 v196, 0x41500000\n"
    "v_mfma_f32_16x16x32_bf16 v[148:151], a[132:135], a[12:15], v[148:151]\n"
    "s_waitcnt lgkmcnt(6)\n"
    "ds_read_b128 a[132:135], v177 offset:57344\n"
    "v_mov_b32 v197, 0x41500000\n"
    "v_mfma_f32_16x16x32_bf16 v[144:147], a[136:139], a[16:19], v[144:147]\n"
    "v_mov_b32 v198, 0\n"
    "v_mov_b32 v199, 0\n"
    "v_mfma_f32_16x16x32_bf16 v[148:151], a[136:139], a[20:23], v[148:151]\n"
    "s_waitcnt lgkmcnt(6)\n"
    "ds_read_b128 a[136:139], v178 offset:57344\n"
    "v_mfma_f32_16x16x32_bf16 v[144:147], a[140:143], a[24:27], v[144:147]\n"
    "v_mfma_f32_16x16x32_bf16 v[148:151], a[140:143], a[28:31], v[148:151]\n"
    "s_waitcnt lgkmcnt(6)\n"
    "ds_read_b128 a[140:143], v179 offset:57344\n"
    "v_mfma_f32_16x16x32_bf16 v[144:147], a[144:147], a[32:35], v[144:147]\n"
    "v_mfma_f32_16x16x32_bf16 v[148:151], a[144:147], a[36:39], v[148:151]\n"
    "s_waitcnt lgkmcnt(6)\n"
    "ds_read_b128 a[144:147], v176 offset:57600\n"
    "v_mfma_f32_16x16x32_bf16 v[144:147], a[148:151], a[40:43], v[144:147]\n"
    "v_mfma_f32_16x16x32_bf16 v[148:151], a[148:151], a[44:47], v[148:151]\n"
    "s_waitcnt lgkmcnt(6)\n"
    "ds_read_b128 a[148:151], v177 offset:57600\n"
    "v_mfma_f32_16x16x32_bf16 v[144:147], a[152:155], a[48:51], v[144:147]\n"
    "v_mfma_f32_16x16x32_bf16 v[148:151], a[152:155], a[52:55], v[148:151]\n"
    "s_waitcnt lgkmcnt(6)\n"
    "ds_read_b128 a[152:155], v178 offset:57600\n"
    "v_mfma_f32_16x16x32_bf16 v[144:147], a[156:159], a[56:59], v[144:147]\n"
    "v_mfma_f32_16x16x32_bf16 v[148:151], a[156:159], a[60:63], v[148:151]\n"
    "s_waitcnt lgkmcnt(6)\n"
    "ds_read_b128 a[156:159], v179 offset:57600\n"
    "v_mfma_f32_16x16x32_bf16 v[152:155], a[128:131], a[0:3], v[248:251]\n"
    "v_mfma_f32_16x16x32_bf16 v[156:159], a[128:131], a[4:7], v[236:239]\n"
    "s_waitcnt lgkmcnt(6)\n"
    "s_add_u32 m0, s58, 32768\n"
    "s_nop 0\n"
    "buffer_load_dwordx4 v188, s[36:39], s54 offen offset:0 lds\n"
    "v_mfma_f32_16x16x32_bf16 v[152:155], a[132:135], a[8:11], v[152:155]\n"
    "v_mfma_f32_16x16x32_bf16 v[156:159], a[132:135], a[12:15], v[156:159]\n"
    "s_waitcnt lgkmcnt(5)\n"
    "buffer_load_dwordx4 v189, s[36:39], s54 offen offset:1024 lds\n"
    "v_mfma_f32_16x16x32_bf16 v[152:155], a[136:139], a[16:19], v[152:155]\n"
    "v_mov_b32 v236, 0xc1500000\n"
    "v_mov_b32 v237, 0xc1500000\n"
    "v_mfma_f32_16x16x32_bf16 v[156:159], a[136:139], a[20:23], v[156:159]\n"
    "s_waitcnt lgkmcnt(4)\n"
    "buffer_load_dwordx4 v190, s[36:39], s54 offen offset:2048 lds\n"
    "v_mfma_f32_16x16x32_bf16 v[152:155], a[140:143], a[24:27], v[152:155]\n"
    "v_mov_b32 v238, 0xc1500000\n"
    "v_mov_b32 v239, 0xc1500000\n"
    "v_mfma_f32_16x16x32_bf16 v[156:159], a[140:143], a[28:31], v[156:159]\n"
    "s_waitcnt lgkmcnt(3)\n"
    "buffer_load_dwordx4 v191, s[36:39], s54 offen offset:3072 lds\n"
    "v_mfma_f32_16x16x32_bf16 v[152:155], a[144:147], a[32:35], v[152:155]\n"
    "v_mov_b32 v248, 0xc1500000\n"
    "v_mov_b32 v249, 0xc1500000\n"
    "v_mfma_f32_16x16x32_bf16 v[156:159], a[144:147], a[36:39], v[156:159]\n"
    "s_waitcnt lgkmcnt(2)\n"
    "v_mov_b32 v250, 0xc1500000\n"
    "v_mov_b32 v251, 0xc1500000\n"
    "v_mfma_f32_16x16x32_bf16 v[152:155], a[148:151], a[40:43], v[152:155]\n"
    "v_min_f32 v204, v144, v148\n"
    "v_cmp_gt_f32 vcc, 0xc2540000, v204\n"
    "v_mfma_f32_16x16x32_bf16 v[156:159], a[148:151], a[44:47], v[156:159]\n"
    "s_waitcnt lgkmcnt(1)\n"
    "v_exp_f32 v240, v144\n"
    "v_mfma_f32_16x16x32_bf16 v[152:155], a[152:155], a[48:51], v[152:155]\n"
    "v_exp_f32 v241, v145\n"
    "v_mfma_f32_16x16x32_bf16 v[156:159], a[152:155], a[52:55], v[156:159]\n"
    "s_waitcnt lgkmcnt(0)\n"
    "v_exp_f32 v242, v146\n"
    "v_mfma_f32_16x16x32_bf16 v[152:155], a[156:159], a[56:59], v[152:155]\n"
    "v_add_f32 v216, v240, v241\n"
    "v_cvt_pk_bf16_f32 v168, v240, v241\n"
    "v_mfma_f32_16x16x32_bf16 v[156:159], a[156:159], a[60:63], v[156:159]\n"
    "v_exp_f32 v243, v147\n"
    "s_waitcnt vmcnt(12)\n"
    "s_barrier\n"
    "global_store_dword v213, v212, s[52:53] offset:64 sc1\n"
    "s_add_u32 m0, s58, 81920\n"
    "s_nop 0\n"
    "buffer_load_dwordx4 v192, s[40:43], s55 offen offset:0 lds\n"
    "ds_read_b128 a[128:131], v176 offset:0\n"
    "ds_read_b128 a[132:135], v177 offset:0\n"
    "s_and_b32 s72, vcc_lo, 0xffff\n"
    "s_cbranch_scc1 .Llow_36\n"
    ".Llowb_37:\n"
    "v_add_f32 v216, v216, v242\n"
    "ds_read_b128 a[136:139], v178 offset:0\n"
    "v_exp_f32 v240, v148\n"
    "buffer_load_dwordx4 v193, s[40:43], s55 offen offset:1024 lds\n"
    "v_add_f32 v216, v216, v243\n"
    "ds_read_b128 a[140:143], v179 offset:0\n"
    "v_cvt_pk_bf16_f32 v169, v242, v243\n"
    "buffer_load_dwordx4 v194, s[40:43], s55 offen offset:2048 lds\n"
    "v_exp_f32 v241, v149\n"
    "ds_read_b128 a[144:147], v176 offset:256\n"
    "v_exp_f32 v242, v150\n"
    "buffer_load_dwordx4 v195, s[40:43], s55 offen offset:3072 lds\n"
    "s_add_u32 s59, s59, 1\n"
    "v_readlane_b32 s55, v211, s59\n"
    "v_add_f32 v217, v240, v241\n"
    "ds_read_b128 a[148:151], v177 offset:256\n"
    "v_cvt_pk_bf16_f32 v172, v240, v241\n"
    "ds_read_b128 a[152:155], v178 offset:256\n"
    "v_exp_f32 v243, v151\n"
    "ds_read_b128 a[156:159], v179 offset:256\n"
    "v_add_f32 v217, v217, v242\n"
    "v_exp_f32 v240, v152\n"
    "v_add_f32 v217, v217, v243\n"
    "v_cvt_pk_bf16_f32 v173, v242, v243\n"
    "v_exp_f32 v241, v153\n"
    "v_add_f32 v216, v216, v240\n"
    "v_exp_f32 v242, v154\n"
    "v_add_f32 v216, v216, v241\n"
    "v_cvt_pk_bf16_f32 v170, v240, v241\n"
    "s_waitcnt lgkmcnt(6)\n"
    "s_add_u32 s65, s65, 1\n"
    "s_cbranch_scc1 .LX3\n"
    "s_branch .LG0\n"
    ".LFP0:\n"
    "s_waitcnt lgkmcnt(0)\n"
    "v_add_u32 v180, s72, v180\n"
    "v_add_u32 v181, s72, v181\n"
    "v_add_u32 v182, s72, v182\n"
    "v_add_u32 v183, s72, v183\n"
    "v_add_u32 v184, s72, v184\n"
    "v_add_u32 v185, s72, v185\n"
    "v_add_u32 v186, s72, v186\n"
    "v_add_u32 v187, s72, v187\n"
    "ds_read_b64_tr_b16 a[160:161], v180 offset:0\n"
    "ds_read_b64_tr_b16 a[162:163], v180 offset:8192\n"
    "ds_read_b64_tr_b16 a[164:165], v181 offset:0\n"
    "ds_read_b64_tr_b16 a[166:167], v181 offset:8192\n"
    "ds_read_b64_tr_b16 a[168:169], v182 offset:0\n"
    "ds_read_b64_tr_b16 a[170:171], v182 offset:8192\n"
    "ds_read_b64_tr_b16 a[172:173], v183 offset:0\n"
    "ds_read_b64_tr_b16 a[174:175], v183 offset:8192\n"
    "s_cmp_eq_u32 s67, 1\n"
    "s_cbranch_scc1 .Lnp_39\n"
    ".Lnp_38:\n"
    "ds_read_b64_tr_b16 a[176:177], v184 offset:0\n"
    "ds_read_b64_tr_b16 a[178:179], v184 offset:8192\n"
    "ds_read_b64_tr_b16 a[180:181], v185 offset:0\n"
    "ds_read_b64_tr_b16 a[182:183], v185 offset:8192\n"
    "ds_read_b64_tr_b16 a[184:185], v186 offset:0\n"
    "ds_read_b64_tr_b16 a[186:187], v186 offset:8192\n"
    "v_exp_f32 v243, v139\n"
    "v_add_f32 v216, v216, v242\n"
    "v_exp_f32 v240, v140\n"
    "v_add_f32 v216, v216, v243\n"
    "v_cvt_pk_bf16_f32 v163, v242, v243\n"
    "v_exp_f32 v241, v141\n"
    "v_add_f32 v217, v217, v240\n"
    "v_exp_f32 v242, v142\n"
    "v_add_f32 v217, v217, v241\n"
    "v_exp_f32 v243, v143\n"
    "v_add_f32 v217, v217, v242\n"
    "v_cvt_pk_bf16_f32 v166, v240, v241\n"
    "v_add_f32 v217, v217, v243\n"
    "v_add_f32 v215, v216, v217\n"
    "v_cmp_lt_f32 vcc, s29, v215\n"
    "v_cvt_pk_bf16_f32 v167, v242, v243\n"
    "s_cbranch_vccnz .Lstub_41\n"
    ".Lstubb_42:\n"
    "v_add_f32 v198, v198, v216\n"
    "v_add_f32 v199, v199, v217\n"
    "v_mov_b32 v168, v160\n"
    "v_mov_b32 v169, v161\n"
    "v_mov_b32 v170, v162\n"
    "v_mov_b32 v171, v163\n"
    "v_mov_b32 v172, v164\n"
    "v_mov_b32 v173, v165\n"
    "v_mov_b32 v174, v166\n"
    "v_mov_b32 v175, v167\n"
    "s_branch .LFC\n"
    ".LFP1:\n"
    "s_waitcnt lgkmcnt(0)\n"
    "v_add_u32 v180, s72, v180\n"
    "v_add_u32 v181, s72, v181\n"
    "v_add_u32 v182, s72, v182\n"
    "v_add_u32 v183, s72, v183\n"
    "v_add_u32 v184, s72, v184\n"
    "v_add_u32 v185, s72, v185\n"
    "v_add_u32 v186, s72, v186\n"
    "v_add_u32 v187, s72, v187\n"
    "ds_read_b64_tr_b16 a[160:161], v180 offset:0\n"
    "ds_read_b64_tr_b16 a[162:163], v180 offset:8192\n"
    "ds_read_b64_tr_b16 a[164:165], v181 offset:0\n"
    "ds_read_b64_tr_b16 a[166:167], v181 offset:8192\n"
    "ds_read_b64_tr_b16 a[168:169], v182 offset:0\n"
    "ds_read_b64_tr_b16 a[170:171], v182 offset:8192\n"
    "ds_read_b64_tr_b16 a[172:173], v183 offset:0\n"
    "ds_read_b64_tr_b16 a[174:175], v183 offset:8192\n"
    "s_cmp_eq_u32 s67, 1\n"
    "s_cbranch_scc1 .Lnp_44\n"
    ".Lnp_43:\n"
    "ds_read_b64_tr_b16 a[176:177], v184 offset:0\n"
    "ds_read_b64_tr_b16 a[178:179], v184 offset:8192\n"
    "ds_read_b64_tr_b16 a[180:181], v185 offset:0\n"
    "ds_read_b64_tr_b16 a[182:183], v185 offset:8192\n"
    "ds_read_b64_tr_b16 a[184:185], v186 offset:0\n"
    "ds_read_b64_tr_b16 a[186:187], v186 offset:8192\n"
    "v_exp_f32 v243, v155\n"
    "v_add_f32 v216, v216, v242\n"
    "v_exp_f32 v240, v156\n"
    "v_add_f32 v216, v216, v243\n"
    "v_cvt_pk_bf16_f32 v171, v242, v243\n"
    "v_exp_f32 v241, v157\n"
    "v_add_f32 v217, v217, v240\n"
    "v_exp_f32 v242, v158\n"
    "v_add_f32 v217, v217, v241\n"
    "v_exp_f32 v243, v159\n"
    "v_add_f32 v217, v217, v242\n"
    "v_cvt_pk_bf16_f32 v174, v240, v241\n"
    "v_add_f32 v217, v217, v243\n"
    "v_add_f32 v215, v216, v217\n"
    "v_cmp_lt_f32 vcc, s29, v215\n"
    "v_cvt_pk_bf16_f32 v175, v242, v243\n"
    "s_cbranch_vccnz .Lstub_46\n"
    ".Lstubb_47:\n"
    "v_add_f32 v198, v198, v216\n"
    "v_add_f32 v199, v199, v217\n"
    ".LFC:\n"
    "s_cmp_eq_u32 s67, 1\n"
    "s_cbranch_scc1 .LFB\n"
    "ds_read_b64_tr_b16 a[188:189], v187 offset:0\n"
    "s_waitcnt lgkmcnt(11)\n"
    "ds_read_b64_tr_b16 a[190:191], v187 offset:8192\n"
    "v_mfma_f32_16x16x32_bf16 v[0:3], a[160:163], v[168:171], v[0:3]\n"
    "v_xor_b32 v153, 32, v220\n"
    "v_xor_b32 v154, 64, v220\n"
    "v_mfma_f32_16x16x32_bf16 v[4:7], a[160:163], v[172:175], v[4:7]\n"
    "ds_read_b64_tr_b16 a[160:161], v180 offset:256\n"
    "v_xor_b32 v155, 96, v220\n"
    "v_mfma_f32_16x16x32_bf16 v[8:11], a[164:167], v[168:171], v[8:11]\n"
    "ds_read_b64_tr_b16 a[162:163], v180 offset:8448\n"
    "v_xor_b32 v156, 128, v220\n"
    "v_mfma_f32_16x16x32_bf16 v[12:15], a[164:167], v[172:175], v[12:15]\n"
    "ds_read_b64_tr_b16 a[164:165], v181 offset:256\n"
    "v_xor_b32 v157, 160, v220\n"
    "s_waitcnt lgkmcnt(13)\n"
    "v_mfma_f32_16x16x32_bf16 v[16:19], a[168:171], v[168:171], v[16:19]\n"
    "ds_read_b64_tr_b16 a[166:167], v181 offset:8448\n"
    "v_xor_b32 v158, 192, v220\n"
    "v_mfma_f32_16x16x32_bf16 v[20:23], a[168:171], v[172:175], v[20:23]\n"
    "ds_read_b64_tr_b16 a[168:169], v182 offset:256\n"
    "v_xor_b32 v159, 224, v220\n"
    "s_waitcnt lgkmcnt(13)\n"
    "v_mfma_f32_16x16x32_bf16 v[24:27], a[172:175], v[168:171], v[24:27]\n"
    "ds_read_b64_tr_b16 a[170:171], v182 offset:8448\n"
    "v_xor_b32 v237, 128, v218\n"
    "s_cmp_eq_u32 s32, 0\n"
    "s_cbranch_scc1 .Lbp_48\n"
    "s_waitcnt vmcnt(8)\n"
    "s_barrier\n"
    "s_sub_u32 s32, s32, 1\n"
    "s_mov_b32 m0, s33\n"
    "s_nop 0\n"
    "buffer_load_dwordx4 v192, s[40:43], s34 offen offset:0 lds\n"
    "buffer_load_dwordx4 v193, s[40:43], s34 offen offset:1024 lds\n"
    "buffer_load_dwordx4 v194, s[40:43], s34 offen offset:2048 lds\n"
    "buffer_load_dwordx4 v195, s[40:43], s34 offen offset:3072 lds\n"
    ".Lbp_48:\n"
    "v_mfma_f32_16x16x32_bf16 v[28:31], a[172:175], v[172:175], v[28:31]\n"
    "ds_read_b64_tr_b16 a[172:173], v183 offset:256\n"
    "v_xor_b32 v238, 16, v218\n"
    "s_waitcnt lgkmcnt(13)\n"
    "v_mfma_f32_16x16x32_bf16 v[32:35], a[176:179], v[168:171], v[32:35]\n"
    "ds_read_b64_tr_b16 a[174:175], v183 offset:8448\n"
    "v_xor_b32 v239, 144, v218\n"
    "v_mfma_f32_16x16x32_bf16 v[36:39], a[176:179], v[172:175], v[36:39]\n"
    "ds_read_b64_tr_b16 a[176:177], v184 offset:256\n"
    "v_mov_b32 v204, v198\n"
    "s_waitcnt lgkmcnt(13)\n"
    "v_mfma_f32_16x16x32_bf16 v[40:43], a[180:183], v[168:171], v[40:43]\n"
    "ds_read_b64_tr_b16 a[178:179], v184 offset:8448\n"
    "v_mov_b32 v205, v199\n"
    "v_mfma_f32_16x16x32_bf16 v[44:47], a[180:183], v[172:175], v[44:47]\n"
    "ds_read_b64_tr_b16 a[180:181], v185 offset:256\n"
    "s_nop 0\n"
    "s_waitcnt lgkmcnt(13)\n"
    "v_mfma_f32_16x16x32_bf16 v[48:51], a[184:187], v[168:171], v[48:51]\n"
    "ds_read_b64_tr_b16 a[182:183], v185 offset:8448\n"
    "v_permlane32_swap_b32 v204, v198\n"
    "v_mfma_f32_16x16x32_bf16 v[52:55], a[184:187], v[172:175], v[52:55]\n"
    "ds_read_b64_tr_b16 a[184:185], v186 offset:256\n"
    "v_permlane32_swap_b32 v205, v199\n"
    "s_waitcnt lgkmcnt(13)\n"
    "v_mfma_f32_16x16x32_bf16 v[56:59], a[188:191], v[168:171], v[56:59]\n"
    "ds_read_b64_tr_b16 a[186:187], v186 offset:8448\n"
    "v_add_f32 v198, v198, v204\n"
    "v_mfma_f32_16x16x32_bf16 v[60:63], a[188:191], v[172:175], v[60:63]\n"
    "ds_read_b64_tr_b16 a[188:189], v187 offset:256\n"
    "v_add_f32 v199, v199, v205\n"
    "s_waitcnt lgkmcnt(13)\n"
    "v_mfma_f32_16x16x32_bf16 v[64:67], a[160:163], v[168:171], v[64:67]\n"
    "ds_read_b64_tr_b16 a[190:191], v187 offset:8448\n"
    "v_mov_b32 v204, v198\n"
    "v_mfma_f32_16x16x32_bf16 v[68:71], a[160:163], v[172:175], v[68:71]\n"
    "v_mov_b32 v205, v199\n"
    "s_nop 0\n"
    "s_waitcnt lgkmcnt(12)\n"
    "v_mfma_f32_16x16x32_bf16 v[72:75], a[164:167], v[168:171], v[72:75]\n"
    "v_permlane16_swap_b32 v204, v198\n"
    "v_mfma_f32_16x16x32_bf16 v[76:79], a[164:167], v[172:175], v[76:79]\n"
    "v_permlane16_swap_b32 v205, v199\n"
    "s_waitcnt lgkmcnt(10)\n"
    "v_mfma_f32_16x16x32_bf16 v[80:83], a[168:171], v[168:171], v[80:83]\n"
    "v_add_f32 v198, v198, v204\n"
    "v_add_f32 v199, v199, v205\n"
    "v_mfma_f32_16x16x32_bf16 v[84:87], a[168:171], v[172:175], v[84:87]\n"
    "v_rcp_f32 v252, v198\n"
    "s_waitcnt lgkmcnt(8)\n"
    "v_mfma_f32_16x16x32_bf16 v[88:91], a[172:175], v[168:171], v[88:91]\n"
    "v_rcp_f32 v253, v199\n"
    "v_mfma_f32_16x16x32_bf16 v[92:95], a[172:175], v[172:175], v[92:95]\n"
    "s_nop 0\n"
    "v_mul_f32 v0, v0, v252\n"
    "s_waitcnt lgkmcnt(6)\n"
    "v_mfma_f32_16x16x32_bf16 v[96:99], a[176:179], v[168:171], v[96:99]\n"
    "v_mul_f32 v1, v1, v252\n"
    "v_mul_f32 v2, v2, v252\n"
    "v_mfma_f32_16x16x32_bf16 v[100:103], a[176:179], v[172:175], v[100:103]\n"
    "v_mul_f32 v3, v3, v252\n"
    "v_mul_f32 v8, v8, v252\n"
    "s_waitcnt lgkmcnt(4)\n"
    "v_mfma_f32_16x16x32_bf16 v[104:107], a[180:183], v[168:171], v[104:107]\n"
    "v_mul_f32 v9, v9, v252\n"
    "v_mul_f32 v10, v10, v252\n"
    "v_mfma_f32_16x16x32_bf16 v[108:111], a[180:183], v[172:175], v[108:111]\n"
    "v_mul_f32 v11, v11, v252\n"
    "v_cvt_pk_bf16_f32 v128, v0, v1\n"
    "s_waitcnt lgkmcnt(2)\n"
    "v_mfma_f32_16x16x32_bf16 v[112:115], a[184:187], v[168:171], v[112:115]\n"
    "v_cvt_pk_bf16_f32 v129, v2, v3\n"
    "v_cvt_pk_bf16_f32 v130, v8, v9\n"
    "v_mfma_f32_16x16x32_bf16 v[116:119], a[184:187], v[172:175], v[116:119]\n"
    "v_cvt_pk_bf16_f32 v131, v10, v11\n"
    "v_mul_f32 v16, v16, v252\n"
    "s_waitcnt lgkmcnt(0)\n"
    "v_mfma_f32_16x16x32_bf16 v[120:123], a[188:191], v[168:171], v[120:123]\n"
    "v_mul_f32 v17, v17, v252\n"
    "v_mul_f32 v18, v18, v252\n"
    "v_mfma_f32_16x16x32_bf16 v[124:127], a[188:191], v[172:175], v[124:127]\n"
    "v_mul_f32 v19, v19, v252\n"
    "v_mul_f32 v24, v24, v252\n"
    "v_mul_f32 v25, v25, v252\n"
    "v_mul_f32 v26, v26, v252\n"
    "v_mul_f32 v27, v27, v252\n"
    "ds_write_b64 v220, v[128:129] offset:0\n"
    "ds_write_b64 v153, v[130:131] offset:0\n"
    "v_cvt_pk_bf16_f32 v132, v16, v17\n"
    "v_cvt_pk_bf16_f32 v133, v18, v19\n"
    "v_cvt_pk_bf16_f32 v134, v24, v25\n"
    "v_cvt_pk_bf16_f32 v135, v26, v27\n"
    "v_pk_mul_f32 v[32:33], v[32:33], v[252:253] op_sel_hi:[1,0]\n"
    "v_pk_mul_f32 v[34:35], v[34:35], v[252:253] op_sel_hi:[1,0]\n"
    "v_pk_mul_f32 v[40:41], v[40:41], v[252:253] op_sel_hi:[1,0]\n"
    "v_pk_mul_f32 v[42:43], v[42:43], v[252:253] op_sel_hi:[1,0]\n"
    "ds_write_b64 v154, v[132:133] offset:0\n"
    "ds_write_b64 v155, v[134:135] offset:0\n"
    "v_cvt_pk_bf16_f32 v136, v32, v33\n"
    "v_cvt_pk_bf16_f32 v137, v34, v35\n"
    "v_cvt_pk_bf16_f32 v138, v40, v41\n"
    "v_cvt_pk_bf16_f32 v139, v42, v43\n"
    "v_pk_mul_f32 v[48:49], v[48:49], v[252:253] op_sel_hi:[1,0]\n"
    "v_pk_mul_f32 v[50:51], v[50:51], v[252:253] op_sel_hi:[1,0]\n"
    "v_pk_mul_f32 v[56:57], v[56:57], v[252:253] op_sel_hi:[1,0]\n"
    "v_pk_mul_f32 v[58:59], v[58:59], v[252:253] op_sel_hi:[1,0]\n"
    "ds_write_b64 v156, v[136:137] offset:0\n"
    "ds_write_b64 v157, v[138:139] offset:0\n"
    "v_cvt_pk_bf16_f32 v140, v48, v49\n"
    "v_cvt_pk_bf16_f32 v141, v50, v51\n"
    "v_cvt_pk_bf16_f32 v142, v56, v57\n"
    "v_cvt_pk_bf16_f32 v143, v58, v59\n"
    "v_pk_mul_f32 v[4:5], v[4:5], v[252:253] op_sel:[0,1] op_sel_hi:[1,1]\n"
    "v_pk_mul_f32 v[6:7], v[6:7], v[252:253] op_sel:[0,1] op_sel_hi:[1,1]\n"
    "v_pk_mul_f32 v[12:13], v[12:13], v[252:253] op_sel:[0,1] op_sel_hi:[1,1]\n"
    "v_pk_mul_f32 v[14:15], v[14:15], v[252:253] op_sel:[0,1] op_sel_hi:[1,1]\n"
    "ds_write_b64 v158, v[140:141] offset:0\n"
    "ds_write_b64 v159, v[142:143] offset:0\n"
    "ds_read_b128 a[192:195], v218 offset:0\n"
    "ds_read_b128 a[196:199], v237 offset:1024\n"
    "ds_read_b128 a[200:203], v238 offset:2048\n"
    "ds_read_b128 a[204:207], v239 offset:3072\n"
    "v_cvt_pk_bf16_f32 v128, v4, v5\n"
    "v_cvt_pk_bf16_f32 v129, v6, v7\n"
    "v_cvt_pk_bf16_f32 v130, v12, v13\n"
    "v_cvt_pk_bf16_f32 v131, v14, v15\n"
    "v_pk_mul_f32 v[20:21], v[20:21], v[252:253] op_sel:[0,1] op_sel_hi:[1,1]\n"
    "v_pk_mul_f32 v[22:23], v[22:23], v[252:253] op_sel:[0,1] op_sel_hi:[1,1]\n"
    "v_pk_mul_f32 v[28:29], v[28:29], v[252:253] op_sel:[0,1] op_sel_hi:[1,1]\n"
    "v_pk_mul_f32 v[30:31], v[30:31], v[252:253] op_sel:[0,1] op_sel_hi:[1,1]\n"
    "ds_write_b64 v220, v[128:129] offset:4096\n"
    "ds_write_b64 v153, v[130:131] offset:4096\n"
    "v_cvt_pk_bf16_f32 v132, v20, v21\n"
    "v_cvt_pk_bf16_f32 v133, v22, v23\n"
    "v_cvt_pk_bf16_f32 v134, v28, v29\n"
    "v_cvt_pk_bf16_f32 v135, v30, v31\n"
    "s_waitcnt lgkmcnt(5)\n"
    "global_store_dwordx4 v232, a[192:195], s[48:49] offset:0 sc1\n"
    "v_pk_mul_f32 v[36:37], v[36:37], v[252:253] op_sel:[0,1] op_sel_hi:[1,1]\n"
    "v_pk_mul_f32 v[38:39], v[38:39], v[252:253] op_sel:[0,1] op_sel_hi:[1,1]\n"
    "v_pk_mul_f32 v[44:45], v[44:45], v[252:253] op_sel:[0,1] op_sel_hi:[1,1]\n"
    "v_pk_mul_f32 v[46:47], v[46:47], v[252:253] op_sel:[0,1] op_sel_hi:[1,1]\n"
    "ds_write_b64 v154, v[132:133] offset:4096\n"
    "ds_write_b64 v155, v[134:135] offset:4096\n"
    "v_cvt_pk_bf16_f32 v136, v36, v37\n"
    "v_cvt_pk_bf16_f32 v137, v38, v39\n"
    "v_cvt_pk_bf16_f32 v138, v44, v45\n"
    "v_cvt_pk_bf16_f32 v139, v46, v47\n"
    "s_waitcnt lgkmcnt(6)\n"
    "global_store_dwordx4 v232, a[196:199], s[48:49] offset:2048 sc1\n"
    "v_pk_mul_f32 v[52:53], v[52:53], v[252:253] op_sel:[0,1] op_sel_hi:[1,1]\n"
    "v_pk_mul_f32 v[54:55], v[54:55], v[252:253] op_sel:[0,1] op_sel_hi:[1,1]\n"
    "v_pk_mul_f32 v[60:61], v[60:61], v[252:253] op_sel:[0,1] op_sel_hi:[1,1]\n"
    "v_pk_mul_f32 v[62:63], v[62:63], v[252:253] op_sel:[0,1] op_sel_hi:[1,1]\n"
    "ds_write_b64 v156, v[136:137] offset:4096\n"
    "ds_write_b64 v157, v[138:139] offset:4096\n"
    "v_cvt_pk_bf16_f32 v140, v52, v53\n"
    "v_cvt_pk_bf16_f32 v141, v54, v55\n"
    "v_cvt_pk_bf16_f32 v142, v60, v61\n"
    "v_cvt_pk_bf16_f32 v143, v62, v63\n"
    "s_waitcnt lgkmcnt(7)\n"
    "global_store_dwordx4 v233, a[200:203], s[48:49] offset:0 sc1\n"
    "s_waitcnt lgkmcnt(6)\n"
    "global_store_dwordx4 v233, a[204:207], s[48:49] offset:2048 sc1\n"
    "v_pk_mul_f32 v[64:65], v[64:65], v[252:253] op_sel_hi:[1,0]\n"
    "v_pk_mul_f32 v[66:67], v[66:67], v[252:253] op_sel_hi:[1,0]\n"
    "v_pk_mul_f32 v[72:73], v[72:73], v[252:253] op_sel_hi:[1,0]\n"
    "v_pk_mul_f32 v[74:75], v[74:75], v[252:253] op_sel_hi:[1,0]\n"
    "ds_write_b64 v158, v[140:141] offset:4096\n"
    "ds_write_b64 v159, v[142:143] offset:4096\n"
    "ds_read_b128 a[208:211], v218 offset:4096\n"
    "ds_read_b128 a[212:215], v237 offset:5120\n"
    "ds_read_b128 a[216:219], v238 offset:6144\n"
    "ds_read_b128 a[220:223], v239 offset:7168\n"
    "v_cvt_pk_bf16_f32 v128, v64, v65\n"
    "v_cvt_pk_bf16_f32 v129, v66, v67\n"
    "v_cvt_pk_bf16_f32 v130, v72, v73\n"
    "v_cvt_pk_bf16_f32 v131, v74, v75\n"
    "s_cmp_eq_u32 s32, 0\n"
    "s_cbranch_scc1 .Lbp_49\n"
    "s_waitcnt vmcnt(8) lgkmcnt(0)\n"
    "s_barrier\n"
    "s_sub_u32 s32, s32, 1\n"
    ".Lbp_49:\n"
    "v_pk_mul_f32 v[80:81], v[80:81], v[252:253] op_sel_hi:[1,0]\n"
    "v_pk_mul_f32 v[82:83], v[82:83], v[252:253] op_sel_hi:[1,0]\n"
    "v_pk_mul_f32 v[88:89], v[88:89], v[252:253] op_sel_hi:[1,0]\n"
    "v_pk_mul_f32 v[90:91], v[90:91], v[252:253] op_sel_hi:[1,0]\n"
    "ds_write_b64 v220, v[128:129] offset:0\n"
    "ds_write_b64 v153, v[130:131] offset:0\n"
    "v_cvt_pk_bf16_f32 v132, v80, v81\n"
    "v_cvt_pk_bf16_f32 v133, v82, v83\n"
    "v_cvt_pk_bf16_f32 v134, v88, v89\n"
    "v_cvt_pk_bf16_f32 v135, v90, v91\n"
    "s_waitcnt lgkmcnt(5)\n"
    "global_store_dwordx4 v234, a[208:211], s[48:49] offset:0 sc1\n"
    "v_pk_mul_f32 v[96:97], v[96:97], v[252:253] op_sel_hi:[1,0]\n"
    "v_pk_mul_f32 v[98:99], v[98:99], v[252:253] op_sel_hi:[1,0]\n"
    "v_pk_mul_f32 v[104:105], v[104:105], v[252:253] op_sel_hi:[1,0]\n"
    "v_pk_mul_f32 v[106:107], v[106:107], v[252:253] op_sel_hi:[1,0]\n"
    "ds_write_b64 v154, v[132:133] offset:0\n"
    "ds_write_b64 v155, v[134:135] offset:0\n"
    "v_cvt_pk_bf16_f32 v136, v96, v97\n"
    "v_cvt_pk_bf16_f32 v137, v98, v99\n"
    "v_cvt_pk_bf16_f32 v138, v104, v105\n"
    "v_cvt_pk_bf16_f32 v139, v106, v107\n"
    "s_waitcnt lgkmcnt(6)\n"
    "global_store_dwordx4 v234, a[212:215], s[48:49] offset:2048 sc1\n"
    "v_pk_mul_f32 v[112:113], v[112:113], v[252:253] op_sel_hi:[1,0]\n"
    "v_pk_mul_f32 v[114:115], v[114:115], v[252:253] op_sel_hi:[1,0]\n"
    "v_pk_mul_f32 v[120:121], v[120:121], v[252:253] op_sel_hi:[1,0]\n"
    "v_pk_mul_f32 v[122:123], v[122:123], v[252:253] op_sel_hi:[1,0]\n"
    "ds_write_b64 v156, v[136:137] offset:0\n"
    "ds_write_b64 v157, v[138:139] offset:0\n"
    "v_cvt_pk_bf16_f32 v140, v112, v113\n"
    "v_cvt_pk_bf16_f32 v141, v114, v115\n"
    "v_cvt_pk_bf16_f32 v142, v120, v121\n"
    "v_cvt_pk_bf16_f32 v143, v122, v123\n"
    "s_waitcnt lgkmcnt(7)\n"
    "global_store_dwordx4 v235, a[216:219], s[48:49] offset:0 sc1\n"
    "s_waitcnt lgkmcnt(6)\n"
    "global_store_dwordx4 v235, a[220:223], s[48:49] offset:2048 sc1\n"
    "v_pk_mul_f32 v[68:69], v[68:69], v[252:253] op_sel:[0,1] op_sel_hi:[1,1]\n"
    "v_pk_mul_f32 v[70:71], v[70:71], v[252:253] op_sel:[0,1] op_sel_hi:[1,1]\n"
    "v_pk_mul_f32 v[76:77], v[76:77], v[252:253] op_sel:[0,1] op_sel_hi:[1,1]\n"
    "v_pk_mul_f32 v[78:79], v[78:79], v[252:253] op_sel:[0,1] op_sel_hi:[1,1]\n"
    "ds_write_b64 v158, v[140:141] offset:0\n"
    "ds_write_b64 v159, v[142:143] offset:0\n"
    "ds_read_b128 a[224:227], v218 offset:0\n"
    "ds_read_b128 a[228:231], v237 offset:1024\n"
    "ds_read_b128 a[232:235], v238 offset:2048\n"
    "ds_read_b128 a[236:239], v239 offset:3072\n"
    "v_cvt_pk_bf16_f32 v128, v68, v69\n"
    "v_cvt_pk_bf16_f32 v129, v70, v71\n"
    "v_cvt_pk_bf16_f32 v130, v76, v77\n"
    "v_cvt_pk_bf16_f32 v131, v78, v79\n"
    "v_pk_mul_f32 v[84:85], v[84:85], v[252:253] op_sel:[0,1] op_sel_hi:[1,1]\n"
    "v_pk_mul_f32 v[86:87], v[86:87], v[252:253] op_sel:[0,1] op_sel_hi:[1,1]\n"
    "v_pk_mul_f32 v[92:93], v[92:93], v[252:253] op_sel:[0,1] op_sel_hi:[1,1]\n"
    "v_pk_mul_f32 v[94:95], v[94:95], v[252:253] op_sel:[0,1] op_sel_hi:[1,1]\n"
    "ds_write_b64 v220, v[128:129] offset:4096\n"
    "ds_write_b64 v153, v[130:131] offset:4096\n"
    "v_cvt_pk_bf16_f32 v132, v84, v85\n"
    "v_cvt_pk_bf16_f32 v133, v86, v87\n"
    "v_cvt_pk_bf16_f32 v134, v92, v93\n"
    "v_cvt_pk_bf16_f32 v135, v94, v95\n"
    "s_waitcnt lgkmcnt(5)\n"
    "global_store_dwordx4 v232, a[224:227], s[48:49] offset:256 sc1\n"
    "v_pk_mul_f32 v[100:101], v[100:101], v[252:253] op_sel:[0,1] op_sel_hi:[1,1]\n"
    "v_pk_mul_f32 v[102:103], v[102:103], v[252:253] op_sel:[0,1] op_sel_hi:[1,1]\n"
    "v_pk_mul_f32 v[108:109], v[108:109], v[252:253] op_sel:[0,1] op_sel_hi:[1,1]\n"
    "v_pk_mul_f32 v[110:111], v[110:111], v[252:253] op_sel:[0,1] op_sel_hi:[1,1]\n"
    "ds_write_b64 v154, v[132:133] offset:4096\n"
    "ds_write_b64 v155, v[134:135] offset:4096\n"
    "v_cvt_pk_bf16_f32 v136, v100, v101\n"
    "v_cvt_pk_bf16_f32 v137, v102, v103\n"
    "v_cvt_pk_bf16_f32 v138, v108, v109\n"
    "v_cvt_pk_bf16_f32 v139, v110, v111\n"
    "s_waitcnt lgkmcnt(6)\n"
    "global_store_dwordx4 v232, a[228:231], s[48:49] offset:2304 sc1\n"
    "v_pk_mul_f32 v[116:117], v[116:117], v[252:253] op_sel:[0,1] op_sel_hi:[1,1]\n"
    "v_pk_mul_f32 v[118:119], v[118:119], v[252:253] op_sel:[0,1] op_sel_hi:[1,1]\n"
    "v_pk_mul_f32 v[124:125], v[124:125], v[252:253] op_sel:[0,1] op_sel_hi:[1,1]\n"
    "v_pk_mul_f32 v[126:127], v[126:127], v[252:253] op_sel:[0,1] op_sel_hi:[1,1]\n"
    "ds_write_b64 v156, v[136:137] offset:4096\n"
    "ds_write_b64 v157, v[138:139] offset:4096\n"
    "v_cvt_pk_bf16_f32 v140, v116, v117\n"
    "v_cvt_pk_bf16_f32 v141, v118, v119\n"
    "v_cvt_pk_bf16_f32 v142, v124, v125\n"
    "v_cvt_pk_bf16_f32 v143, v126, v127\n"
    "s_waitcnt lgkmcnt(7)\n"
    "global_store_dwordx4 v233, a[232:235], s[48:49] offset:256 sc1\n"
    "s_waitcnt lgkmcnt(6)\n"
    "global_store_dwordx4 v233, a[236:239], s[48:49] offset:2304 sc1\n"
    "ds_write_b64 v158, v[140:141] offset:4096\n"
    "ds_write_b64 v159, v[142:143] offset:4096\n"
    "ds_read_b128 a[240:243], v218 offset:4096\n"
    "ds_read_b128 a[244:247], v237 offset:5120\n"
    "ds_read_b128 a[248:251], v238 offset:6144\n"
    "ds_read_b128 a[252:255], v239 offset:7168\n"
    "s_waitcnt lgkmcnt(3)\n"
    "global_store_dwordx4 v234, a[240:243], s[48:49] offset:256 sc1\n"
    "s_waitcnt lgkmcnt(2)\n"
    "global_store_dwordx4 v234, a[244:247], s[48:49] offset:2304 sc1\n"
    "s_waitcnt lgkmcnt(1)\n"
    "global_store_dwordx4 v235, a[248:251], s[48:49] offset:256 sc1\n"
    "s_waitcnt lgkmcnt(0)\n"
    "global_store_dwordx4 v235, a[252:255], s[48:49] offset:2304 sc1\n"
    "s_cmp_eq_u32 s32, 0\n"
    "s_cbranch_scc1 .Lbp_50\n"
    "s_waitcnt vmcnt(16)\n"
    "s_barrier\n"
    "s_sub_u32 s32, s32, 1\n"
    ".Lbp_50:\n"
    "s_cmp_lg_u32 s67, 0\n"
    "s_cbranch_scc1 .Ldone\n"
    "s_mov_b32 s90, 0\n"
    ".Lackloop:\n"
    "global_load_dword v252, v213, s[52:53] offset:128 sc1\n"
    "global_load_dword v253, v213, s[52:53] offset:0 sc1\n"
    "s_waitcnt vmcnt(0)\n"
    "v_readfirstlane_b32 s72, v252\n"
    "v_readfirstlane_b32 s73, v253\n"
    "v_readfirstlane_b32 s74, v212\n"
    "s_nop 3\n"
    "s_cmp_eq_u32 s72, 1\n"
    "s_cbranch_scc1 .Ldone\n"
    "s_cmp_eq_u32 s73, s74\n"
    "s_cbranch_scc1 .Lacksl\n"
    "v_mov_b32 v212, v253\n"
    "global_store_dword v213, v212, s[52:53] offset:64 sc1\n"
    ".Lacksl:\n"
    "s_sleep 8\n"
    "s_add_u32 s90, s90, 1\n"
    "s_cmp_lt_u32 s90, 0x4000\n"
    "s_cbranch_scc1 .Lackloop\n"
    "s_branch .Ldone\n"
    ".LFB:\n"
    "ds_read_b64_tr_b16 a[188:189], v187 offset:0\n"
    "s_waitcnt lgkmcnt(11)\n"
    "ds_read_b64_tr_b16 a[190:191], v187 offset:8192\n"
    "v_mfma_f32_16x16x32_bf16 v[0:3], a[160:163], v[168:171], v[0:3]\n"
    "v_lshrrev_b32 v252, 2, v210\n"
    "v_mfma_f32_16x16x32_bf16 v[4:7], a[160:163], v[172:175], v[4:7]\n"
    "ds_read_b64_tr_b16 a[160:161], v180 offset:256\n"
    "v_and_b32 v253, 7, v252\n"
    "v_mfma_f32_16x16x32_bf16 v[8:11], a[164:167], v[168:171], v[8:11]\n"
    "ds_read_b64_tr_b16 a[162:163], v180 offset:8448\n"
    "v_lshrrev_b32 v254, 5, v210\n"
    "v_mfma_f32_16x16x32_bf16 v[12:15], a[164:167], v[172:175], v[12:15]\n"
    "ds_read_b64_tr_b16 a[164:165], v181 offset:256\n"
    "v_lshl_add_u32 v253, v253, 1, v254\n"
    "s_waitcnt lgkmcnt(13)\n"
    "v_mfma_f32_16x16x32_bf16 v[16:19], a[168:171], v[168:171], v[16:19]\n"
    "ds_read_b64_tr_b16 a[166:167], v181 offset:8448\n"
    "v_bfe_u32 v254, v210, 1, 1\n"
    "v_mfma_f32_16x16x32_bf16 v[20:23], a[168:171], v[172:175], v[20:23]\n"
    "ds_read_b64_tr_b16 a[168:169], v182 offset:256\n"
    "v_xor_b32 v253, v253, v254\n"
    "s_waitcnt lgkmcnt(13)\n"
    "v_mfma_f32_16x16x32_bf16 v[24:27], a[172:175], v[168:171], v[24:27]\n"
    "ds_read_b64_tr_b16 a[170:171], v182 offset:8448\n"
    "v_lshlrev_b32 v253, 4, v253\n"
    "v_mfma_f32_16x16x32_bf16 v[28:31], a[172:175], v[172:175], v[28:31]\n"
    "ds_read_b64_tr_b16 a[172:173], v183 offset:256\n"
    "v_lshl_add_u32 v252, v252, 8, v253\n"
    "s_waitcnt lgkmcnt(13)\n"
    "v_mfma_f32_16x16x32_bf16 v[32:35], a[176:179], v[168:171], v[32:35]\n"
    "ds_read_b64_tr_b16 a[174:175], v183 offset:8448\n"
    "v_and_b32 v253, 1, v210\n"
    "v_mfma_f32_16x16x32_bf16 v[36:39], a[176:179], v[172:175], v[36:39]\n"
    "ds_read_b64_tr_b16 a[176:177], v184 offset:256\n"
    "v_lshl_add_u32 v252, v253, 3, v252\n"
    "s_waitcnt lgkmcnt(13)\n"
    "v_mfma_f32_16x16x32_bf16 v[40:43], a[180:183], v[168:171], v[40:43]\n"
    "ds_read_b64_tr_b16 a[178:179], v184 offset:8448\n"
    "v_add_u32 v144, s91, v252\n"
    "v_mfma_f32_16x16x32_bf16 v[44:47], a[180:183], v[172:175], v[44:47]\n"
    "ds_read_b64_tr_b16 a[180:181], v185 offset:256\n"
    "v_xor_b32 v145, 32, v144\n"
    "s_waitcnt lgkmcnt(13)\n"
    "v_mfma_f32_16x16x32_bf16 v[48:51], a[184:187], v[168:171], v[48:51]\n"
    "ds_read_b64_tr_b16 a[182:183], v185 offset:8448\n"
    "v_xor_b32 v146, 64, v144\n"
    "v_mfma_f32_16x16x32_bf16 v[52:55], a[184:187], v[172:175], v[52:55]\n"
    "ds_read_b64_tr_b16 a[184:185], v186 offset:256\n"
    "v_xor_b32 v147, 96, v144\n"
    "s_waitcnt lgkmcnt(13)\n"
    "v_mfma_f32_16x16x32_bf16 v[56:59], a[188:191], v[168:171], v[56:59]\n"
    "ds_read_b64_tr_b16 a[186:187], v186 offset:8448\n"
    "v_xor_b32 v148, 128, v144\n"
    "v_mfma_f32_16x16x32_bf16 v[60:63], a[188:191], v[172:175], v[60:63]\n"
    "ds_read_b64_tr_b16 a[188:189], v187 offset:256\n"
    "v_xor_b32 v149, 160, v144\n"
    "s_waitcnt lgkmcnt(13)\n"
    "v_mfma_f32_16x16x32_bf16 v[64:67], a[160:163], v[168:171], v[64:67]\n"
    "ds_read_b64_tr_b16 a[190:191], v187 offset:8448\n"
    "v_xor_b32 v150, 192, v144\n"
    "v_mfma_f32_16x16x32_bf16 v[68:71], a[160:163], v[172:175], v[68:71]\n"
    "v_xor_b32 v151, 224, v144\n"
    "s_waitcnt lgkmcnt(12)\n"
    "v_mfma_f32_16x16x32_bf16 v[72:75], a[164:167], v[168:171], v[72:75]\n"
    "v_xor_b32 v153, 32, v220\n"
    "v_mfma_f32_16x16x32_bf16 v[76:79], a[164:167], v[172:175], v[76:79]\n"
    "v_xor_b32 v154, 64, v220\n"
    "v_xor_b32 v155, 96, v220\n"
    "s_waitcnt lgkmcnt(10)\n"
    "v_mfma_f32_16x16x32_bf16 v[80:83], a[168:171], v[168:171], v[80:83]\n"
    "v_xor_b32 v156, 128, v220\n"
    "v_mfma_f32_16x16x32_bf16 v[84:87], a[168:171], v[172:175], v[84:87]\n"
    "v_xor_b32 v157, 160, v220\n"
    "v_xor_b32 v158, 192, v220\n"
    "s_waitcnt lgkmcnt(8)\n"
    "v_mfma_f32_16x16x32_bf16 v[88:91], a[172:175], v[168:171], v[88:91]\n"
    "v_xor_b32 v159, 224, v220\n"
    "v_mfma_f32_16x16x32_bf16 v[92:95], a[172:175], v[172:175], v[92:95]\n"
    "v_xor_b32 v237, 128, v218\n"
    "v_xor_b32 v238, 16, v218\n"
    "s_waitcnt lgkmcnt(6)\n"
    "v_mfma_f32_16x16x32_bf16 v[96:99], a[176:179], v[168:171], v[96:99]\n"
    "v_xor_b32 v239, 144, v218\n"
    "v_mfma_f32_16x16x32_bf16 v[100:103], a[176:179], v[172:175], v[100:103]\n"
    "v_cmp_eq_u32 s[80:81], 0, v219\n"
    "v_cmp_eq_u32 s[82:83], 1, v219\n"
    "s_waitcnt lgkmcnt(4)\n"
    "v_mfma_f32_16x16x32_bf16 v[104:107], a[180:183], v[168:171], v[104:107]\n"
    "v_cmp_eq_u32 s[84:85], 2, v219\n"
    "v_mfma_f32_16x16x32_bf16 v[108:111], a[180:183], v[172:175], v[108:111]\n"
    "v_cmp_eq_u32 s[86:87], 3, v219\n"
    "s_waitcnt lgkmcnt(2)\n"
    "v_mfma_f32_16x16x32_bf16 v[112:115], a[184:187], v[168:171], v[112:115]\n"
    "v_mfma_f32_16x16x32_bf16 v[116:119], a[184:187], v[172:175], v[116:119]\n"
    "s_waitcnt lgkmcnt(0)\n"
    "v_mfma_f32_16x16x32_bf16 v[120:123], a[188:191], v[168:171], v[120:123]\n"
    "v_mfma_f32_16x16x32_bf16 v[124:127], a[188:191], v[172:175], v[124:127]\n"
    "s_cmp_lg_u32 s28, 0\n"
    "s_cbranch_scc1 .Lbjoin\n"
    "s_mov_b32 s90, 0\n"
    ".Lbpoll:\n"
    "s_waitcnt vmcnt(0)\n"
    "s_getpc_b64 s[30:31]\n"
    "s_branch .LPAY\n"
    "s_cmp_lg_u32 s28, 0\n"
    "s_cbranch_scc1 .Lbok\n"
    "s_add_u32 s90, s90, 1\n"
    "s_cmp_lt_u32 s90, 0x4000\n"
    "s_cbranch_scc1 .Lbpoll\n"
    ".Lbok:\n"
    "global_load_dwordx4 v[200:203], v214, s[44:45] offset:0 sc1\n"
    "v_mov_b32 v215, 1\n"
    "global_store_dword v213, v215, s[52:53] offset:128 sc1\n"
    ".Lbjoin:\n"
    "s_waitcnt vmcnt(1)\n"
    "v_sub_f32 v206, v200, v196\n"
    "ds_write_b64 v220, a[64:65] offset:0\n"
    "v_sub_f32 v207, v201, v197\n"
    "ds_write_b64 v220, a[66:67] offset:4096\n"
    "v_max_f32 v204, v206, v207\n"
    "ds_write_b64 v153, a[68:69] offset:0\n"
    "v_cmp_lt_f32 vcc, s61, v204\n"
    "ds_write_b64 v153, a[70:71] offset:4096\n"
    "s_cbranch_vccnz .Lmrare_51\n"
    ".Lmrareb_52:\n"
    "v_exp_f32 v204, v206\n"
    "v_exp_f32 v205, v207\n"
    "s_nop 0\n"
    "ds_write_b64 v154, a[72:73] offset:0\n"
    "v_cvt_pk_bf16_f32 v204, v204, v204\n"
    "ds_write_b64 v154, a[74:75] offset:4096\n"
    "v_cvt_pk_bf16_f32 v205, v205, v205\n"
    "ds_write_b64 v155, a[76:77] offset:0\n"
    "v_and_b32 v204, 0xffff0000, v204\n"
    "ds_write_b64 v155, a[78:79] offset:4096\n"
    "v_and_b32 v205, 0xffff0000, v205\n"
    "ds_write_b64 v156, a[80:81] offset:0\n"
    "v_fma_f32 v198, v202, v204, v198\n"
    "ds_write_b64 v156, a[82:83] offset:4096\n"
    "v_fma_f32 v199, v203, v205, v199\n"
    "ds_write_b64 v157, a[84:85] offset:0\n"
    "v_cndmask_b32 v240, 0, v204, s[80:81]\n"
    "ds_write_b64 v157, a[86:87] offset:4096\n"
    "v_cndmask_b32 v241, 0, v204, s[82:83]\n"
    "ds_write_b64 v158, a[88:89] offset:0\n"
    "v_cndmask_b32 v242, 0, v204, s[84:85]\n"
    "ds_write_b64 v158, a[90:91] offset:4096\n"
    "v_cndmask_b32 v243, 0, v204, s[86:87]\n"
    "ds_write_b64 v159, a[92:93] offset:0\n"
    "v_cndmask_b32 v244, 0, v205, s[80:81]\n"
    "s_waitcnt lgkmcnt(11)\n"
    "ds_write_b64 v159, a[94:95] offset:4096\n"
    "v_cndmask_b32 v245, 0, v205, s[82:83]\n"
    "ds_read_b64_tr_b16 a[160:161], v144 offset:0\n"
    "v_cndmask_b32 v246, 0, v205, s[84:85]\n"
    "ds_read_b64_tr_b16 a[162:163], v144 offset:4096\n"
    "v_cndmask_b32 v247, 0, v205, s[86:87]\n"
    "ds_read_b64_tr_b16 a[164:165], v145 offset:0\n"
    "v_cvt_pk_bf16_f32 v160, v240, v241\n"
    "s_waitcnt lgkmcnt(11)\n"
    "ds_read_b64_tr_b16 a[166:167], v145 offset:4096\n"
    "v_mov_b32 v162, 0\n"
    "ds_read_b64_tr_b16 a[168:169], v146 offset:0\n"
    "v_cvt_pk_bf16_f32 v161, v242, v243\n"
    "ds_read_b64_tr_b16 a[170:171], v146 offset:4096\n"
    "v_mov_b32 v163, 0\n"
    "ds_read_b64_tr_b16 a[172:173], v147 offset:0\n"
    "v_cvt_pk_bf16_f32 v166, v244, v245\n"
    "s_waitcnt lgkmcnt(11)\n"
    "ds_read_b64_tr_b16 a[174:175], v147 offset:4096\n"
    "v_mov_b32 v164, 0\n"
    "ds_read_b64_tr_b16 a[176:177], v148 offset:0\n"
    "v_cvt_pk_bf16_f32 v167, v246, v247\n"
    "ds_read_b64_tr_b16 a[178:179], v148 offset:4096\n"
    "v_mov_b32 v165, 0\n"
    "ds_read_b64_tr_b16 a[180:181], v149 offset:0\n"
    "s_waitcnt lgkmcnt(11)\n"
    "ds_read_b64_tr_b16 a[182:183], v149 offset:4096\n"
    "ds_read_b64_tr_b16 a[184:185], v150 offset:0\n"
    "ds_read_b64_tr_b16 a[186:187], v150 offset:4096\n"
    "ds_read_b64_tr_b16 a[188:189], v151 offset:0\n"
    "s_waitcnt lgkmcnt(11)\n"
    "ds_read_b64_tr_b16 a[190:191], v151 offset:4096\n"
    "ds_write_b64 v220, a[96:97] offset:0\n"
    "ds_write_b64 v220, a[98:99] offset:4096\n"
    "ds_write_b64 v153, a[100:101] offset:0\n"
    "s_waitcnt lgkmcnt(11)\n"
    "ds_write_b64 v153, a[102:103] offset:4096\n"
    "ds_write_b64 v154, a[104:105] offset:0\n"
    "ds_write_b64 v154, a[106:107] offset:4096\n"
    "ds_write_b64 v155, a[108:109] offset:0\n"
    "s_waitcnt lgkmcnt(11)\n"
    "ds_write_b64 v155, a[110:111] offset:4096\n"
    "ds_write_b64 v156, a[112:113] offset:0\n"
    "ds_write_b64 v156, a[114:115] offset:4096\n"
    "ds_write_b64 v157, a[116:117] offset:0\n"
    "s_waitcnt lgkmcnt(11)\n"
    "ds_write_b64 v157, a[118:119] offset:4096\n"
    "ds_write_b64 v158, a[120:121] offset:0\n"
    "ds_write_b64 v158, a[122:123] offset:4096\n"
    "ds_write_b64 v159, a[124:125] offset:0\n"
    "s_waitcnt lgkmcnt(11)\n"
    "ds_write_b64 v159, a[126:127] offset:4096\n"
    "v_mfma_f32_16x16x32_bf16 v[0:3], a[160:163], v[160:163], v[0:3]\n"
    "v_mov_b32 v204, v198\n"
    "v_mov_b32 v205, v199\n"
    "v_mfma_f32_16x16x32_bf16 v[4:7], a[160:163], v[164:167], v[4:7]\n"
    "ds_read_b64_tr_b16 a[160:161], v144 offset:0\n"
    "s_nop 0\n"
    "v_mfma_f32_16x16x32_bf16 v[8:11], a[164:167], v[160:163], v[8:11]\n"
    "ds_read_b64_tr_b16 a[162:163], v144 offset:4096\n"
    "v_permlane32_swap_b32 v204, v198\n"
    "v_mfma_f32_16x16x32_bf16 v[12:15], a[164:167], v[164:167], v[12:15]\n"
    "ds_read_b64_tr_b16 a[164:165], v145 offset:0\n"
    "v_permlane32_swap_b32 v205, v199\n"
    "v_mfma_f32_16x16x32_bf16 v[16:19], a[168:171], v[160:163], v[16:19]\n"
    "s_waitcnt lgkmcnt(11)\n"
    "ds_read_b64_tr_b16 a[166:167], v145 offset:4096\n"
    "v_add_f32 v198, v198, v204\n"
    "v_mfma_f32_16x16x32_bf16 v[20:23], a[168:171], v[164:167], v[20:23]\n"
    "ds_read_b64_tr_b16 a[168:169], v146 offset:0\n"
    "v_add_f32 v199, v199, v205\n"
    "v_mfma_f32_16x16x32_bf16 v[24:27], a[172:175], v[160:163], v[24:27]\n"
    "ds_read_b64_tr_b16 a[170:171], v146 offset:4096\n"
    "v_mov_b32 v204, v198\n"
    "v_mfma_f32_16x16x32_bf16 v[28:31], a[172:175], v[164:167], v[28:31]\n"
    "ds_read_b64_tr_b16 a[172:173], v147 offset:0\n"
    "v_mov_b32 v205, v199\n"
    "v_mfma_f32_16x16x32_bf16 v[32:35], a[176:179], v[160:163], v[32:35]\n"
    "s_waitcnt lgkmcnt(11)\n"
    "ds_read_b64_tr_b16 a[174:175], v147 offset:4096\n"
    "s_nop 0\n"
    "v_mfma_f32_16x16x32_bf16 v[36:39], a[176:179], v[164:167], v[36:39]\n"
    "ds_read_b64_tr_b16 a[176:177], v148 offset:0\n"
    "v_permlane16_swap_b32 v204, v198\n"
    "v_mfma_f32_16x16x32_bf16 v[40:43], a[180:183], v[160:163], v[40:43]\n"
    "ds_read_b64_tr_b16 a[178:179], v148 offset:4096\n"
    "v_permlane16_swap_b32 v205, v199\n"
    "v_mfma_f32_16x16x32_bf16 v[44:47], a[180:183], v[164:167], v[44:47]\n"
    "ds_read_b64_tr_b16 a[180:181], v149 offset:0\n"
    "v_add_f32 v198, v198, v204\n"
    "v_mfma_f32_16x16x32_bf16 v[48:51], a[184:187], v[160:163], v[48:51]\n"
    "s_waitcnt lgkmcnt(11)\n"
    "ds_read_b64_tr_b16 a[182:183], v149 offset:4096\n"
    "v_add_f32 v199, v199, v205\n"
    "v_mfma_f32_16x16x32_bf16 v[52:55], a[184:187], v[164:167], v[52:55]\n"
    "ds_read_b64_tr_b16 a[184:185], v150 offset:0\n"
    "v_rcp_f32 v252, v198\n"
    "v_mfma_f32_16x16x32_bf16 v[56:59], a[188:191], v[160:163], v[56:59]\n"
    "ds_read_b64_tr_b16 a[186:187], v150 offset:4096\n"
    "v_rcp_f32 v253, v199\n"
    "v_mfma_f32_16x16x32_bf16 v[60:63], a[188:191], v[164:167], v[60:63]\n"
    "ds_read_b64_tr_b16 a[188:189], v151 offset:0\n"
    "s_nop 0\n"
    "s_waitcnt lgkmcnt(13)\n"
    "v_mfma_f32_16x16x32_bf16 v[64:67], a[160:163], v[160:163], v[64:67]\n"
    "ds_read_b64_tr_b16 a[190:191], v151 offset:4096\n"
    "v_mul_f32 v0, v0, v252\n"
    "v_mfma_f32_16x16x32_bf16 v[68:71], a[160:163], v[164:167], v[68:71]\n"
    "v_mul_f32 v1, v1, v252\n"
    "v_mul_f32 v2, v2, v252\n"
    "s_waitcnt lgkmcnt(12)\n"
    "v_mfma_f32_16x16x32_bf16 v[72:75], a[164:167], v[160:163], v[72:75]\n"
    "v_mul_f32 v3, v3, v252\n"
    "v_mul_f32 v8, v8, v252\n"
    "v_mfma_f32_16x16x32_bf16 v[76:79], a[164:167], v[164:167], v[76:79]\n"
    "v_mul_f32 v9, v9, v252\n"
    "v_mul_f32 v10, v10, v252\n"
    "s_waitcnt lgkmcnt(10)\n"
    "v_mfma_f32_16x16x32_bf16 v[80:83], a[168:171], v[160:163], v[80:83]\n"
    "v_mul_f32 v11, v11, v252\n"
    "v_cvt_pk_bf16_f32 v128, v0, v1\n"
    "v_mfma_f32_16x16x32_bf16 v[84:87], a[168:171], v[164:167], v[84:87]\n"
    "v_cvt_pk_bf16_f32 v129, v2, v3\n"
    "v_cvt_pk_bf16_f32 v130, v8, v9\n"
    "s_waitcnt lgkmcnt(8)\n"
    "v_mfma_f32_16x16x32_bf16 v[88:91], a[172:175], v[160:163], v[88:91]\n"
    "v_cvt_pk_bf16_f32 v131, v10, v11\n"
    "v_mul_f32 v16, v16, v252\n"
    "v_mfma_f32_16x16x32_bf16 v[92:95], a[172:175], v[164:167], v[92:95]\n"
    "v_mul_f32 v17, v17, v252\n"
    "v_mul_f32 v18, v18, v252\n"
    "s_waitcnt lgkmcnt(6)\n"
    "v_mfma_f32_16x16x32_bf16 v[96:99], a[176:179], v[160:163], v[96:99]\n"
    "v_mul_f32 v19, v19, v252\n"
    "v_mul_f32 v24, v24, v252\n"
    "v_mfma_f32_16x16x32_bf16 v[100:103], a[176:179], v[164:167], v[100:103]\n"
    "v_mul_f32 v25, v25, v252\n"
    "v_mul_f32 v26, v26, v252\n"
    "s_waitcnt lgkmcnt(4)\n"
    "v_mfma_f32_16x16x32_bf16 v[104:107], a[180:183], v[160:163], v[104:107]\n"
    "v_mul_f32 v27, v27, v252\n"
    "ds_write_b64 v220, v[128:129] offset:0\n"
    "v_mfma_f32_16x16x32_bf16 v[108:111], a[180:183], v[164:167], v[108:111]\n"
    "ds_write_b64 v153, v[130:131] offset:0\n"
    "s_waitcnt lgkmcnt(4)\n"
    "v_mfma_f32_16x16x32_bf16 v[112:115], a[184:187], v[160:163], v[112:115]\n"
    "v_cvt_pk_bf16_f32 v132, v16, v17\n"
    "v_cvt_pk_bf16_f32 v133, v18, v19\n"
    "v_mfma_f32_16x16x32_bf16 v[116:119], a[184:187], v[164:167], v[116:119]\n"
    "v_cvt_pk_bf16_f32 v134, v24, v25\n"
    "v_cvt_pk_bf16_f32 v135, v26, v27\n"
    "s_waitcnt lgkmcnt(2)\n"
    "v_mfma_f32_16x16x32_bf16 v[120:123], a[188:191], v[160:163], v[120:123]\n"
    "v_mul_f32 v32, v32, v252\n"
    "v_mul_f32 v33, v33, v252\n"
    "v_mfma_f32_16x16x32_bf16 v[124:127], a[188:191], v[164:167], v[124:127]\n"
    "v_mul_f32 v34, v34, v252\n"
    "v_mul_f32 v35, v35, v252\n"
    "v_mul_f32 v40, v40, v252\n"
    "v_mul_f32 v41, v41, v252\n"
    "v_mul_f32 v42, v42, v252\n"
    "v_mul_f32 v43, v43, v252\n"
    "ds_write_b64 v154, v[132:133] offset:0\n"
    "ds_write_b64 v155, v[134:135] offset:0\n"
    "v_cvt_pk_bf16_f32 v136, v32, v33\n"
    "v_cvt_pk_bf16_f32 v137, v34, v35\n"
    "v_cvt_pk_bf16_f32 v138, v40, v41\n"
    "v_cvt_pk_bf16_f32 v139, v42, v43\n"
    "v_pk_mul_f32 v[48:49], v[48:49], v[252:253] op_sel_hi:[1,0]\n"
    "v_pk_mul_f32 v[50:51], v[50:51], v[252:253] op_sel_hi:[1,0]\n"
    "v_pk_mul_f32 v[56:57], v[56:57], v[252:253] op_sel_hi:[1,0]\n"
    "v_pk_mul_f32 v[58:59], v[58:59], v[252:253] op_sel_hi:[1,0]\n"
    "ds_write_b64 v156, v[136:137] offset:0\n"
    "ds_write_b64 v157, v[138:139] offset:0\n"
    "v_cvt_pk_bf16_f32 v140, v48, v49\n"
    "v_cvt_pk_bf16_f32 v141, v50, v51\n"
    "v_cvt_pk_bf16_f32 v142, v56, v57\n"
    "v_cvt_pk_bf16_f32 v143, v58, v59\n"
    "v_pk_mul_f32 v[4:5], v[4:5], v[252:253] op_sel:[0,1] op_sel_hi:[1,1]\n"
    "v_pk_mul_f32 v[6:7], v[6:7], v[252:253] op_sel:[0,1] op_sel_hi:[1,1]\n"
    "v_pk_mul_f32 v[12:13], v[12:13], v[252:253] op_sel:[0,1] op_sel_hi:[1,1]\n"
    "v_pk_mul_f32 v[14:15], v[14:15], v[252:253] op_sel:[0,1] op_sel_hi:[1,1]\n"
    "ds_write_b64 v158, v[140:141] offset:0\n"
    "ds_write_b64 v159, v[142:143] offset:0\n"
    "ds_read_b128 a[192:195], v218 offset:0\n"
    "ds_read_b128 a[196:199], v237 offset:1024\n"
    "ds_read_b128 a[200:203], v238 offset:2048\n"
    "ds_read_b128 a[204:207], v239 offset:3072\n"
    "v_cvt_pk_bf16_f32 v128, v4, v5\n"
    "v_cvt_pk_bf16_f32 v129, v6, v7\n"
    "v_cvt_pk_bf16_f32 v130, v12, v13\n"
    "v_cvt_pk_bf16_f32 v131, v14, v15\n"
    "v_pk_mul_f32 v[20:21], v[20:21], v[252:253] op_sel:[0,1] op_sel_hi:[1,1]\n"
    "v_pk_mul_f32 v[22:23], v[22:23], v[252:253] op_sel:[0,1] op_sel_hi:[1,1]\n"
    "v_pk_mul_f32 v[28:29], v[28:29], v[252:253] op_sel:[0,1] op_sel_hi:[1,1]\n"
    "v_pk_mul_f32 v[30:31], v[30:31], v[252:253] op_sel:[0,1] op_sel_hi:[1,1]\n"
    "ds_write_b64 v220, v[128:129] offset:4096\n"
    "ds_write_b64 v153, v[130:131] offset:4096\n"
    "v_cvt_pk_bf16_f32 v132, v20, v21\n"
    "v_cvt_pk_bf16_f32 v133, v22, v23\n"
    "v_cvt_pk_bf16_f32 v134, v28, v29\n"
    "v_cvt_pk_bf16_f32 v135, v30, v31\n"
    "s_waitcnt lgkmcnt(5)\n"
    "global_store_dwordx4 v232, a[192:195], s[48:49] offset:0 sc1\n"
    "v_pk_mul_f32 v[36:37], v[36:37], v[252:253] op_sel:[0,1] op_sel_hi:[1,1]\n"
    "v_pk_mul_f32 v[38:39], v[38:39], v[252:253] op_sel:[0,1] op_sel_hi:[1,1]\n"
    "v_pk_mul_f32 v[44:45], v[44:45], v[252:253] op_sel:[0,1] op_sel_hi:[1,1]\n"
    "v_pk_mul_f32 v[46:47], v[46:47], v[252:253] op_sel:[0,1] op_sel_hi:[1,1]\n"
    "ds_write_b64 v154, v[132:133] offset:4096\n"
    "ds_write_b64 v155, v[134:135] offset:4096\n"
    "v_cvt_pk_bf16_f32 v136, v36, v37\n"
    "v_cvt_pk_bf16_f32 v137, v38, v39\n"
    "v_cvt_pk_bf16_f32 v138, v44, v45\n"
    "v_cvt_pk_bf16_f32 v139, v46, v47\n"
    "s_waitcnt lgkmcnt(6)\n"
    "global_store_dwordx4 v232, a[196:199], s[48:49] offset:2048 sc1\n"
    "v_pk_mul_f32 v[52:53], v[52:53], v[252:253] op_sel:[0,1] op_sel_hi:[1,1]\n"
    "v_pk_mul_f32 v[54:55], v[54:55], v[252:253] op_sel:[0,1] op_sel_hi:[1,1]\n"
    "v_pk_mul_f32 v[60:61], v[60:61], v[252:253] op_sel:[0,1] op_sel_hi:[1,1]\n"
    "v_pk_mul_f32 v[62:63], v[62:63], v[252:253] op_sel:[0,1] op_sel_hi:[1,1]\n"
    "ds_write_b64 v156, v[136:137] offset:4096\n"
    "ds_write_b64 v157, v[138:139] offset:4096\n"
    "v_cvt_pk_bf16_f32 v140, v52, v53\n"
    "v_cvt_pk_bf16_f32 v141, v54, v55\n"
    "v_cvt_pk_bf16_f32 v142, v60, v61\n"
    "v_cvt_pk_bf16_f32 v143, v62, v63\n"
    "s_waitcnt lgkmcnt(7)\n"
    "global_store_dwordx4 v233, a[200:203], s[48:49] offset:0 sc1\n"
    "s_waitcnt lgkmcnt(6)\n"
    "global_store_dwordx4 v233, a[204:207], s[48:49] offset:2048 sc1\n"
    "v_pk_mul_f32 v[64:65], v[64:65], v[252:253] op_sel_hi:[1,0]\n"
    "v_pk_mul_f32 v[66:67], v[66:67], v[252:253] op_sel_hi:[1,0]\n"
    "v_pk_mul_f32 v[72:73], v[72:73], v[252:253] op_sel_hi:[1,0]\n"
    "v_pk_mul_f32 v[74:75], v[74:75], v[252:253] op_sel_hi:[1,0]\n"
    "ds_write_b64 v158, v[140:141] offset:4096\n"
    "ds_write_b64 v159, v[142:143] offset:4096\n"
    "ds_read_b128 a[208:211], v218 offset:4096\n"
    "ds_read_b128 a[212:215], v237 offset:5120\n"
    "ds_read_b128 a[216:219], v238 offset:6144\n"
    "ds_read_b128 a[220:223], v239 offset:7168\n"
    "v_cvt_pk_bf16_f32 v128, v64, v65\n"
    "v_cvt_pk_bf16_f32 v129, v66, v67\n"
    "v_cvt_pk_bf16_f32 v130, v72, v73\n"
    "v_cvt_pk_bf16_f32 v131, v74, v75\n"
    "v_pk_mul_f32 v[80:81], v[80:81], v[252:253] op_sel_hi:[1,0]\n"
    "v_pk_mul_f32 v[82:83], v[82:83], v[252:253] op_sel_hi:[1,0]\n"
    "v_pk_mul_f32 v[88:89], v[88:89], v[252:253] op_sel_hi:[1,0]\n"
    "v_pk_mul_f32 v[90:91], v[90:91], v[252:253] op_sel_hi:[1,0]\n"
    "ds_write_b64 v220, v[128:129] offset:0\n"
    "ds_write_b64 v153, v[130:131] offset:0\n"
    "v_cvt_pk_bf16_f32 v132, v80, v81\n"
    "v_cvt_pk_bf16_f32 v133, v82, v83\n"
    "v_cvt_pk_bf16_f32 v134, v88, v89\n"
    "v_cvt_pk_bf16_f32 v135, v90, v91\n"
    "s_waitcnt lgkmcnt(5)\n"
    "global_store_dwordx4 v234, a[208:211], s[48:49] offset:0 sc1\n"
    "v_pk_mul_f32 v[96:97], v[96:97], v[252:253] op_sel_hi:[1,0]\n"
    "v_pk_mul_f32 v[98:99], v[98:99], v[252:253] op_sel_hi:[1,0]\n"
    "v_pk_mul_f32 v[104:105], v[104:105], v[252:253] op_sel_hi:[1,0]\n"
    "v_pk_mul_f32 v[106:107], v[106:107], v[252:253] op_sel_hi:[1,0]\n"
    "ds_write_b64 v154, v[132:133] offset:0\n"
    "ds_write_b64 v155, v[134:135] offset:0\n"
    "v_cvt_pk_bf16_f32 v136, v96, v97\n"
    "v_cvt_pk_bf16_f32 v137, v98, v99\n"
    "v_cvt_pk_bf16_f32 v138, v104, v105\n"
    "v_cvt_pk_bf16_f32 v139, v106, v107\n"
    "s_waitcnt lgkmcnt(6)\n"
    "global_store_dwordx4 v234, a[212:215], s[48:49] offset:2048 sc1\n"
    "v_pk_mul_f32 v[112:113], v[112:113], v[252:253] op_sel_hi:[1,0]\n"
    "v_pk_mul_f32 v[114:115], v[114:115], v[252:253] op_sel_hi:[1,0]\n"
    "v_pk_mul_f32 v[120:121], v[120:121], v[252:253] op_sel_hi:[1,0]\n"
    "v_pk_mul_f32 v[122:123], v[122:123], v[252:253] op_sel_hi:[1,0]\n"
    "ds_write_b64 v156, v[136:137] offset:0\n"
    "ds_write_b64 v157, v[138:139] offset:0\n"
    "v_cvt_pk_bf16_f32 v140, v112, v113\n"
    "v_cvt_pk_bf16_f32 v141, v114, v115\n"
    "v_cvt_pk_bf16_f32 v142, v120, v121\n"
    "v_cvt_pk_bf16_f32 v143, v122, v123\n"
    "s_waitcnt lgkmcnt(7)\n"
    "global_store_dwordx4 v235, a[216:219], s[48:49] offset:0 sc1\n"
    "s_waitcnt lgkmcnt(6)\n"
    "global_store_dwordx4 v235, a[220:223], s[48:49] offset:2048 sc1\n"
    "v_pk_mul_f32 v[68:69], v[68:69], v[252:253] op_sel:[0,1] op_sel_hi:[1,1]\n"
    "v_pk_mul_f32 v[70:71], v[70:71], v[252:253] op_sel:[0,1] op_sel_hi:[1,1]\n"
    "v_pk_mul_f32 v[76:77], v[76:77], v[252:253] op_sel:[0,1] op_sel_hi:[1,1]\n"
    "v_pk_mul_f32 v[78:79], v[78:79], v[252:253] op_sel:[0,1] op_sel_hi:[1,1]\n"
    "ds_write_b64 v158, v[140:141] offset:0\n"
    "ds_write_b64 v159, v[142:143] offset:0\n"
    "ds_read_b128 a[224:227], v218 offset:0\n"
    "ds_read_b128 a[228:231], v237 offset:1024\n"
    "ds_read_b128 a[232:235], v238 offset:2048\n"
    "ds_read_b128 a[236:239], v239 offset:3072\n"
    "v_cvt_pk_bf16_f32 v128, v68, v69\n"
    "v_cvt_pk_bf16_f32 v129, v70, v71\n"
    "v_cvt_pk_bf16_f32 v130, v76, v77\n"
    "v_cvt_pk_bf16_f32 v131, v78, v79\n"
    "v_pk_mul_f32 v[84:85], v[84:85], v[252:253] op_sel:[0,1] op_sel_hi:[1,1]\n"
    "v_pk_mul_f32 v[86:87], v[86:87], v[252:253] op_sel:[0,1] op_sel_hi:[1,1]\n"
    "v_pk_mul_f32 v[92:93], v[92:93], v[252:253] op_sel:[0,1] op_sel_hi:[1,1]\n"
    "v_pk_mul_f32 v[94:95], v[94:95], v[252:253] op_sel:[0,1] op_sel_hi:[1,1]\n"
    "ds_write_b64 v220, v[128:129] offset:4096\n"
    "ds_write_b64 v153, v[130:131] offset:4096\n"
    "v_cvt_pk_bf16_f32 v132, v84, v85\n"
    "v_cvt_pk_bf16_f32 v133, v86, v87\n"
    "v_cvt_pk_bf16_f32 v134, v92, v93\n"
    "v_cvt_pk_bf16_f32 v135, v94, v95\n"
    "s_waitcnt lgkmcnt(5)\n"
    "global_store_dwordx4 v232, a[224:227], s[48:49] offset:256 sc1\n"
    "v_pk_mul_f32 v[100:101], v[100:101], v[252:253] op_sel:[0,1] op_sel_hi:[1,1]\n"
    "v_pk_mul_f32 v[102:103], v[102:103], v[252:253] op_sel:[0,1] op_sel_hi:[1,1]\n"
    "v_pk_mul_f32 v[108:109], v[108:109], v[252:253] op_sel:[0,1] op_sel_hi:[1,1]\n"
    "v_pk_mul_f32 v[110:111], v[110:111], v[252:253] op_sel:[0,1] op_sel_hi:[1,1]\n"
    "ds_write_b64 v154, v[132:133] offset:4096\n"
    "ds_write_b64 v155, v[134:135] offset:4096\n"
    "v_cvt_pk_bf16_f32 v136, v100, v101\n"
    "v_cvt_pk_bf16_f32 v137, v102, v103\n"
    "v_cvt_pk_bf16_f32 v138, v108, v109\n"
    "v_cvt_pk_bf16_f32 v139, v110, v111\n"
    "s_waitcnt lgkmcnt(6)\n"
    "global_store_dwordx4 v232, a[228:231], s[48:49] offset:2304 sc1\n"
    "v_pk_mul_f32 v[116:117], v[116:117], v[252:253] op_sel:[0,1] op_sel_hi:[1,1]\n"
    "v_pk_mul_f32 v[118:119], v[118:119], v[252:253] op_sel:[0,1] op_sel_hi:[1,1]\n"
    "v_pk_mul_f32 v[124:125], v[124:125], v[252:253] op_sel:[0,1] op_sel_hi:[1,1]\n"
    "v_pk_mul_f32 v[126:127], v[126:127], v[252:253] op_sel:[0,1] op_sel_hi:[1,1]\n"
    "ds_write_b64 v156, v[136:137] offset:4096\n"
    "ds_write_b64 v157, v[138:139] offset:4096\n"
    "v_cvt_pk_bf16_f32 v140, v116, v117\n"
    "v_cvt_pk_bf16_f32 v141, v118, v119\n"
    "v_cvt_pk_bf16_f32 v142, v124, v125\n"
    "v_cvt_pk_bf16_f32 v143, v126, v127\n"
    "s_waitcnt lgkmcnt(7)\n"
    "global_store_dwordx4 v233, a[232:235], s[48:49] offset:256 sc1\n"
    "s_waitcnt lgkmcnt(6)\n"
    "global_store_dwordx4 v233, a[236:239], s[48:49] offset:2304 sc1\n"
    "ds_write_b64 v158, v[140:141] offset:4096\n"
    "ds_write_b64 v159, v[142:143] offset:4096\n"
    "ds_read_b128 a[240:243], v218 offset:4096\n"
    "ds_read_b128 a[244:247], v237 offset:5120\n"
    "ds_read_b128 a[248:251], v238 offset:6144\n"
    "ds_read_b128 a[252:255], v239 offset:7168\n"
    "s_waitcnt lgkmcnt(3)\n"
    "global_store_dwordx4 v234, a[240:243], s[48:49] offset:256 sc1\n"
    "s_waitcnt lgkmcnt(2)\n"
    "global_store_dwordx4 v234, a[244:247], s[48:49] offset:2304 sc1\n"
    "s_waitcnt lgkmcnt(1)\n"
    "global_store_dwordx4 v235, a[248:251], s[48:49] offset:256 sc1\n"
    "s_waitcnt lgkmcnt(0)\n"
    "global_store_dwordx4 v235, a[252:255], s[48:49] offset:2304 sc1\n"
    ".Ldone:\n"
    "s_endpgm\n"
    ".Llow_4:\n"
    "s_nop 7\n"
    "v_max3_f32 v204, v128, v129, v130\n"
    "v_max3_f32 v206, v131, v136, v137\n"
    "v_max3_f32 v205, v132, v133, v134\n"
    "v_max3_f32 v207, v135, v140, v141\n"
    "v_max3_f32 v204, v204, v138, v139\n"
    "v_max3_f32 v205, v205, v142, v143\n"
    "v_max_f32 v204, v204, v206\n"
    "v_max_f32 v205, v205, v207\n"
    "v_mov_b32 v206, v204\n"
    "v_mov_b32 v207, v205\n"
    "s_nop 0\n"
    "v_permlane32_swap_b32 v206, v204\n"
    "v_permlane32_swap_b32 v207, v205\n"
    "v_max_f32 v204, v204, v206\n"
    "v_max_f32 v205, v205, v207\n"
    "v_mov_b32 v206, v204\n"
    "v_mov_b32 v207, v205\n"
    "s_nop 0\n"
    "v_permlane16_swap_b32 v206, v204\n"
    "v_permlane16_swap_b32 v207, v205\n"
    "v_max_f32 v204, v204, v206\n"
    "v_max_f32 v205, v205, v207\n"
    "v_add_f32 v204, 0x41500000, v204\n"
    "v_add_f32 v205, 0x41500000, v205\n"
    "v_max_f32 v196, s62, v204\n"
    "v_max_f32 v197, s62, v205\n"
    "v_add_f32 v204, 0xc1500000, v196\n"
    "v_add_f32 v205, 0xc1500000, v197\n"
    "v_sub_f32 v128, v128, v204\n"
    "v_sub_f32 v129, v129, v204\n"
    "v_sub_f32 v130, v130, v204\n"
    "v_sub_f32 v131, v131, v204\n"
    "v_sub_f32 v132, v132, v205\n"
    "v_sub_f32 v133, v133, v205\n"
    "v_sub_f32 v134, v134, v205\n"
    "v_sub_f32 v135, v135, v205\n"
    "v_sub_f32 v136, v136, v204\n"
    "v_sub_f32 v137, v137, v204\n"
    "v_sub_f32 v138, v138, v204\n"
    "v_sub_f32 v139, v139, v204\n"
    "v_sub_f32 v140, v140, v205\n"
    "v_sub_f32 v141, v141, v205\n"
    "v_sub_f32 v142, v142, v205\n"
    "v_sub_f32 v143, v143, v205\n"
    "v_sub_f32 v236, 0, v196\n"
    "v_sub_f32 v237, 0, v196\n"
    "v_sub_f32 v238, 0, v196\n"
    "v_sub_f32 v239, 0, v196\n"
    "v_sub_f32 v244, 0, v197\n"
    "v_sub_f32 v245, 0, v197\n"
    "v_sub_f32 v246, 0, v197\n"
    "v_sub_f32 v247, 0, v197\n"
    "v_sub_f32 v248, 0, v196\n"
    "v_sub_f32 v249, 0, v196\n"
    "v_sub_f32 v250, 0, v196\n"
    "v_sub_f32 v251, 0, v196\n"
    "v_sub_f32 v252, 0, v197\n"
    "v_sub_f32 v253, 0, v197\n"
    "v_sub_f32 v254, 0, v197\n"
    "v_sub_f32 v255, 0, v197\n"
    "v_exp_f32 v240, v128\n"
    "v_exp_f32 v241, v129\n"
    "v_exp_f32 v242, v130\n"
    "v_add_f32 v216, v240, v241\n"
    "v_cvt_pk_bf16_f32 v160, v240, v241\n"
    "v_exp_f32 v243, v131\n"
    "v_add_f32 v216, v216, v242\n"
    "v_exp_f32 v240, v132\n"
    "v_add_f32 v216, v216, v243\n"
    "v_cvt_pk_bf16_f32 v161, v242, v243\n"
    "v_exp_f32 v241, v133\n"
    "v_exp_f32 v242, v134\n"
    "s_nop 0\n"
    "s_branch .Llowb_5\n"
    ".Lstub_6:\n"
    "s_getpc_b64 s[30:31]\n"
    "s_branch .Lrare0\n"
    "v_add_f32 v144, v144, v206\n"
    "v_add_f32 v145, v145, v206\n"
    "v_add_f32 v146, v146, v206\n"
    "v_add_f32 v147, v147, v206\n"
    "v_add_f32 v148, v148, v207\n"
    "v_add_f32 v149, v149, v207\n"
    "v_add_f32 v150, v150, v207\n"
    "v_add_f32 v151, v151, v207\n"
    "v_add_f32 v152, v152, v206\n"
    "v_add_f32 v153, v153, v206\n"
    "v_add_f32 v154, v154, v206\n"
    "v_add_f32 v155, v155, v206\n"
    "v_add_f32 v156, v156, v207\n"
    "v_add_f32 v157, v157, v207\n"
    "v_add_f32 v158, v158, v207\n"
    "v_add_f32 v159, v159, v207\n"
    "v_add_f32 v236, v236, v206\n"
    "v_add_f32 v237, v237, v206\n"
    "v_add_f32 v238, v238, v206\n"
    "v_add_f32 v239, v239, v206\n"
    "v_add_f32 v244, v244, v207\n"
    "v_add_f32 v245, v245, v207\n"
    "v_add_f32 v246, v246, v207\n"
    "v_add_f32 v247, v247, v207\n"
    "v_add_f32 v248, v248, v206\n"
    "v_add_f32 v249, v249, v206\n"
    "v_add_f32 v250, v250, v206\n"
    "v_add_f32 v251, v251, v206\n"
    "v_add_f32 v252, v252, v207\n"
    "v_add_f32 v253, v253, v207\n"
    "v_add_f32 v254, v254, v207\n"
    "v_add_f32 v255, v255, v207\n"
    "s_nop 1\n"
    "s_branch .Lstubb_7\n"
    ".Lstub_8:\n"
    "s_getpc_b64 s[30:31]\n"
    "s_branch .Lrare1\n"
    "v_add_f32 v128, v128, v206\n"
    "v_add_f32 v129, v129, v206\n"
    "v_add_f32 v130, v130, v206\n"
    "v_add_f32 v131, v131, v206\n"
    "v_add_f32 v132, v132, v207\n"
    "v_add_f32 v133, v133, v207\n"
    "v_add_f32 v134, v134, v207\n"
    "v_add_f32 v135, v135, v207\n"
    "v_add_f32 v136, v136, v206\n"
    "v_add_f32 v137, v137, v206\n"
    "v_add_f32 v138, v138, v206\n"
    "v_add_f32 v139, v139, v206\n"
    "v_add_f32 v140, v140, v207\n"
    "v_add_f32 v141, v141, v207\n"
    "v_add_f32 v142, v142, v207\n"
    "v_add_f32 v143, v143, v207\n"
    "v_add_f32 v236, v236, v206\n"
    "v_add_f32 v237, v237, v206\n"
    "v_add_f32 v238, v238, v206\n"
    "v_add_f32 v239, v239, v206\n"
    "v_add_f32 v244, v244, v207\n"
    "v_add_f32 v245, v245, v207\n"
    "v_add_f32 v246, v246, v207\n"
    "v_add_f32 v247, v247, v207\n"
    "v_add_f32 v248, v248, v206\n"
    "v_add_f32 v249, v249, v206\n"
    "v_add_f32 v250, v250, v206\n"
    "v_add_f32 v251, v251, v206\n"
    "v_add_f32 v252, v252, v207\n"
    "v_add_f32 v253, v253, v207\n"
    "v_add_f32 v254, v254, v207\n"
    "v_add_f32 v255, v255, v207\n"
    "s_nop 1\n"
    "s_branch .Lstubb_9\n"
    ".Lstub_10:\n"
    "s_getpc_b64 s[30:31]\n"
    "s_branch .Lrare0\n"
    "v_add_f32 v144, v144, v206\n"
    "v_add_f32 v145, v145, v206\n"
    "v_add_f32 v146, v146, v206\n"
    "v_add_f32 v147, v147, v206\n"
    "v_add_f32 v148, v148, v207\n"
    "v_add_f32 v149, v149, v207\n"
    "v_add_f32 v150, v150, v207\n"
    "v_add_f32 v151, v151, v207\n"
    "v_add_f32 v152, v152, v206\n"
    "v_add_f32 v153, v153, v206\n"
    "v_add_f32 v154, v154, v206\n"
    "v_add_f32 v155, v155, v206\n"
    "v_add_f32 v156, v156, v207\n"
    "v_add_f32 v157, v157, v207\n"
    "v_add_f32 v158, v158, v207\n"
    "v_add_f32 v159, v159, v207\n"
    "v_add_f32 v236, v236, v206\n"
    "v_add_f32 v237, v237, v206\n"
    "v_add_f32 v238, v238, v206\n"
    "v_add_f32 v239, v239, v206\n"
    "v_add_f32 v244, v244, v207\n"
    "v_add_f32 v245, v245, v207\n"
    "v_add_f32 v246, v246, v207\n"
    "v_add_f32 v247, v247, v207\n"
    "v_add_f32 v248, v248, v206\n"
    "v_add_f32 v249, v249, v206\n"
    "v_add_f32 v250, v250, v206\n"
    "v_add_f32 v251, v251, v206\n"
    "v_add_f32 v252, v252, v207\n"
    "v_add_f32 v253, v253, v207\n"
    "v_add_f32 v254, v254, v207\n"
    "v_add_f32 v255, v255, v207\n"
    "s_nop 1\n"
    "s_branch .Lstubb_11\n"
    ".Lstub_12:\n"
    "s_getpc_b64 s[30:31]\n"
    "s_branch .Lrare1\n"
    "v_add_f32 v128, v128, v206\n"
    "v_add_f32 v129, v129, v206\n"
    "v_add_f32 v130, v130, v206\n"
    "v_add_f32 v131, v131, v206\n"
    "v_add_f32 v132, v132, v207\n"
    "v_add_f32 v133, v133, v207\n"
    "v_add_f32 v134, v134, v207\n"
    "v_add_f32 v135, v135, v207\n"
    "v_add_f32 v136, v136, v206\n"
    "v_add_f32 v137, v137, v206\n"
    "v_add_f32 v138, v138, v206\n"
    "v_add_f32 v139, v139, v206\n"
    "v_add_f32 v140, v140, v207\n"
    "v_add_f32 v141, v141, v207\n"
    "v_add_f32 v142, v142, v207\n"
    "v_add_f32 v143, v143, v207\n"
    "v_add_f32 v236, v236, v206\n"
    "v_add_f32 v237, v237, v206\n"
    "v_add_f32 v238, v238, v206\n"
    "v_add_f32 v239, v239, v206\n"
    "v_add_f32 v244, v244, v207\n"
    "v_add_f32 v245, v245, v207\n"
    "v_add_f32 v246, v246, v207\n"
    "v_add_f32 v247, v247, v207\n"
    "v_add_f32 v248, v248, v206\n"
    "v_add_f32 v249, v249, v206\n"
    "v_add_f32 v250, v250, v206\n"
    "v_add_f32 v251, v251, v206\n"
    "v_add_f32 v252, v252, v207\n"
    "v_add_f32 v253, v253, v207\n"
    "v_add_f32 v254, v254, v207\n"
    "v_add_f32 v255, v255, v207\n"
    "s_nop 1\n"
    "s_branch .Lstubb_13\n"
    ".Lev_14:\n"
    "s_getpc_b64 s[30:31]\n"
    "s_branch .LEV\n"
    "s_branch .LG1\n"
    ".Lev_15:\n"
    "s_add_u32 s33, s58, 114688\n"
    "s_mov_b32 s34, s55\n"
    "s_mov_b32 s72, 0\n"
    "s_branch .LFP0\n"
    ".Lstub_16:\n"
    "s_getpc_b64 s[30:31]\n"
    "s_branch .Lrare0\n"
    "s_branch .Lstubb_17\n"
    ".Llow_18:\n"
    "s_nop 7\n"
    "v_max3_f32 v204, v128, v129, v130\n"
    "v_max3_f32 v206, v131, v136, v137\n"
    "v_max3_f32 v205, v132, v133, v134\n"
    "v_max3_f32 v207, v135, v140, v141\n"
    "v_max3_f32 v204, v204, v138, v139\n"
    "v_max3_f32 v205, v205, v142, v143\n"
    "v_max_f32 v204, v204, v206\n"
    "v_max_f32 v205, v205, v207\n"
    "v_mov_b32 v206, v204\n"
    "v_mov_b32 v207, v205\n"
    "s_nop 0\n"
    "v_permlane32_swap_b32 v206, v204\n"
    "v_permlane32_swap_b32 v207, v205\n"
    "v_max_f32 v204, v204, v206\n"
    "v_max_f32 v205, v205, v207\n"
    "v_mov_b32 v206, v204\n"
    "v_mov_b32 v207, v205\n"
    "s_nop 0\n"
    "v_permlane16_swap_b32 v206, v204\n"
    "v_permlane16_swap_b32 v207, v205\n"
    "v_max_f32 v204, v204, v206\n"
    "v_max_f32 v205, v205, v207\n"
    "v_add_f32 v204, 0x41500000, v204\n"
    "v_add_f32 v205, 0x41500000, v205\n"
    "v_max_f32 v196, s62, v204\n"
    "v_max_f32 v197, s62, v205\n"
    "v_add_f32 v204, 0xc1500000, v196\n"
    "v_add_f32 v205, 0xc1500000, v197\n"
    "v_sub_f32 v128, v128, v204\n"
    "v_sub_f32 v129, v129, v204\n"
    "v_sub_f32 v130, v130, v204\n"
    "v_sub_f32 v131, v131, v204\n"
    "v_sub_f32 v132, v132, v205\n"
    "v_sub_f32 v133, v133, v205\n"
    "v_sub_f32 v134, v134, v205\n"
    "v_sub_f32 v135, v135, v205\n"
    "v_sub_f32 v136, v136, v204\n"
    "v_sub_f32 v137, v137, v204\n"
    "v_sub_f32 v138, v138, v204\n"
    "v_sub_f32 v139, v139, v204\n"
    "v_sub_f32 v140, v140, v205\n"
    "v_sub_f32 v141, v141, v205\n"
    "v_sub_f32 v142, v142, v205\n"
    "v_sub_f32 v143, v143, v205\n"
    "v_sub_f32 v236, 0, v196\n"
    "v_sub_f32 v237, 0, v196\n"
    "v_sub_f32 v238, 0, v196\n"
    "v_sub_f32 v239, 0, v196\n"
    "v_sub_f32 v244, 0, v197\n"
    "v_sub_f32 v245, 0, v197\n"
    "v_sub_f32 v246, 0, v197\n"
    "v_sub_f32 v247, 0, v197\n"
    "v_sub_f32 v248, 0, v196\n"
    "v_sub_f32 v249, 0, v196\n"
    "v_sub_f32 v250, 0, v196\n"
    "v_sub_f32 v251, 0, v196\n"
    "v_sub_f32 v252, 0, v197\n"
    "v_sub_f32 v253, 0, v197\n"
    "v_sub_f32 v254, 0, v197\n"
    "v_sub_f32 v255, 0, v197\n"
    "v_exp_f32 v240, v128\n"
    "v_exp_f32 v241, v129\n"
    "v_exp_f32 v242, v130\n"
    "v_add_f32 v216, v240, v241\n"
    "v_cvt_pk_bf16_f32 v160, v240, v241\n"
    "v_exp_f32 v243, v131\n"
    "s_nop 0\n"
    "s_branch .Llowb_19\n"
    ".Lev_20:\n"
    "s_getpc_b64 s[30:31]\n"
    "s_branch .LEV\n"
    "s_branch .LG2\n"
    ".Lev_21:\n"
    "s_add_u32 s33, s58, 65536\n"
    "s_mov_b32 s34, s54\n"
    "s_mov_b32 s72, 16384\n"
    "s_branch .LFP1\n"
    ".Lstub_22:\n"
    "s_getpc_b64 s[30:31]\n"
    "s_branch .Lrare1\n"
    "s_branch .Lstubb_23\n"
    ".Llow_24:\n"
    "s_nop 7\n"
    "v_max3_f32 v204, v144, v145, v146\n"
    "v_max3_f32 v206, v147, v152, v153\n"
    "v_max3_f32 v205, v148, v149, v150\n"
    "v_max3_f32 v207, v151, v156, v157\n"
    "v_max3_f32 v204, v204, v154, v155\n"
    "v_max3_f32 v205, v205, v158, v159\n"
    "v_max_f32 v204, v204, v206\n"
    "v_max_f32 v205, v205, v207\n"
    "v_mov_b32 v206, v204\n"
    "v_mov_b32 v207, v205\n"
    "s_nop 0\n"
    "v_permlane32_swap_b32 v206, v204\n"
    "v_permlane32_swap_b32 v207, v205\n"
    "v_max_f32 v204, v204, v206\n"
    "v_max_f32 v205, v205, v207\n"
    "v_mov_b32 v206, v204\n"
    "v_mov_b32 v207, v205\n"
    "s_nop 0\n"
    "v_permlane16_swap_b32 v206, v204\n"
    "v_permlane16_swap_b32 v207, v205\n"
    "v_max_f32 v204, v204, v206\n"
    "v_max_f32 v205, v205, v207\n"
    "v_add_f32 v204, 0x41500000, v204\n"
    "v_add_f32 v205, 0x41500000, v205\n"
    "v_max_f32 v196, s62, v204\n"
    "v_max_f32 v197, s62, v205\n"
    "v_add_f32 v204, 0xc1500000, v196\n"
    "v_add_f32 v205, 0xc1500000, v197\n"
    "v_sub_f32 v144, v144, v204\n"
    "v_sub_f32 v145, v145, v204\n"
    "v_sub_f32 v146, v146, v204\n"
    "v_sub_f32 v147, v147, v204\n"
    "v_sub_f32 v148, v148, v205\n"
    "v_sub_f32 v149, v149, v205\n"
    "v_sub_f32 v150, v150, v205\n"
    "v_sub_f32 v151, v151, v205\n"
    "v_sub_f32 v152, v152, v204\n"
    "v_sub_f32 v153, v153, v204\n"
    "v_sub_f32 v154, v154, v204\n"
    "v_sub_f32 v155, v155, v204\n"
    "v_sub_f32 v156, v156, v205\n"
    "v_sub_f32 v157, v157, v205\n"
    "v_sub_f32 v158, v158, v205\n"
    "v_sub_f32 v159, v159, v205\n"
    "v_sub_f32 v236, 0, v196\n"
    "v_sub_f32 v237, 0, v196\n"
    "v_sub_f32 v238, 0, v196\n"
    "v_sub_f32 v239, 0, v196\n"
    "v_sub_f32 v244, 0, v197\n"
    "v_sub_f32 v245, 0, v197\n"
    "v_sub_f32 v246, 0, v197\n"
    "v_sub_f32 v247, 0, v197\n"
    "v_sub_f32 v248, 0, v196\n"
    "v_sub_f32 v249, 0, v196\n"
    "v_sub_f32 v250, 0, v196\n"
    "v_sub_f32 v251, 0, v196\n"
    "v_sub_f32 v252, 0, v197\n"
    "v_sub_f32 v253, 0, v197\n"
    "v_sub_f32 v254, 0, v197\n"
    "v_sub_f32 v255, 0, v197\n"
    "v_exp_f32 v240, v144\n"
    "v_exp_f32 v241, v145\n"
    "v_exp_f32 v242, v146\n"
    "v_add_f32 v216, v240, v241\n"
    "v_cvt_pk_bf16_f32 v168, v240, v241\n"
    "v_exp_f32 v243, v147\n"
    "s_nop 0\n"
    "s_branch .Llowb_25\n"
    ".Lev_26:\n"
    "s_getpc_b64 s[30:31]\n"
    "s_branch .LEV\n"
    "s_branch .LG3\n"
    ".Lev_27:\n"
    "s_add_u32 s33, s58, 81920\n"
    "s_mov_b32 s34, s55\n"
    "s_mov_b32 s72, 32768\n"
    "s_branch .LFP0\n"
    ".Lstub_28:\n"
    "s_getpc_b64 s[30:31]\n"
    "s_branch .Lrare0\n"
    "s_branch .Lstubb_29\n"
    ".Llow_30:\n"
    "s_nop 7\n"
    "v_max3_f32 v204, v128, v129, v130\n"
    "v_max3_f32 v206, v131, v136, v137\n"
    "v_max3_f32 v205, v132, v133, v134\n"
    "v_max3_f32 v207, v135, v140, v141\n"
    "v_max3_f32 v204, v204, v138, v139\n"
    "v_max3_f32 v205, v205, v142, v143\n"
    "v_max_f32 v204, v204, v206\n"
    "v_max_f32 v205, v205, v207\n"
    "v_mov_b32 v206, v204\n"
    "v_mov_b32 v207, v205\n"
    "s_nop 0\n"
    "v_permlane32_swap_b32 v206, v204\n"
    "v_permlane32_swap_b32 v207, v205\n"
    "v_max_f32 v204, v204, v206\n"
    "v_max_f32 v205, v205, v207\n"
    "v_mov_b32 v206, v204\n"
    "v_mov_b32 v207, v205\n"
    "s_nop 0\n"
    "v_permlane16_swap_b32 v206, v204\n"
    "v_permlane16_swap_b32 v207, v205\n"
    "v_max_f32 v204, v204, v206\n"
    "v_max_f32 v205, v205, v207\n"
    "v_add_f32 v204, 0x41500000, v204\n"
    "v_add_f32 v205, 0x41500000, v205\n"
    "v_max_f32 v196, s62, v204\n"
    "v_max_f32 v197, s62, v205\n"
    "v_add_f32 v204, 0xc1500000, v196\n"
    "v_add_f32 v205, 0xc1500000, v197\n"
    "v_sub_f32 v128, v128, v204\n"
    "v_sub_f32 v129, v129, v204\n"
    "v_sub_f32 v130, v130, v204\n"
    "v_sub_f32 v131, v131, v204\n"
    "v_sub_f32 v132, v132, v205\n"
    "v_sub_f32 v133, v133, v205\n"
    "v_sub_f32 v134, v134, v205\n"
    "v_sub_f32 v135, v135, v205\n"
    "v_sub_f32 v136, v136, v204\n"
    "v_sub_f32 v137, v137, v204\n"
    "v_sub_f32 v138, v138, v204\n"
    "v_sub_f32 v139, v139, v204\n"
    "v_sub_f32 v140, v140, v205\n"
    "v_sub_f32 v141, v141, v205\n"
    "v_sub_f32 v142, v142, v205\n"
    "v_sub_f32 v143, v143, v205\n"
    "v_sub_f32 v236, 0, v196\n"
    "v_sub_f32 v237, 0, v196\n"
    "v_sub_f32 v238, 0, v196\n"
    "v_sub_f32 v239, 0, v196\n"
    "v_sub_f32 v244, 0, v197\n"
    "v_sub_f32 v245, 0, v197\n"
    "v_sub_f32 v246, 0, v197\n"
    "v_sub_f32 v247, 0, v197\n"
    "v_sub_f32 v248, 0, v196\n"
    "v_sub_f32 v249, 0, v196\n"
    "v_sub_f32 v250, 0, v196\n"
    "v_sub_f32 v251, 0, v196\n"
    "v_sub_f32 v252, 0, v197\n"
    "v_sub_f32 v253, 0, v197\n"
    "v_sub_f32 v254, 0, v197\n"
    "v_sub_f32 v255, 0, v197\n"
    "v_exp_f32 v240, v128\n"
    "v_exp_f32 v241, v129\n"
    "v_exp_f32 v242, v130\n"
    "v_add_f32 v216, v240, v241\n"
    "v_cvt_pk_bf16_f32 v160, v240, v241\n"
    "v_exp_f32 v243, v131\n"
    "s_nop 0\n"
    "s_branch .Llowb_31\n"
    ".Lev_32:\n"
    "s_getpc_b64 s[30:31]\n"
    "s_branch .LEV\n"
    "s_branch .LG0\n"
    ".Lev_33:\n"
    "s_add_u32 s33, s58, 98304\n"
    "s_mov_b32 s34, s54\n"
    "s_mov_b32 s72, 49152\n"
    "s_branch .LFP1\n"
    ".Lstub_34:\n"
    "s_getpc_b64 s[30:31]\n"
    "s_branch .Lrare1\n"
    "s_branch .Lstubb_35\n"
    ".Llow_36:\n"
    "s_nop 7\n"
    "v_max3_f32 v204, v144, v145, v146\n"
    "v_max3_f32 v206, v147, v152, v153\n"
    "v_max3_f32 v205, v148, v149, v150\n"
    "v_max3_f32 v207, v151, v156, v157\n"
    "v_max3_f32 v204, v204, v154, v155\n"
    "v_max3_f32 v205, v205, v158, v159\n"
    "v_max_f32 v204, v204, v206\n"
    "v_max_f32 v205, v205, v207\n"
    "v_mov_b32 v206, v204\n"
    "v_mov_b32 v207, v205\n"
    "s_nop 0\n"
    "v_permlane32_swap_b32 v206, v204\n"
    "v_permlane32_swap_b32 v207, v205\n"
    "v_max_f32 v204, v204, v206\n"
    "v_max_f32 v205, v205, v207\n"
    "v_mov_b32 v206, v204\n"
    "v_mov_b32 v207, v205\n"
    "s_nop 0\n"
    "v_permlane16_swap_b32 v206, v204\n"
    "v_permlane16_swap_b32 v207, v205\n"
    "v_max_f32 v204, v204, v206\n"
    "v_max_f32 v205, v205, v207\n"
    "v_add_f32 v204, 0x41500000, v204\n"
    "v_add_f32 v205, 0x41500000, v205\n"
    "v_max_f32 v196, s62, v204\n"
    "v_max_f32 v197, s62, v205\n"
    "v_add_f32 v204, 0xc1500000, v196\n"
    "v_add_f32 v205, 0xc1500000, v197\n"
    "v_sub_f32 v144, v144, v204\n"
    "v_sub_f32 v145, v145, v204\n"
    "v_sub_f32 v146, v146, v204\n"
    "v_sub_f32 v147, v147, v204\n"
    "v_sub_f32 v148, v148, v205\n"
    "v_sub_f32 v149, v149, v205\n"
    "v_sub_f32 v150, v150, v205\n"
    "v_sub_f32 v151, v151, v205\n"
    "v_sub_f32 v152, v152, v204\n"
    "v_sub_f32 v153, v153, v204\n"
    "v_sub_f32 v154, v154, v204\n"
    "v_sub_f32 v155, v155, v204\n"
    "v_sub_f32 v156, v156, v205\n"
    "v_sub_f32 v157, v157, v205\n"
    "v_sub_f32 v158, v158, v205\n"
    "v_sub_f32 v159, v159, v205\n"
    "v_sub_f32 v236, 0, v196\n"
    "v_sub_f32 v237, 0, v196\n"
    "v_sub_f32 v238, 0, v196\n"
    "v_sub_f32 v239, 0, v196\n"
    "v_sub_f32 v244, 0, v197\n"
    "v_sub_f32 v245, 0, v197\n"
    "v_sub_f32 v246, 0, v197\n"
    "v_sub_f32 v247, 0, v197\n"
    "v_sub_f32 v248, 0, v196\n"
    "v_sub_f32 v249, 0, v196\n"
    "v_sub_f32 v250, 0, v196\n"
    "v_sub_f32 v251, 0, v196\n"
    "v_sub_f32 v252, 0, v197\n"
    "v_sub_f32 v253, 0, v197\n"
    "v_sub_f32 v254, 0, v197\n"
    "v_sub_f32 v255, 0, v197\n"
    "v_exp_f32 v240, v144\n"
    "v_exp_f32 v241, v145\n"
    "v_exp_f32 v242, v146\n"
    "v_add_f32 v216, v240, v241\n"
    "v_cvt_pk_bf16_f32 v168, v240, v241\n"
    "v_exp_f32 v243, v147\n"
    "s_nop 0\n"
    "s_branch .Llowb_37\n"
    ".LEV:\n"
    "s_mov_b32 s65, -1\n"
    "s_sub_u32 s63, s63, 1\n"
    "s_cmp_eq_u32 s67, 1\n"
    "s_cbranch_scc1 .LEVB\n"
    "s_cmp_lg_u32 s67, 0\n"
    "s_cbranch_scc1 .LEV2\n"
    "s_add_u32 m0, s91, 0\n"
    "s_mov_b32 s75, 0\n"
    "buffer_load_dwordx4 v228, s[92:95], s75 offen offset:0 lds\n"
    "buffer_load_dwordx4 v229, s[92:95], s75 offen offset:1024 lds\n"
    "buffer_load_dwordx4 v230, s[92:95], s75 offen offset:2048 lds\n"
    "buffer_load_dwordx4 v231, s[92:95], s75 offen offset:3072 lds\n"
    "s_add_u32 m0, s91, 4096\n"
    "s_mov_b32 s75, 8192\n"
    "buffer_load_dwordx4 v228, s[92:95], s75 offen offset:0 lds\n"
    "buffer_load_dwordx4 v229, s[92:95], s75 offen offset:1024 lds\n"
    "buffer_load_dwordx4 v230, s[92:95], s75 offen offset:2048 lds\n"
    "buffer_load_dwordx4 v231, s[92:95], s75 offen offset:3072 lds\n"
    ".LEV2:\n"
    "v_mov_b32 v248, 0xf149f2ca\n"
    "v_mov_b32 v249, 0xf149f2ca\n"
    "v_mov_b32 v250, 0xf149f2ca\n"
    "v_mov_b32 v251, 0xf149f2ca\n"
    "v_cmp_gt_i32 s[80:81], 0, v219\n"
    "v_cmp_gt_i32 s[82:83], 1, v219\n"
    "v_cmp_gt_i32 s[84:85], 2, v219\n"
    "v_cmp_gt_i32 s[86:87], 3, v219\n"
    "s_nop 0\n"
    "v_cndmask_b32 v236, v236, v248, s[80:81]\n"
    "v_cndmask_b32 v237, v237, v248, s[82:83]\n"
    "v_cndmask_b32 v238, v238, v248, s[84:85]\n"
    "v_cndmask_b32 v239, v239, v248, s[86:87]\n"
    "v_cndmask_b32 v252, v252, v248, s[80:81]\n"
    "v_cndmask_b32 v253, v253, v248, s[82:83]\n"
    "v_cndmask_b32 v254, v254, v248, s[84:85]\n"
    "v_cndmask_b32 v255, v255, v248, s[86:87]\n"
    "s_nop 0\n"
    "s_add_u32 s30, s30, 4\n"
    "s_addc_u32 s31, s31, 0\n"
    "s_setpc_b64 s[30:31]\n"
    ".LEVB:\n"
    "s_cmp_eq_u32 s63, s71\n"
    "s_cbranch_scc1 .LPF\n"
    "s_cmp_eq_u32 s28, 2\n"
    "s_cbranch_scc1 .LPR\n"
    ".LPAY:\n"
    "s_cmp_eq_u32 s28, 1\n"
    "s_cbranch_scc1 .LS1\n"
    "s_waitcnt vmcnt(8)\n"
    "v_readfirstlane_b32 s73, v212\n"
    "s_nop 3\n"
    "s_cmp_lg_u32 s73, s88\n"
    "s_cbranch_scc1 .LPF\n"
    ".LS0:\n"
    "s_mov_b32 s28, 1\n"
    "global_load_dwordx4 a[64:67], v232, s[50:51] offset:0 sc1\n"
    "global_load_dwordx4 a[68:71], v232, s[50:51] offset:2048 sc1\n"
    "global_load_dwordx4 a[72:75], v233, s[50:51] offset:0 sc1\n"
    "global_load_dwordx4 a[76:79], v233, s[50:51] offset:2048 sc1\n"
    "global_load_dwordx4 a[80:83], v234, s[50:51] offset:0 sc1\n"
    "global_load_dwordx4 a[84:87], v234, s[50:51] offset:2048 sc1\n"
    "global_load_dwordx4 a[88:91], v235, s[50:51] offset:0 sc1\n"
    "global_load_dwordx4 a[92:95], v235, s[50:51] offset:2048 sc1\n"
    "s_cmp_eq_u32 s66, 0\n"
    "s_cbranch_scc1 .LPR\n"
    ".LS1:\n"
    "s_mov_b32 s28, 2\n"
    "global_load_dwordx4 a[96:99], v232, s[50:51] offset:256 sc1\n"
    "global_load_dwordx4 a[100:103], v232, s[50:51] offset:2304 sc1\n"
    "global_load_dwordx4 a[104:107], v233, s[50:51] offset:256 sc1\n"
    "global_load_dwordx4 a[108:111], v233, s[50:51] offset:2304 sc1\n"
    "global_load_dwordx4 a[112:115], v234, s[50:51] offset:256 sc1\n"
    "global_load_dwordx4 a[116:119], v234, s[50:51] offset:2304 sc1\n"
    "global_load_dwordx4 a[120:123], v235, s[50:51] offset:256 sc1\n"
    "global_load_dwordx4 a[124:127], v235, s[50:51] offset:2304 sc1\n"
    "s_sub_u32 s65, -1, s63\n"
    "s_mov_b32 s63, 0\n"
    "s_add_u32 s30, s30, 4\n"
    "s_addc_u32 s31, s31, 0\n"
    "s_setpc_b64 s[30:31]\n"
    ".LPF:\n"
    "global_load_dword v212, v213, s[52:53] offset:64 sc1\n"
    ".LPR:\n"
    "s_add_u32 s30, s30, 4\n"
    "s_addc_u32 s31, s31, 0\n"
    "s_setpc_b64 s[30:31]\n"
    ".Lnp_39:\n"
    "s_mov_b32 s66, 1\n"
    "s_cmp_eq_u32 s28, 2\n"
    "s_cbranch_scc1 .Lnp_40\n"
    "s_getpc_b64 s[30:31]\n"
    "s_branch .LPAY\n"
    "s_cmp_eq_u32 s28, 0\n"
    "s_cbranch_scc1 .Lnp_38\n"
    ".Lnp_40:\n"
    "global_load_dwordx4 v[200:203], v214, s[44:45] offset:0 sc1\n"
    "v_mov_b32 v215, 1\n"
    "global_store_dword v213, v215, s[52:53] offset:128 sc1\n"
    "s_branch .Lnp_38\n"
    ".Lstub_41:\n"
    "s_getpc_b64 s[30:31]\n"
    "s_branch .Lrare0\n"
    "s_branch .Lstubb_42\n"
    ".Lnp_44:\n"
    "s_mov_b32 s66, 1\n"
    "s_cmp_eq_u32 s28, 2\n"
    "s_cbranch_scc1 .Lnp_45\n"
    "s_getpc_b64 s[30:31]\n"
    "s_branch .LPAY\n"
    "s_cmp_eq_u32 s28, 0\n"
    "s_cbranch_scc1 .Lnp_43\n"
    ".Lnp_45:\n"
    "global_load_dwordx4 v[200:203], v214, s[44:45] offset:0 sc1\n"
    "v_mov_b32 v215, 1\n"
    "global_store_dword v213, v215, s[52:53] offset:128 sc1\n"
    "s_branch .Lnp_43\n"
    ".Lstub_46:\n"
    "s_getpc_b64 s[30:31]\n"
    "s_branch .Lrare1\n"
    "s_branch .Lstubb_47\n"
    ".Lmrare_51:\n"
    "v_max_f32 v204, v200, v196\n"
    "v_sub_f32 v206, v196, v204\n"
    "v_exp_f32 v208, v206\n"
    "v_mov_b32 v196, v204\n"
    "v_max_f32 v205, v201, v197\n"
    "v_sub_f32 v207, v197, v205\n"
    "v_exp_f32 v209, v207\n"
    "v_mov_b32 v197, v205\n"
    "s_nop 7\n"
    "v_mul_f32 v198, v198, v208\n"
    "v_mul_f32 v199, v199, v209\n"
    "v_mul_f32 v0, v0, v208\n"
    "v_mul_f32 v1, v1, v208\n"
    "v_mul_f32 v2, v2, v208\n"
    "v_mul_f32 v3, v3, v208\n"
    "v_mul_f32 v4, v4, v209\n"
    "v_mul_f32 v5, v5, v209\n"
    "v_mul_f32 v6, v6, v209\n"
    "v_mul_f32 v7, v7, v209\n"
    "v_mul_f32 v8, v8, v208\n"
    "v_mul_f32 v9, v9, v208\n"
    "v_mul_f32 v10, v10, v208\n"
    "v_mul_f32 v11, v11, v208\n"
    "v_mul_f32 v12, v12, v209\n"
    "v_mul_f32 v13, v13, v209\n"
    "v_mul_f32 v14, v14, v209\n"
    "v_mul_f32 v15, v15, v209\n"
    "v_mul_f32 v16, v16, v208\n"
    "v_mul_f32 v17, v17, v208\n"
    "v_mul_f32 v18, v18, v208\n"
    "v_mul_f32 v19, v19, v208\n"
    "v_mul_f32 v20, v20, v209\n"
    "v_mul_f32 v21, v21, v209\n"
    "v_mul_f32 v22, v22, v209\n"
    "v_mul_f32 v23, v23, v209\n"
    "v_mul_f32 v24, v24, v208\n"
    "v_mul_f32 v25, v25, v208\n"
    "v_mul_f32 v26, v26, v208\n"
    "v_mul_f32 v27, v27, v208\n"
    "v_mul_f32 v28, v28, v209\n"
    "v_mul_f32 v29, v29, v209\n"
    "v_mul_f32 v30, v30, v209\n"
    "v_mul_f32 v31, v31, v209\n"
    "v_mul_f32 v32, v32, v208\n"
    "v_mul_f32 v33, v33, v208\n"
    "v_mul_f32 v34, v34, v208\n"
    "v_mul_f32 v35, v35, v208\n"
    "v_mul_f32 v36, v36, v209\n"
    "v_mul_f32 v37, v37, v209\n"
    "v_mul_f32 v38, v38, v209\n"
    "v_mul_f32 v39, v39, v209\n"
    "v_mul_f32 v40, v40, v208\n"
    "v_mul_f32 v41, v41, v208\n"
    "v_mul_f32 v42, v42, v208\n"
    "v_mul_f32 v43, v43, v208\n"
    "v_mul_f32 v44, v44, v209\n"
    "v_mul_f32 v45, v45, v209\n"
    "v_mul_f32 v46, v46, v209\n"
    "v_mul_f32 v47, v47, v209\n"
    "v_mul_f32 v48, v48, v208\n"
    "v_mul_f32 v49, v49, v208\n"
    "v_mul_f32 v50, v50, v208\n"
    "v_mul_f32 v51, v51, v208\n"
    "v_mul_f32 v52, v52, v209\n"
    "v_mul_f32 v53, v53, v209\n"
    "v_mul_f32 v54, v54, v209\n"
    "v_mul_f32 v55, v55, v209\n"
    "v_mul_f32 v56, v56, v208\n"
    "v_mul_f32 v57, v57, v208\n"
    "v_mul_f32 v58, v58, v208\n"
    "v_mul_f32 v59, v59, v208\n"
    "v_mul_f32 v60, v60, v209\n"
    "v_mul_f32 v61, v61, v209\n"
    "v_mul_f32 v62, v62, v209\n"
    "v_mul_f32 v63, v63, v209\n"
    "v_mul_f32 v64, v64, v208\n"
    "v_mul_f32 v65, v65, v208\n"
    "v_mul_f32 v66, v66, v208\n"
    "v_mul_f32 v67, v67, v208\n"
    "v_mul_f32 v68, v68, v209\n"
    "v_mul_f32 v69, v69, v209\n"
    "v_mul_f32 v70, v70, v209\n"
    "v_mul_f32 v71, v71, v209\n"
    "v_mul_f32 v72, v72, v208\n"
    "v_mul_f32 v73, v73, v208\n"
    "v_mul_f32 v74, v74, v208\n"
    "v_mul_f32 v75, v75, v208\n"
    "v_mul_f32 v76, v76, v209\n"
    "v_mul_f32 v77, v77, v209\n"
    "v_mul_f32 v78, v78, v209\n"
    "v_mul_f32 v79, v79, v209\n"
    "v_mul_f32 v80, v80, v208\n"
    "v_mul_f32 v81, v81, v208\n"
    "v_mul_f32 v82, v82, v208\n"
    "v_mul_f32 v83, v83, v208\n"
    "v_mul_f32 v84, v84, v209\n"
    "v_mul_f32 v85, v85, v209\n"
    "v_mul_f32 v86, v86, v209\n"
    "v_mul_f32 v87, v87, v209\n"
    "v_mul_f32 v88, v88, v208\n"
    "v_mul_f32 v89, v89, v208\n"
    "v_mul_f32 v90, v90, v208\n"
    "v_mul_f32 v91, v91, v208\n"
    "v_mul_f32 v92, v92, v209\n"
    "v_mul_f32 v93, v93, v209\n"
    "v_mul_f32 v94, v94, v209\n"
    "v_mul_f32 v95, v95, v209\n"
    "v_mul_f32 v96, v96, v208\n"
    "v_mul_f32 v97, v97, v208\n"
    "v_mul_f32 v98, v98, v208\n"
    "v_mul_f32 v99, v99, v208\n"
    "v_mul_f32 v100, v100, v209\n"
    "v_mul_f32 v101, v101, v209\n"
    "v_mul_f32 v102, v102, v209\n"
    "v_mul_f32 v103, v103, v209\n"
    "v_mul_f32 v104, v104, v208\n"
    "v_mul_f32 v105, v105, v208\n"
    "v_mul_f32 v106, v106, v208\n"
    "v_mul_f32 v107, v107, v208\n"
    "v_mul_f32 v108, v108, v209\n"
    "v_mul_f32 v109, v109, v209\n"
    "v_mul_f32 v110, v110, v209\n"
    "v_mul_f32 v111, v111, v209\n"
    "v_mul_f32 v112, v112, v208\n"
    "v_mul_f32 v113, v113, v208\n"
    "v_mul_f32 v114, v114, v208\n"
    "v_mul_f32 v115, v115, v208\n"
    "v_mul_f32 v116, v116, v209\n"
    "v_mul_f32 v117, v117, v209\n"
    "v_mul_f32 v118, v118, v209\n"
    "v_mul_f32 v119, v119, v209\n"
    "v_mul_f32 v120, v120, v208\n"
    "v_mul_f32 v121, v121, v208\n"
    "v_mul_f32 v122, v122, v208\n"
    "v_mul_f32 v123, v123, v208\n"
    "v_mul_f32 v124, v124, v209\n"
    "v_mul_f32 v125, v125, v209\n"
    "v_mul_f32 v126, v126, v209\n"
    "v_mul_f32 v127, v127, v209\n"
    "v_sub_f32 v206, v200, v196\n"
    "v_sub_f32 v207, v201, v197\n"
    "s_branch .Lmrareb_52\n"
    ".Lrare0:\n"
    "v_max3_f32 v204, v128, v129, v130\n"
    "v_max3_f32 v206, v131, v136, v137\n"
    "v_max3_f32 v205, v132, v133, v134\n"
    "v_max3_f32 v207, v135, v140, v141\n"
    "v_max3_f32 v204, v204, v138, v139\n"
    "v_max3_f32 v205, v205, v142, v143\n"
    "v_max_f32 v204, v204, v206\n"
    "v_max_f32 v205, v205, v207\n"
    "v_mov_b32 v206, v204\n"
    "v_mov_b32 v207, v205\n"
    "s_nop 0\n"
    "v_permlane32_swap_b32 v206, v204\n"
    "v_permlane32_swap_b32 v207, v205\n"
    "v_max_f32 v204, v204, v206\n"
    "v_max_f32 v205, v205, v207\n"
    "v_mov_b32 v206, v204\n"
    "v_mov_b32 v207, v205\n"
    "s_nop 0\n"
    "v_permlane16_swap_b32 v206, v204\n"
    "v_permlane16_swap_b32 v207, v205\n"
    "v_max_f32 v204, v204, v206\n"
    "v_max_f32 v205, v205, v207\n"
    "v_add_f32 v204, v196, v204\n"
    "v_max_f32 v204, v204, v196\n"
    "v_sub_f32 v206, v196, v204\n"
    "v_exp_f32 v208, v206\n"
    "v_mov_b32 v196, v204\n"
    "v_add_f32 v205, v197, v205\n"
    "v_max_f32 v205, v205, v197\n"
    "v_sub_f32 v207, v197, v205\n"
    "v_exp_f32 v209, v207\n"
    "v_mov_b32 v197, v205\n"
    "v_mul_f32 v198, v198, v208\n"
    "v_mul_f32 v199, v199, v209\n"
    "v_mul_f32 v0, v0, v208\n"
    "v_mul_f32 v1, v1, v208\n"
    "v_mul_f32 v2, v2, v208\n"
    "v_mul_f32 v3, v3, v208\n"
    "v_mul_f32 v4, v4, v209\n"
    "v_mul_f32 v5, v5, v209\n"
    "v_mul_f32 v6, v6, v209\n"
    "v_mul_f32 v7, v7, v209\n"
    "v_mul_f32 v8, v8, v208\n"
    "v_mul_f32 v9, v9, v208\n"
    "v_mul_f32 v10, v10, v208\n"
    "v_mul_f32 v11, v11, v208\n"
    "v_mul_f32 v12, v12, v209\n"
    "v_mul_f32 v13, v13, v209\n"
    "v_mul_f32 v14, v14, v209\n"
    "v_mul_f32 v15, v15, v209\n"
    "v_mul_f32 v16, v16, v208\n"
    "v_mul_f32 v17, v17, v208\n"
    "v_mul_f32 v18, v18, v208\n"
    "v_mul_f32 v19, v19, v208\n"
    "v_mul_f32 v20, v20, v209\n"
    "v_mul_f32 v21, v21, v209\n"
    "v_mul_f32 v22, v22, v209\n"
    "v_mul_f32 v23, v23, v209\n"
    "v_mul_f32 v24, v24, v208\n"
    "v_mul_f32 v25, v25, v208\n"
    "v_mul_f32 v26, v26, v208\n"
    "v_mul_f32 v27, v27, v208\n"
    "v_mul_f32 v28, v28, v209\n"
    "v_mul_f32 v29, v29, v209\n"
    "v_mul_f32 v30, v30, v209\n"
    "v_mul_f32 v31, v31, v209\n"
    "v_mul_f32 v32, v32, v208\n"
    "v_mul_f32 v33, v33, v208\n"
    "v_mul_f32 v34, v34, v208\n"
    "v_mul_f32 v35, v35, v208\n"
    "v_mul_f32 v36, v36, v209\n"
    "v_mul_f32 v37, v37, v209\n"
    "v_mul_f32 v38, v38, v209\n"
    "v_mul_f32 v39, v39, v209\n"
    "v_mul_f32 v40, v40, v208\n"
    "v_mul_f32 v41, v41, v208\n"
    "v_mul_f32 v42, v42, v208\n"
    "v_mul_f32 v43, v43, v208\n"
    "v_mul_f32 v44, v44, v209\n"
    "v_mul_f32 v45, v45, v209\n"
    "v_mul_f32 v46, v46, v209\n"
    "v_mul_f32 v47, v47, v209\n"
    "v_mul_f32 v48, v48, v208\n"
    "v_mul_f32 v49, v49, v208\n"
    "v_mul_f32 v50, v50, v208\n"
    "v_mul_f32 v51, v51, v208\n"
    "v_mul_f32 v52, v52, v209\n"
    "v_mul_f32 v53, v53, v209\n"
    "v_mul_f32 v54, v54, v209\n"
    "v_mul_f32 v55, v55, v209\n"
    "v_mul_f32 v56, v56, v208\n"
    "v_mul_f32 v57, v57, v208\n"
    "v_mul_f32 v58, v58, v208\n"
    "v_mul_f32 v59, v59, v208\n"
    "v_mul_f32 v60, v60, v209\n"
    "v_mul_f32 v61, v61, v209\n"
    "v_mul_f32 v62, v62, v209\n"
    "v_mul_f32 v63, v63, v209\n"
    "v_mul_f32 v64, v64, v208\n"
    "v_mul_f32 v65, v65, v208\n"
    "v_mul_f32 v66, v66, v208\n"
    "v_mul_f32 v67, v67, v208\n"
    "v_mul_f32 v68, v68, v209\n"
    "v_mul_f32 v69, v69, v209\n"
    "v_mul_f32 v70, v70, v209\n"
    "v_mul_f32 v71, v71, v209\n"
    "v_mul_f32 v72, v72, v208\n"
    "v_mul_f32 v73, v73, v208\n"
    "v_mul_f32 v74, v74, v208\n"
    "v_mul_f32 v75, v75, v208\n"
    "v_mul_f32 v76, v76, v209\n"
    "v_mul_f32 v77, v77, v209\n"
    "v_mul_f32 v78, v78, v209\n"
    "v_mul_f32 v79, v79, v209\n"
    "v_mul_f32 v80, v80, v208\n"
    "v_mul_f32 v81, v81, v208\n"
    "v_mul_f32 v82, v82, v208\n"
    "v_mul_f32 v83, v83, v208\n"
    "v_mul_f32 v84, v84, v209\n"
    "v_mul_f32 v85, v85, v209\n"
    "v_mul_f32 v86, v86, v209\n"
    "v_mul_f32 v87, v87, v209\n"
    "v_mul_f32 v88, v88, v208\n"
    "v_mul_f32 v89, v89, v208\n"
    "v_mul_f32 v90, v90, v208\n"
    "v_mul_f32 v91, v91, v208\n"
    "v_mul_f32 v92, v92, v209\n"
    "v_mul_f32 v93, v93, v209\n"
    "v_mul_f32 v94, v94, v209\n"
    "v_mul_f32 v95, v95, v209\n"
    "v_mul_f32 v96, v96, v208\n"
    "v_mul_f32 v97, v97, v208\n"
    "v_mul_f32 v98, v98, v208\n"
    "v_mul_f32 v99, v99, v208\n"
    "v_mul_f32 v100, v100, v209\n"
    "v_mul_f32 v101, v101, v209\n"
    "v_mul_f32 v102, v102, v209\n"
    "v_mul_f32 v103, v103, v209\n"
    "v_mul_f32 v104, v104, v208\n"
    "v_mul_f32 v105, v105, v208\n"
    "v_mul_f32 v106, v106, v208\n"
    "v_mul_f32 v107, v107, v208\n"
    "v_mul_f32 v108, v108, v209\n"
    "v_mul_f32 v109, v109, v209\n"
    "v_mul_f32 v110, v110, v209\n"
    "v_mul_f32 v111, v111, v209\n"
    "v_mul_f32 v112, v112, v208\n"
    "v_mul_f32 v113, v113, v208\n"
    "v_mul_f32 v114, v114, v208\n"
    "v_mul_f32 v115, v115, v208\n"
    "v_mul_f32 v116, v116, v209\n"
    "v_mul_f32 v117, v117, v209\n"
    "v_mul_f32 v118, v118, v209\n"
    "v_mul_f32 v119, v119, v209\n"
    "v_mul_f32 v120, v120, v208\n"
    "v_mul_f32 v121, v121, v208\n"
    "v_mul_f32 v122, v122, v208\n"
    "v_mul_f32 v123, v123, v208\n"
    "v_mul_f32 v124, v124, v209\n"
    "v_mul_f32 v125, v125, v209\n"
    "v_mul_f32 v126, v126, v209\n"
    "v_mul_f32 v127, v127, v209\n"
    "v_add_f32 v128, v128, v206\n"
    "v_add_f32 v129, v129, v206\n"
    "v_add_f32 v130, v130, v206\n"
    "v_add_f32 v131, v131, v206\n"
    "v_add_f32 v132, v132, v207\n"
    "v_add_f32 v133, v133, v207\n"
    "v_add_f32 v134, v134, v207\n"
    "v_add_f32 v135, v135, v207\n"
    "v_add_f32 v136, v136, v206\n"
    "v_add_f32 v137, v137, v206\n"
    "v_add_f32 v138, v138, v206\n"
    "v_add_f32 v139, v139, v206\n"
    "v_add_f32 v140, v140, v207\n"
    "v_add_f32 v141, v141, v207\n"
    "v_add_f32 v142, v142, v207\n"
    "v_add_f32 v143, v143, v207\n"
    "v_exp_f32 v240, v128\n"
    "s_nop 0\n"
    "v_exp_f32 v241, v129\n"
    "s_nop 0\n"
    "v_exp_f32 v242, v130\n"
    "s_nop 0\n"
    "v_add_f32 v216, v240, v241\n"
    "v_cvt_pk_bf16_f32 v160, v240, v241\n"
    "v_exp_f32 v243, v131\n"
    "s_nop 0\n"
    "v_add_f32 v216, v216, v242\n"
    "v_exp_f32 v240, v132\n"
    "s_nop 0\n"
    "v_add_f32 v216, v216, v243\n"
    "v_cvt_pk_bf16_f32 v161, v242, v243\n"
    "v_exp_f32 v241, v133\n"
    "s_nop 0\n"
    "v_exp_f32 v242, v134\n"
    "s_nop 0\n"
    "v_add_f32 v217, v240, v241\n"
    "v_cvt_pk_bf16_f32 v164, v240, v241\n"
    "v_exp_f32 v243, v135\n"
    "s_nop 0\n"
    "v_add_f32 v217, v217, v242\n"
    "v_exp_f32 v240, v136\n"
    "s_nop 0\n"
    "v_add_f32 v217, v217, v243\n"
    "v_cvt_pk_bf16_f32 v165, v242, v243\n"
    "v_exp_f32 v241, v137\n"
    "s_nop 0\n"
    "v_add_f32 v216, v216, v240\n"
    "v_exp_f32 v242, v138\n"
    "s_nop 0\n"
    "v_add_f32 v216, v216, v241\n"
    "v_cvt_pk_bf16_f32 v162, v240, v241\n"
    "v_exp_f32 v243, v139\n"
    "s_nop 0\n"
    "v_add_f32 v216, v216, v242\n"
    "v_exp_f32 v240, v140\n"
    "s_nop 0\n"
    "v_add_f32 v216, v216, v243\n"
    "v_cvt_pk_bf16_f32 v163, v242, v243\n"
    "v_exp_f32 v241, v141\n"
    "s_nop 0\n"
    "v_add_f32 v217, v217, v240\n"
    "v_exp_f32 v242, v142\n"
    "s_nop 0\n"
    "v_add_f32 v217, v217, v241\n"
    "v_exp_f32 v243, v143\n"
    "s_nop 0\n"
    "v_add_f32 v217, v217, v242\n"
    "v_cvt_pk_bf16_f32 v166, v240, v241\n"
    "v_add_f32 v217, v217, v243\n"
    "v_add_f32 v215, v216, v217\n"
    "v_cmp_lt_f32 vcc, s29, v215\n"
    "v_cvt_pk_bf16_f32 v167, v242, v243\n"
    "s_add_u32 s30, s30, 4\n"
    "s_addc_u32 s31, s31, 0\n"
    "s_setpc_b64 s[30:31]\n"
    ".Lrare1:\n"
    "v_max3_f32 v204, v144, v145, v146\n"
    "v_max3_f32 v206, v147, v152, v153\n"
    "v_max3_f32 v205, v148, v149, v150\n"
    "v_max3_f32 v207, v151, v156, v157\n"
    "v_max3_f32 v204, v204, v154, v155\n"
    "v_max3_f32 v205, v205, v158, v159\n"
    "v_max_f32 v204, v204, v206\n"
    "v_max_f32 v205, v205, v207\n"
    "v_mov_b32 v206, v204\n"
    "v_mov_b32 v207, v205\n"
    "s_nop 0\n"
    "v_permlane32_swap_b32 v206, v204\n"
    "v_permlane32_swap_b32 v207, v205\n"
    "v_max_f32 v204, v204, v206\n"
    "v_max_f32 v205, v205, v207\n"
    "v_mov_b32 v206, v204\n"
    "v_mov_b32 v207, v205\n"
    "s_nop 0\n"
    "v_permlane16_swap_b32 v206, v204\n"
    "v_permlane16_swap_b32 v207, v205\n"
    "v_max_f32 v204, v204, v206\n"
    "v_max_f32 v205, v205, v207\n"
    "v_add_f32 v204, v196, v204\n"
    "v_max_f32 v204, v204, v196\n"
    "v_sub_f32 v206, v196, v204\n"
    "v_exp_f32 v208, v206\n"
    "v_mov_b32 v196, v204\n"
    "v_add_f32 v205, v197, v205\n"
    "v_max_f32 v205, v205, v197\n"
    "v_sub_f32 v207, v197, v205\n"
    "v_exp_f32 v209, v207\n"
    "v_mov_b32 v197, v205\n"
    "v_mul_f32 v198, v198, v208\n"
    "v_mul_f32 v199, v199, v209\n"
    "v_mul_f32 v0, v0, v208\n"
    "v_mul_f32 v1, v1, v208\n"
    "v_mul_f32 v2, v2, v208\n"
    "v_mul_f32 v3, v3, v208\n"
    "v_mul_f32 v4, v4, v209\n"
    "v_mul_f32 v5, v5, v209\n"
    "v_mul_f32 v6, v6, v209\n"
    "v_mul_f32 v7, v7, v209\n"
    "v_mul_f32 v8, v8, v208\n"
    "v_mul_f32 v9, v9, v208\n"
    "v_mul_f32 v10, v10, v208\n"
    "v_mul_f32 v11, v11, v208\n"
    "v_mul_f32 v12, v12, v209\n"
    "v_mul_f32 v13, v13, v209\n"
    "v_mul_f32 v14, v14, v209\n"
    "v_mul_f32 v15, v15, v209\n"
    "v_mul_f32 v16, v16, v208\n"
    "v_mul_f32 v17, v17, v208\n"
    "v_mul_f32 v18, v18, v208\n"
    "v_mul_f32 v19, v19, v208\n"
    "v_mul_f32 v20, v20, v209\n"
    "v_mul_f32 v21, v21, v209\n"
    "v_mul_f32 v22, v22, v209\n"
    "v_mul_f32 v23, v23, v209\n"
    "v_mul_f32 v24, v24, v208\n"
    "v_mul_f32 v25, v25, v208\n"
    "v_mul_f32 v26, v26, v208\n"
    "v_mul_f32 v27, v27, v208\n"
    "v_mul_f32 v28, v28, v209\n"
    "v_mul_f32 v29, v29, v209\n"
    "v_mul_f32 v30, v30, v209\n"
    "v_mul_f32 v31, v31, v209\n"
    "v_mul_f32 v32, v32, v208\n"
    "v_mul_f32 v33, v33, v208\n"
    "v_mul_f32 v34, v34, v208\n"
    "v_mul_f32 v35, v35, v208\n"
    "v_mul_f32 v36, v36, v209\n"
    "v_mul_f32 v37, v37, v209\n"
    "v_mul_f32 v38, v38, v209\n"
    "v_mul_f32 v39, v39, v209\n"
    "v_mul_f32 v40, v40, v208\n"
    "v_mul_f32 v41, v41, v208\n"
    "v_mul_f32 v42, v42, v208\n"
    "v_mul_f32 v43, v43, v208\n"
    "v_mul_f32 v44, v44, v209\n"
    "v_mul_f32 v45, v45, v209\n"
    "v_mul_f32 v46, v46, v209\n"
    "v_mul_f32 v47, v47, v209\n"
    "v_mul_f32 v48, v48, v208\n"
    "v_mul_f32 v49, v49, v208\n"
    "v_mul_f32 v50, v50, v208\n"
    "v_mul_f32 v51, v51, v208\n"
    "v_mul_f32 v52, v52, v209\n"
    "v_mul_f32 v53, v53, v209\n"
    "v_mul_f32 v54, v54, v209\n"
    "v_mul_f32 v55, v55, v209\n"
    "v_mul_f32 v56, v56, v208\n"
    "v_mul_f32 v57, v57, v208\n"
    "v_mul_f32 v58, v58, v208\n"
    "v_mul_f32 v59, v59, v208\n"
    "v_mul_f32 v60, v60, v209\n"
    "v_mul_f32 v61, v61, v209\n"
    "v_mul_f32 v62, v62, v209\n"
    "v_mul_f32 v63, v63, v209\n"
    "v_mul_f32 v64, v64, v208\n"
    "v_mul_f32 v65, v65, v208\n"
    "v_mul_f32 v66, v66, v208\n"
    "v_mul_f32 v67, v67, v208\n"
    "v_mul_f32 v68, v68, v209\n"
    "v_mul_f32 v69, v69, v209\n"
    "v_mul_f32 v70, v70, v209\n"
    "v_mul_f32 v71, v71, v209\n"
    "v_mul_f32 v72, v72, v208\n"
    "v_mul_f32 v73, v73, v208\n"
    "v_mul_f32 v74, v74, v208\n"
    "v_mul_f32 v75, v75, v208\n"
    "v_mul_f32 v76, v76, v209\n"
    "v_mul_f32 v77, v77, v209\n"
    "v_mul_f32 v78, v78, v209\n"
    "v_mul_f32 v79, v79, v209\n"
    "v_mul_f32 v80, v80, v208\n"
    "v_mul_f32 v81, v81, v208\n"
    "v_mul_f32 v82, v82, v208\n"
    "v_mul_f32 v83, v83, v208\n"
    "v_mul_f32 v84, v84, v209\n"
    "v_mul_f32 v85, v85, v209\n"
    "v_mul_f32 v86, v86, v209\n"
    "v_mul_f32 v87, v87, v209\n"
    "v_mul_f32 v88, v88, v208\n"
    "v_mul_f32 v89, v89, v208\n"
    "v_mul_f32 v90, v90, v208\n"
    "v_mul_f32 v91, v91, v208\n"
    "v_mul_f32 v92, v92, v209\n"
    "v_mul_f32 v93, v93, v209\n"
    "v_mul_f32 v94, v94, v209\n"
    "v_mul_f32 v95, v95, v209\n"
    "v_mul_f32 v96, v96, v208\n"
    "v_mul_f32 v97, v97, v208\n"
    "v_mul_f32 v98, v98, v208\n"
    "v_mul_f32 v99, v99, v208\n"
    "v_mul_f32 v100, v100, v209\n"
    "v_mul_f32 v101, v101, v209\n"
    "v_mul_f32 v102, v102, v209\n"
    "v_mul_f32 v103, v103, v209\n"
    "v_mul_f32 v104, v104, v208\n"
    "v_mul_f32 v105, v105, v208\n"
    "v_mul_f32 v106, v106, v208\n"
    "v_mul_f32 v107, v107, v208\n"
    "v_mul_f32 v108, v108, v209\n"
    "v_mul_f32 v109, v109, v209\n"
    "v_mul_f32 v110, v110, v209\n"
    "v_mul_f32 v111, v111, v209\n"
    "v_mul_f32 v112, v112, v208\n"
    "v_mul_f32 v113, v113, v208\n"
    "v_mul_f32 v114, v114, v208\n"
    "v_mul_f32 v115, v115, v208\n"
    "v_mul_f32 v116, v116, v209\n"
    "v_mul_f32 v117, v117, v209\n"
    "v_mul_f32 v118, v118, v209\n"
    "v_mul_f32 v119, v119, v209\n"
    "v_mul_f32 v120, v120, v208\n"
    "v_mul_f32 v121, v121, v208\n"
    "v_mul_f32 v122, v122, v208\n"
    "v_mul_f32 v123, v123, v208\n"
    "v_mul_f32 v124, v124, v209\n"
    "v_mul_f32 v125, v125, v209\n"
    "v_mul_f32 v126, v126, v209\n"
    "v_mul_f32 v127, v127, v209\n"
    "v_add_f32 v144, v144, v206\n"
    "v_add_f32 v145, v145, v206\n"
    "v_add_f32 v146, v146, v206\n"
    "v_add_f32 v147, v147, v206\n"
    "v_add_f32 v148, v148, v207\n"
    "v_add_f32 v149, v149, v207\n"
    "v_add_f32 v150, v150, v207\n"
    "v_add_f32 v151, v151, v207\n"
    "v_add_f32 v152, v152, v206\n"
    "v_add_f32 v153, v153, v206\n"
    "v_add_f32 v154, v154, v206\n"
    "v_add_f32 v155, v155, v206\n"
    "v_add_f32 v156, v156, v207\n"
    "v_add_f32 v157, v157, v207\n"
    "v_add_f32 v158, v158, v207\n"
    "v_add_f32 v159, v159, v207\n"
    "v_exp_f32 v240, v144\n"
    "s_nop 0\n"
    "v_exp_f32 v241, v145\n"
    "s_nop 0\n"
    "v_exp_f32 v242, v146\n"
    "s_nop 0\n"
    "v_add_f32 v216, v240, v241\n"
    "v_cvt_pk_bf16_f32 v168, v240, v241\n"
    "v_exp_f32 v243, v147\n"
    "s_nop 0\n"
    "v_add_f32 v216, v216, v242\n"
    "v_exp_f32 v240, v148\n"
    "s_nop 0\n"
    "v_add_f32 v216, v216, v243\n"
    "v_cvt_pk_bf16_f32 v169, v242, v243\n"
    "v_exp_f32 v241, v149\n"
    "s_nop 0\n"
    "v_exp_f32 v242, v150\n"
    "s_nop 0\n"
    "v_add_f32 v217, v240, v241\n"
    "v_cvt_pk_bf16_f32 v172, v240, v241\n"
    "v_exp_f32 v243, v151\n"
    "s_nop 0\n"
    "v_add_f32 v217, v217, v242\n"
    "v_exp_f32 v240, v152\n"
    "s_nop 0\n"
    "v_add_f32 v217, v217, v243\n"
    "v_cvt_pk_bf16_f32 v173, v242, v243\n"
    "v_exp_f32 v241, v153\n"
    "s_nop 0\n"
    "v_add_f32 v216, v216, v240\n"
    "v_exp_f32 v242, v154\n"
    "s_nop 0\n"
    "v_add_f32 v216, v216, v241\n"
    "v_cvt_pk_bf16_f32 v170, v240, v241\n"
    "v_exp_f32 v243, v155\n"
    "s_nop 0\n"
    "v_add_f32 v216, v216, v242\n"
    "v_exp_f32 v240, v156\n"
    "s_nop 0\n"
    "v_add_f32 v216, v216, v243\n"
    "v_cvt_pk_bf16_f32 v171, v242, v243\n"
    "v_exp_f32 v241, v157\n"
    "s_nop 0\n"
    "v_add_f32 v217, v217, v240\n"
    "v_exp_f32 v242, v158\n"
    "s_nop 0\n"
    "v_add_f32 v217, v217, v241\n"
    "v_exp_f32 v243, v159\n"
    "s_nop 0\n"
    "v_add_f32 v217, v217, v242\n"
    "v_cvt_pk_bf16_f32 v174, v240, v241\n"
    "v_add_f32 v217, v217, v243\n"
    "v_add_f32 v215, v216, v217\n"
    "v_cmp_lt_f32 vcc, s29, v215\n"
    "v_cvt_pk_bf16_f32 v175, v242, v243\n"
    "s_add_u32 s30, s30, 4\n"
    "s_addc_u32 s31, s31, 0\n"
    "s_setpc_b64 s[30:31]\n"
    ".Lcw1:\n"
    :: "s"(__builtin_amdgcn_kernarg_segment_ptr()), "s"(blockIdx.x), "v"(threadIdx.x)
    : "v1", "v2", "v3", "v4", "v5", "v6", "v7", "v8", "v9", "v10", "v11", "v12", "v13", "v14", "v15", "v16", "v17", "v18", "v19", "v20", "v21", "v22", "v23", "v24", "v25", "v26", "v27", "v28", "v29", "v30", "v31", "v32", "v33", "v34", "v35", "v36", "v37", "v38", "v39", "v40", "v41", "v42", "v43", "v44", "v45", "v46", "v47", "v48", "v49", "v50", "v51", "v52", "v53", "v54", "v55", "v56", "v57", "v58", "v59", "v60", "v61", "v62", "v63", "v64", "v65", "v66", "v67", "v68", "v69", "v70", "v71", "v72", "v73", "v74", "v75", "v76", "v77", "v78", "v79", "v80", "v81", "v82", "v83", "v84", "v85", "v86", "v87", "v88", "v89", "v90", "v91", "v92", "v93", "v94", "v95", "v96", "v97", "v98", "v99", "v100", "v101", "v102", "v103", "v104", "v105", "v106", "v107", "v108", "v109", "v110", "v111", "v112", "v113", "v114", "v115", "v116", "v117", "v118", "v119", "v120", "v121", "v122", "v123", "v124", "v125", "v126", "v127", "v128", "v129", "v130", "v131", "v132", "v133", "v134", "v135", "v136", "v137", "v138", "v139", "v140", "v141", "v142", "v143", "v144", "v145", "v146", "v147", "v148", "v149", "v150", "v151", "v152", "v153", "v154", "v155", "v156", "v157", "v158", "v159", "v160", "v161", "v162", "v163", "v164", "v165", "v166", "v167", "v168", "v169", "v170", "v171", "v172", "v173", "v174", "v175", "v176", "v177", "v178", "v179", "v180", "v181", "v182", "v183", "v184", "v185", "v186", "v187", "v188", "v189", "v190", "v191", "v192", "v193", "v194", "v195", "v196", "v197", "v198", "v199", "v200", "v201", "v202", "v203", "v204", "v205", "v206", "v207", "v208", "v209", "v210", "v211", "v212", "v213", "v214", "v215", "v216", "v217", "v218", "v219", "v220", "v221", "v222", "v223", "v224", "v225", "v226", "v227", "v228", "v229", "v230", "v231", "v232", "v233", "v234", "v235", "v236", "v237", "v238", "v239", "v240", "v241", "v242", "v243", "v244", "v245", "v246", "v247", "v248", "v249", "v250", "v251", "v252", "v253", "v254", "v255", "a0", "a1", "a2", "a3", "a4", "a5", "a6", "a7", "a8", "a9", "a10", "a11", "a12", "a13", "a14", "a15", "a16", "a17", "a18", "a19", "a20", "a21", "a22", "a23", "a24", "a25", "a26", "a27", "a28", "a29", "a30", "a31", "a32", "a33", "a34", "a35", "a36", "a37", "a38", "a39", "a40", "a41", "a42", "a43", "a44", "a45", "a46", "a47", "a48", "a49", "a50", "a51", "a52", "a53", "a54", "a55", "a56", "a57", "a58", "a59", "a60", "a61", "a62", "a63", "a64", "a65", "a66", "a67", "a68", "a69", "a70", "a71", "a72", "a73", "a74", "a75", "a76", "a77", "a78", "a79", "a80", "a81", "a82", "a83", "a84", "a85", "a86", "a87", "a88", "a89", "a90", "a91", "a92", "a93", "a94", "a95", "a96", "a97", "a98", "a99", "a100", "a101", "a102", "a103", "a104", "a105", "a106", "a107", "a108", "a109", "a110", "a111", "a112", "a113", "a114", "a115", "a116", "a117", "a118", "a119", "a120", "a121", "a122", "a123", "a124", "a125", "a126", "a127", "a128", "a129", "a130", "a131", "a132", "a133", "a134", "a135", "a136", "a137", "a138", "a139", "a140", "a141", "a142", "a143", "a144", "a145", "a146", "a147", "a148", "a149", "a150", "a151", "a152", "a153", "a154", "a155", "a156", "a157", "a158", "a159", "a160", "a161", "a162", "a163", "a164", "a165", "a166", "a167", "a168", "a169", "a170", "a171", "a172", "a173", "a174", "a175", "a176", "a177", "a178", "a179", "a180", "a181", "a182", "a183", "a184", "a185", "a186", "a187", "a188", "a189", "a190", "a191", "a192", "a193", "a194", "a195", "a196", "a197", "a198", "a199", "a200", "a201", "a202", "a203", "a204", "a205", "a206", "a207", "a208", "a209", "a210", "a211", "a212", "a213", "a214", "a215", "a216", "a217", "a218", "a219", "a220", "a221", "a222", "a223", "a224", "a225", "a226", "a227", "a228", "a229", "a230", "a231", "a232", "a233", "a234", "a235", "a236", "a237", "a238", "a239", "a240", "a241", "a242", "a243", "a244", "a245", "a246", "a247", "a248", "a249", "a250", "a251", "a252", "a253", "a254", "a255", "s36", "s37", "s38", "s39", "s40", "s41", "s42", "s43", "s44", "s45", "s46", "s47", "s48", "s49", "s50", "s51", "s52", "s53", "s54", "s55", "s56", "s57", "s58", "s59", "s60", "s61", "s62", "s63", "s64", "s65", "s66", "s67", "s68", "s69", "s70", "s71", "s72", "s73", "s74", "s75", "s76", "s77", "s78", "s79", "s80", "s81", "s82", "s83", "s84", "s85", "s86", "s87", "s88", "s89", "s90", "s91", "s92", "s93", "s94", "s95", "s96", "s97", "s98", "s99", "vcc", "scc", "memory");
    asm volatile("" :: "v"(lds) : "memory");
}

extern "C" void kernel_launch(void* const* d_in, const int* in_sizes, int n_in, void* d_out, int out_size, void* d_ws,
                              size_t ws_size, hipStream_t stream) {
    attn_causal_d256<<<256, 256, 0, stream>>>((const char*)d_in[0], (const char*)d_in[1], (const char*)d_in[2], (char*)d_out, (char*)d_ws);
}
```
